```python
import torch

def _softmax(x, axis=-1):
    return torch.softmax(x, dim=axis)
def _gelu(x):
    return torch.nn.functional.gelu(x, approximate="tanh")
def _silu(x):
    return torch.nn.functional.silu(x)
def _sigmoid(x):
    return torch.sigmoid(x)
def _layernorm(x, axis=-1, eps=1e-5):
    m = x.mean(dim=axis, keepdim=True)
    v = x.var(dim=axis, keepdim=True, unbiased=False)
    return (x - m) / torch.sqrt(v + eps)
def _scatter_add(tgt, idx, src, axis=0):
    return tgt.clone().index_add_(axis, idx, src)
def _segment_sum(src, seg, n):
    out = torch.zeros((n,) + src.shape[1:], dtype=src.dtype)
    return out.index_add_(0, seg, src)
def _pad(x, pw):
    flat = tuple(v for pair in reversed(pw) for v in pair)
    return torch.nn.functional.pad(x, flat)

kernel_name = 'concat_heads__compressed_kv_cla__linear_kernel_hedgehog__prompt'

def setup_inputs():

    _w = (lambda a: (lambda t: t.to(torch.bfloat16) if t.is_floating_point() else t)(torch.as_tensor(a)))
    def _A_setup():
        B, H, Sq, Sk, D, M = 1, 32, 2048, 2048, 128, 576
        Dc = 4 * D
        return {
            "x": _w((lambda *s: torch.randn(*s, dtype=torch.bfloat16) * 0.8)(B, Sq, M)),
            "c_kv": _w((lambda *s: torch.randn(*s, dtype=torch.bfloat16) * 0.4)(B, Sk, Dc)),
            "w_q": _w((lambda *s: torch.randn(*s, dtype=torch.bfloat16) * 6.4)(M, H, D) * (M ** -0.5)),
            "w_uk": _w((lambda *s: torch.randn(*s, dtype=torch.bfloat16) * 6.4)(Dc, H, D) * (Dc ** -0.5)),
            "w_uv": _w((lambda *s: torch.randn(*s, dtype=torch.bfloat16) * 0.4)(Dc, H, D) * (Dc ** -0.5)),
            "w_o": _w((lambda *s: torch.randn(*s, dtype=torch.bfloat16) * 0.4)(H, D, M) * ((H * D) ** -0.5)),
        }
    _A_in = _A_setup()

    def _B_setup():
        B, S, D, H = 1, 2048, 576, 32
        Dh = D // H
        return {
            "x": _w((lambda *s: torch.randn(*s, dtype=torch.bfloat16) * 0.4)(B, S, D)),
            "wqkv": _w((lambda *s: torch.randn(*s, dtype=torch.bfloat16) * 0.4)(D, 3 * D)),
            "w_phi": _w((lambda *s: torch.randn(*s, dtype=torch.bfloat16) * 0.4)(H, Dh, Dh) * (Dh ** -0.5)),
            "wo": _w((lambda *s: torch.randn(*s, dtype=torch.bfloat16) * 0.05)(D, D)),
        }
    _B_in = _B_setup()

    return {
        "A_x": _A_in["x"],
        "A_c_kv": _A_in["c_kv"],
        "A_w_q": _A_in["w_q"],
        "A_w_uk": _A_in["w_uk"],
        "A_w_uv": _A_in["w_uv"],
        "A_w_o": _A_in["w_o"],
        "B_x": _B_in["x"],
        "B_wqkv": _B_in["wqkv"],
        "B_w_phi": _B_in["w_phi"],
        "B_wo": _B_in["wo"],
    }

def reference(A_x, A_c_kv, A_w_q, A_w_uk, A_w_uv, A_w_o, B_x, B_wqkv, B_w_phi, B_wo):
    def _A_ref(x, c_kv, w_q, w_uk, w_uv, w_o):
        B, Sq, M = x.shape[0], x.shape[1], x.shape[2]
        Sk = c_kv.shape[1]
        H, D = w_q.shape[1], w_q.shape[2]
        q = torch.einsum("bqm,mhd->bhqd", x, w_q)
        k = torch.einsum("bkc,chd->bhkd", c_kv, w_uk)
        v = torch.einsum("bkc,chd->bhkd", c_kv, w_uv)
        scores = torch.einsum("bhqd,bhkd->bhqk", q, k) * (D ** -0.5)
        off = Sk - Sq
        qi = torch.arange(Sq)[:, None]
        kj = torch.arange(Sk)[None, :]
        cmask = kj <= (qi + off)
        scores = torch.where(cmask[None, None], scores, -1e9)
        p = _softmax(scores, axis=-1)
        ctx = torch.einsum("bhqk,bhkd->bhqd", p, v)
        return torch.einsum("bhqd,hdm->bqm", ctx, w_o)

    def _B_ref(x, wqkv, w_phi, wo):
        B, S, D = x.shape
        H = 32; Dh = D // H
        qkv = (lambda x, *d: x.permute(*d))((x @ wqkv).reshape(B, S, 3, H, Dh), 2, 0, 3, 1, 4)
        q, k, v = qkv[0], qkv[1], qkv[2]
        pq = torch.einsum("bhsd,hdf->bhsf", q, w_phi)
        pk = torch.einsum("bhsd,hdf->bhsf", k, w_phi)
        phi_q = torch.concatenate([_softmax(pq, axis=-1), _softmax(-pq, axis=-1)], axis=-1)
        phi_k = torch.concatenate([_softmax(pk, axis=-1), _softmax(-pk, axis=-1)], axis=-1)
        kv = torch.einsum("bhsf,bhsd->bhfd", phi_k, v)
        z  = torch.einsum("bhsf->bhf", phi_k)
        num = torch.einsum("bhqf,bhfd->bhqd", phi_q, kv)
        den = torch.einsum("bhqf,bhf->bhq", phi_q, z)[..., None] + 1e-6
        o = num / den
        return (lambda x, *d: x.permute(*d))(o, 0, 2, 1, 3).reshape(B, S, D) @ wo

    a = _A_ref(A_x, A_c_kv, A_w_q, A_w_uk, A_w_uv, A_w_o)
    b = _B_ref(B_x, B_wqkv, B_w_phi, B_wo)
    return torch.concatenate([a, b], axis=-1)
```

```cpp
#include <hip/hip_runtime.h>
#include <hip/hip_bf16.h>
#include <cstdio>
typedef unsigned short u16;
__device__ __forceinline__ float bf2f(u16 b) { return __uint_as_float(((unsigned)b) << 16); }
__device__ __forceinline__ u16 f2bf(float f) { unsigned u = __float_as_uint(f); u += 0x7FFFu + ((u >> 16) & 1u); return (u16)(u >> 16); }
__device__ __forceinline__ float rbf(float f) { return bf2f(f2bf(f)); }

__global__ __launch_bounds__(256) void gemm_naive(const u16* A, const u16* B, u16* C, int M, int N, int K, int lda, int ldb, int ldc) {
  __shared__ float As[16][65], Bs[16][65];
  int tx = threadIdx.x & 15, ty = threadIdx.x >> 4;
  int m0 = blockIdx.y * 64, n0 = blockIdx.x * 64;
  float acc[4][4] = {};
  for (int k0 = 0; k0 < K; k0 += 16) {
    for (int i = threadIdx.x; i < 1024; i += 256) { int r = i >> 4, c = i & 15; As[c][r] = bf2f(A[(size_t)(m0 + r) * lda + k0 + c]); }
    for (int i = threadIdx.x; i < 1024; i += 256) { int r = i >> 6, c = i & 63; Bs[r][c] = (n0 + c < N) ? bf2f(B[(size_t)(k0 + r) * ldb + n0 + c]) : 0.f; }
    __syncthreads();
    for (int k = 0; k < 16; ++k) {
      float a[4], b[4];
      for (int i = 0; i < 4; ++i) { a[i] = As[k][ty * 4 + i]; b[i] = Bs[k][tx * 4 + i]; }
      for (int i = 0; i < 4; ++i) for (int j = 0; j < 4; ++j) acc[i][j] += a[i] * b[j];
    }
    __syncthreads();
  }
  for (int i = 0; i < 4; ++i) for (int j = 0; j < 4; ++j) if (n0 + tx * 4 + j < N) C[(size_t)(m0 + ty * 4 + i) * ldc + n0 + tx * 4 + j] = f2bf(acc[i][j]);
}

#define MIMIC 1
__global__ __launch_bounds__(256) void attn_naive(const u16* Q, const u16* Kk, const u16* V, u16* ctx) {
  __shared__ float s[2048]; __shared__ float qs[128]; __shared__ float red[256];
  int q = blockIdx.x, h = blockIdx.y, t = threadIdx.x;
  if (t < 128) qs[t] = bf2f(Q[(size_t)q * 4096 + h * 128 + t]);
  __syncthreads();
  float mx = -3e38f;
  for (int k = t; k <= q; k += 256) {
    const uint4* kp = (const uint4*)(Kk + (size_t)k * 4096 + h * 128);
    float a = 0;
    for (int d8 = 0; d8 < 16; ++d8) { uint4 w = kp[d8]; unsigned ww[4] = {w.x, w.y, w.z, w.w};
      for (int j = 0; j < 4; ++j) { a += qs[d8 * 8 + 2 * j] * __uint_as_float(ww[j] << 16); a += qs[d8 * 8 + 2 * j + 1] * __uint_as_float(ww[j] & 0xffff0000u); } }
    if (MIMIC) a = rbf(rbf(a) * 0.08838834764831845f); else a *= 0.08838834764831845f;
    s[k] = a; mx = fmaxf(mx, a);
  }
  red[t] = mx; __syncthreads();
  for (int o = 128; o > 0; o >>= 1) { if (t < o) red[t] = fmaxf(red[t], red[t + o]); __syncthreads(); }
  mx = red[0]; __syncthreads();
  float sum = 0;
  for (int k = t; k <= q; k += 256) { float e = __expf(s[k] - mx); s[k] = e; sum += e; }
  red[t] = sum; __syncthreads();
  for (int o = 128; o > 0; o >>= 1) { if (t < o) red[t] += red[t + o]; __syncthreads(); }
  sum = red[0]; __syncthreads();
  float inv = 1.f / sum;
  int d = t & 127, half = t >> 7;
  float acc = 0;
  for (int k = half; k <= q; k += 2) acc += rbf(s[k] * inv) * bf2f(V[(size_t)k * 4096 + h * 128 + d]);
  red[t] = acc; __syncthreads();
  if (t < 128) ctx[(size_t)q * 4096 + h * 128 + t] = f2bf(red[t] + red[t + 128]);
}

__global__ void phi_naive(const u16* qkv, const u16* wphi, float* phi) {
  int idx = blockIdx.x * blockDim.x + threadIdx.x;
  if (idx >= 2048 * 64) return;
  int s = idx >> 6, which = (idx >> 5) & 1, h = idx & 31;
  float x[18], p[18];
  for (int d = 0; d < 18; ++d) x[d] = bf2f(qkv[(size_t)s * 1728 + which * 576 + h * 18 + d]);
  float mx = -3e38f, mn = 3e38f;
  for (int f = 0; f < 18; ++f) { float a = 0; for (int d = 0; d < 18; ++d) a += x[d] * bf2f(wphi[h * 324 + d * 18 + f]); a = rbf(a); p[f] = a; mx = fmaxf(mx, a); mn = fminf(mn, a); }
  float s1 = 0, s2 = 0;
  for (int f = 0; f < 18; ++f) { s1 += __expf(p[f] - mx); s2 += __expf(mn - p[f]); }
  float* o = phi + ((size_t)which * 2048 * 32 + (size_t)s * 32 + h) * 36;
  for (int f = 0; f < 18; ++f) { o[f] = rbf(__expf(p[f] - mx) / s1); o[18 + f] = rbf(__expf(mn - p[f]) / s2); }
}
__global__ void kv_naive(const u16* qkv, const float* phi, float* kv) {
  int h = blockIdx.x, t = threadIdx.x; if (t >= 36 * 19) return;
  int f = t / 19, d = t % 19; float a = 0;
  for (int s = 0; s < 2048; ++s) { float pk = phi[((size_t)2048 * 32 + (size_t)s * 32 + h) * 36 + f]; float v = d < 18 ? bf2f(qkv[(size_t)s * 1728 + 1152 + h * 18 + d]) : 1.f; a += pk * v; }
  kv[h * 684 + t] = rbf(a);
}
__global__ void o_naive(const float* phi, const float* kv, u16* o) {
  int idx = blockIdx.x * blockDim.x + threadIdx.x; if (idx >= 2048 * 576) return;
  int s = idx / 576, c = idx % 576, h = c / 18, d = c % 18;
  float num = 0, den = 0;
  for (int f = 0; f < 36; ++f) { float pq = phi[((size_t)s * 32 + h) * 36 + f]; num += pq * kv[h * 684 + f * 19 + d]; den += pq * kv[h * 684 + f * 19 + 18]; }
  den = rbf(rbf(den) + 1e-6f);
  o[idx] = f2bf(rbf(num) / den);
}

extern "C" void kernel_launch(void* const* d_in, const int* in_sizes, int n_in, void* d_out, int out_size, void* d_ws, size_t ws_size, hipStream_t stream) {
  const u16* Ax = (const u16*)d_in[0]; const u16* ckv = (const u16*)d_in[1]; const u16* wq = (const u16*)d_in[2]; const u16* wuk = (const u16*)d_in[3];
  const u16* wuv = (const u16*)d_in[4]; const u16* wo = (const u16*)d_in[5]; const u16* Bx = (const u16*)d_in[6]; const u16* wqkv = (const u16*)d_in[7];
  const u16* wphi = (const u16*)d_in[8]; const u16* Bwo = (const u16*)d_in[9];
  u16* out = (u16*)d_out; char* ws = (char*)d_ws;
  const size_t T = (size_t)2048 * 4096 * 2;
  u16* Q = (u16*)ws; u16* K = (u16*)(ws + T); u16* V = (u16*)(ws + 2 * T); u16* ctx = (u16*)(ws + 3 * T);
  u16* qkv = (u16*)(ws + 4 * T); float* phi = (float*)(ws + 5 * T); float* kv = (float*)(ws + 7 * T); u16* ob = (u16*)(ws + 8 * T);
  gemm_naive<<<dim3(64, 32), 256, 0, stream>>>(Ax, wq, Q, 2048, 4096, 576, 576, 4096, 4096);
  gemm_naive<<<dim3(64, 32), 256, 0, stream>>>(ckv, wuk, K, 2048, 4096, 512, 512, 4096, 4096);
  gemm_naive<<<dim3(64, 32), 256, 0, stream>>>(ckv, wuv, V, 2048, 4096, 512, 512, 4096, 4096);
  attn_naive<<<dim3(2048, 32), 256, 0, stream>>>(Q, K, V, ctx);
  gemm_naive<<<dim3(9, 32), 256, 0, stream>>>(ctx, wo, out, 2048, 576, 4096, 4096, 576, 1152);
  gemm_naive<<<dim3(27, 32), 256, 0, stream>>>(Bx, wqkv, qkv, 2048, 1728, 576, 576, 1728, 1728);
  phi_naive<<<512, 256, 0, stream>>>(qkv, wphi, phi);
  kv_naive<<<32, 704, 0, stream>>>(qkv, phi, kv);
  o_naive<<<(2048 * 576 + 255) / 256, 256, 0, stream>>>(phi, kv, ob);
  gemm_naive<<<dim3(9, 32), 256, 0, stream>>>(ob, Bwo, out + 576, 2048, 576, 576, 576, 576, 1152);
}
```

```cpp
#include <hip/hip_runtime.h>
typedef unsigned short u16;

__global__ __launch_bounds__(256, 1) void fused_k(const u16* p0, const u16* p1, const u16* p2, const u16* p3, const u16* p4, const u16* p5, const u16* p6, const u16* p7, const u16* p8, const u16* p9, u16* out, char* ws) {
  unsigned wid = __builtin_amdgcn_readfirstlane(threadIdx.x >> 6);
  unsigned bid = blockIdx.x;
  asm volatile(
R"(.macro M a,b,c,d
v_mfma_f32_16x16x32_bf16 \a,\b,\c,\d
.endm
.macro C a,b,c
v_cvt_pk_bf16_f32 \a,\b,\c
.endm
.macro AR a,b
v_accvgpr_read_b32 \a,\b
.endm
.macro AW a,b
v_accvgpr_write_b32 \a,\b
.endm
.macro E a,b
v_exp_f32 \a,\b
.endm
.macro D d,a,o
ds_read_b128 \d,\a offset:\o
.endm
.macro T d,a,o
ds_read_b64_tr_b16 \d,\a offset:\o
.endm
.macro G v,s,o,i
buffer_load_dwordx4 \v,\s,\o offen offset:\i lds
.endm
.macro GS v,s,o,i
buffer_load_dwordx4 \v,\s,\o offen offset:\i sc1 lds
.endm
.macro WL n
s_waitcnt lgkmcnt(\n)
.endm
.macro WV n
s_waitcnt vmcnt(\n)
.endm
s_load_dwordx2 s[94:95],%0,88
s_mov_b32 s20,%1
s_load_dwordx2 s[36:37],%0,8
s_mov_b32 s38,2097152
s_mov_b32 s39,131072
s_load_dwordx2 s[40:41],%0,24
s_mov_b32 s42,4194304
s_mov_b32 s43,131072
v_mbcnt_lo_u32_b32 v0,-1,0
v_mbcnt_hi_u32_b32 v0,-1,v0
v_mov_b32 v40,v0
v_lshlrev_b32 v0,4,v40
v_lshrrev_b32 v41,3,v40
v_and_b32 v42,7,v40
v_lshrrev_b32 v43,1,v41
v_xor_b32 v43,v43,v42
v_lshlrev_b32 v43,4,v43
v_xor_b32 v44,64,v43
v_lshl_add_u32 v45,v41,10,v43
v_xor_b32 v46,64,v45
v_add_u32 v1,0,v45
v_add_u32 v2,7168,v46
v_add_u32 v3,14336,v45
v_add_u32 v4,21504,v46
v_and_b32 v46,2,v41
v_xor_b32 v46,v46,v42
v_lshlrev_b32 v46,4,v46
v_lshl_add_u32 v46,v41,13,v46
v_xor_b32 v47,64,v46
v_add_u32 v9,0,v46
v_add_u32 v10,64512,v47
v_add_u32 v11,129024,v46
v_add_u32 v12,193536,v47
s_and_b32 s35,%2,7
s_lshl_b32 s35,s35,2
s_lshr_b32 s21,%2,6
s_add_u32 s35,s35,s21
s_lshr_b32 s27,%2,3
s_and_b32 s27,s27,7
s_lshl_b32 s64,s27,2
s_add_u32 s64,s64,s20
s_lshl_b32 s64,s64,16
s_add_u32 s65,s64,32768
s_and_b32 s22,s20,1
s_lshl_b32 s66,s22,18
s_lshl_b32 s21,s35,8
s_add_u32 s66,s66,s21
s_lshr_b32 s23,s20,1
s_lshl_b32 s21,s23,7
s_add_u32 s66,s66,s21
s_mov_b32 s74,s66
s_lshl_b32 s67,s20,13
s_lshl_b32 s68,s20,12
WL 0
s_and_b32 s37,s37,65535
s_and_b32 s41,s41,65535
s_add_u32 m0,s67,98304
s_nop 0
G v1,s[36:39],s64,0
G v2,s[36:39],s64,1024
G v3,s[36:39],s64,2048
G v4,s[36:39],s64,3072
s_add_u32 s64,s64,128
s_add_u32 m0,s67,102400
s_nop 0
G v1,s[36:39],s65,0
G v2,s[36:39],s65,1024
G v3,s[36:39],s65,2048
G v4,s[36:39],s65,3072
s_add_u32 s65,s65,128
s_add_u32 m0,s68,131072
s_nop 0
G v9,s[40:43],s66,0
G v10,s[40:43],s66,1024
G v11,s[40:43],s66,2048
G v12,s[40:43],s66,3072
s_add_u32 s66,s66,524288
s_add_u32 s94,s94,83886080
s_addc_u32 s95,s95,0
v_mov_b32 v38,65536
global_load_dword v38,v38,s[94:95] sc1
s_lshl_b32 s14,%2,2
s_add_u32 s14,s14,s20
s_mul_i32 s15,s14,2048
s_mov_b32 m0,147456
s_nop 0
G v0,s[36:39],s15,0
G v0,s[36:39],s15,1024
s_mul_i32 s15,s14,4096
s_mov_b32 m0,147456
s_nop 0
G v0,s[40:43],s15,0
G v0,s[40:43],s15,1024
G v0,s[40:43],s15,2048
G v0,s[40:43],s15,3072
s_add_u32 m0,s67,0
s_nop 0
G v1,s[36:39],s64,0
G v2,s[36:39],s64,1024
G v3,s[36:39],s64,2048
G v4,s[36:39],s64,3072
s_add_u32 s64,s64,128
s_add_u32 m0,s67,4096
s_nop 0
G v1,s[36:39],s65,0
G v2,s[36:39],s65,1024
G v3,s[36:39],s65,2048
G v4,s[36:39],s65,3072
s_add_u32 s65,s65,128
s_add_u32 m0,s68,32768
s_nop 0
G v9,s[40:43],s66,0
G v10,s[40:43],s66,1024
G v11,s[40:43],s66,2048
G v12,s[40:43],s66,3072
s_add_u32 s66,s66,524288
s_add_u32 m0,s67,65536
s_nop 0
G v1,s[36:39],s64,0
G v2,s[36:39],s64,1024
G v3,s[36:39],s64,2048
G v4,s[36:39],s64,3072
s_add_u32 s64,s64,128
s_add_u32 m0,s67,69632
s_nop 0
G v1,s[36:39],s65,0
G v2,s[36:39],s65,1024
G v3,s[36:39],s65,2048
G v4,s[36:39],s65,3072
s_add_u32 s65,s65,128
s_add_u32 m0,s68,49152
s_nop 0
G v9,s[40:43],s66,0
G v10,s[40:43],s66,1024
G v11,s[40:43],s66,2048
G v12,s[40:43],s66,3072
s_add_u32 s66,s66,524288
s_getpc_b64 s[88:89]
f_pc:
s_and_b32 s89,s89,65535
s_mov_b32 s90,f_last-f_pc
s_mov_b32 s91,131072
v_lshlrev_b32 v25,3,v0
s_lshr_b32 s21,%2,3
s_lshl_b32 s21,s21,2
s_add_u32 s21,s21,s20
s_lshl_b32 s21,s21,13
buffer_load_dword v26,v25,s[88:91],s21 offen offset:0
s_load_dwordx2 s[60:61],%0,88
s_mov_b32 s62,33554432
s_mov_b32 s63,131072
s_load_dwordx2 s[52:53],%0,48
s_mov_b32 s54,2359296
s_mov_b32 s55,131072
s_load_dwordx2 s[56:57],%0,56
s_mov_b32 s58,1990656
s_mov_b32 s59,131072
s_load_dwordx2 s[88:89],%0,64
s_mov_b32 s90,20736
s_mov_b32 s91,131072
s_load_dwordx2 s[80:81],%0,88
s_mov_b32 s82,5242880
s_mov_b32 s83,131072
s_load_dwordx2 s[84:85],%0,32
s_mov_b32 s86,4194304
s_mov_b32 s87,131072
s_load_dwordx2 s[44:45],%0,0
s_mov_b32 s46,2359296
s_mov_b32 s47,131072
s_load_dwordx2 s[48:49],%0,16
s_mov_b32 s50,4718592
s_mov_b32 s51,131072
v_mul_u32_u24 v45,1152,v41
v_add_u32 v5,v45,v43
v_add_u32 v6,v45,v44
v_add_u32 v6,8192,v6
v_add_u32 v7,v45,v43
v_add_u32 v7,16384,v7
v_add_u32 v8,v45,v44
v_add_u32 v8,24576,v8
v_and_b32 v48,15,v40
v_lshrrev_b32 v49,4,v40
v_lshrrev_b32 v45,1,v48
v_xor_b32 v45,v45,v49
v_lshlrev_b32 v45,4,v45
v_lshl_add_u32 v45,v48,7,v45
v_xor_b32 v46,64,v45
v_add_u32 v17,s67,v45
v_add_u32 v18,s67,v46
v_add_u32 v19,s68,v45
v_add_u32 v20,s68,v46
v_bfe_u32 v46,v40,2,2
v_lshl_add_u32 v45,v49,3,v46
v_lshlrev_b32 v45,7,v45
v_and_b32 v47,3,v40
v_lshl_add_u32 v45,v47,3,v45
v_lshrrev_b32 v46,1,v46
v_lshl_add_u32 v45,v46,5,v45
v_and_b32 v46,1,v49
v_lshl_add_u32 v45,v46,6,v45
v_xor_b32 v21,0,v45
v_xor_b32 v22,32,v45
v_xor_b32 v23,64,v45
v_xor_b32 v24,96,v45
s_lshl_b32 s70,s35,19
s_lshl_b32 s21,s27,16
s_add_u32 s70,s70,s21
s_lshl_b32 s21,s20,14
s_add_u32 s70,s70,s21
s_mov_b32 s71,1040356334
s_lshr_b32 s21,%2,3
s_and_b32 s31,s21,7
s_and_b32 s24,%2,1
s_lshl_b32 s24,s24,3
s_add_u32 s31,s31,s24
s_lshr_b32 s34,%2,6
s_and_b32 s24,%2,6
s_lshl_b32 s24,s24,1
s_add_u32 s34,s34,s24
s_lshl_b32 s21,s34,2
s_add_u32 s21,s21,s20
s_mul_i32 s77,s21,36864
s_mul_i32 s78,s20,55296
s_mul_i32 s21,s31,72
s_add_u32 s78,s78,s21
s_sub_u32 s21,15,s27
s_lshl_b32 s21,s21,2
s_add_u32 s21,s21,s20
s_mul_i32 s72,s21,36864
s_lshl_b32 s21,s27,2
s_add_u32 s21,s21,s20
s_mul_i32 s73,s21,36864
v_and_b32 v42,1,v49
v_and_b32 v43,2,v49
v_lshl_add_u32 v42,v43,1,v42
v_xor_b32 v42,v42,v48
v_mul_u32_u24 v44,3456,v49
v_xor_b32 v45,0,v42
v_lshlrev_b32 v46,4,v45
v_mul_u32_u24 v47,52,v45
v_lshrrev_b32 v47,8,v47
v_mul_u32_u24 v47,1072,v47
v_add_u32 v46,v46,v47
v_cmp_eq_u32 vcc,15,v45
s_nop 1
v_cndmask_b32 v46,v46,0,vcc
v_add_u32 v46,v46,v44
v_add_u32 v13,0,v46
v_add_u32 v14,12800,v46
v_xor_b32 v45,8,v42
v_lshlrev_b32 v46,4,v45
v_mul_u32_u24 v47,52,v45
v_lshrrev_b32 v47,8,v47
v_mul_u32_u24 v47,1072,v47
v_add_u32 v46,v46,v47
v_cmp_eq_u32 vcc,15,v45
s_nop 1
v_cndmask_b32 v46,v46,0,vcc
v_add_u32 v46,v46,v44
v_add_u32 v15,25600,v46
v_add_u32 v16,38400,v46
v_bfe_u32 v42,v40,2,2
v_and_b32 v43,3,v40
v_lshlrev_b32 v43,3,v43
v_and_b32 v44,1,v42
v_and_b32 v46,2,v42
v_lshl_add_u32 v44,v46,1,v44
v_and_b32 v46,1,v49
v_lshl_add_u32 v44,v46,3,v44
v_lshl_add_u32 v45,v49,3,v42
v_lshlrev_b32 v45,8,v45
s_mov_b32 s21,251923200
v_bfe_u32 v46,s21,v43,8
v_lshrrev_b32 v47,1,v46
v_xor_b32 v47,v47,v44
v_lshl_add_u32 v47,v47,4,v45
v_and_b32 v46,1,v46
v_lshl_add_u32 v28,v46,3,v47
s_mov_b32 s21,268766209
v_bfe_u32 v46,s21,v43,8
v_lshrrev_b32 v47,1,v46
v_xor_b32 v47,v47,v44
v_lshl_add_u32 v47,v47,4,v45
v_and_b32 v46,1,v46
v_lshl_add_u32 v29,v46,3,v47
s_mov_b32 s21,285609218
v_bfe_u32 v46,s21,v43,8
v_lshrrev_b32 v47,1,v46
v_xor_b32 v47,v47,v44
v_lshl_add_u32 v47,v47,4,v45
v_and_b32 v46,1,v46
v_lshl_add_u32 v30,v46,3,v47
s_mov_b32 s21,302452227
v_bfe_u32 v46,s21,v43,8
v_lshrrev_b32 v47,1,v46
v_xor_b32 v47,v47,v44
v_lshl_add_u32 v47,v47,4,v45
v_and_b32 v46,1,v46
v_lshl_add_u32 v31,v46,3,v47
s_mov_b32 s21,235407876
v_bfe_u32 v46,s21,v43,8
v_lshrrev_b32 v47,1,v46
v_xor_b32 v47,v47,v44
v_lshl_add_u32 v47,v47,4,v45
v_and_b32 v46,1,v46
v_lshl_add_u32 v32,v46,3,v47
s_mov_b32 s21,421008660
v_bfe_u32 v46,s21,v43,8
v_lshrrev_b32 v47,1,v46
v_xor_b32 v47,v47,v44
v_lshl_add_u32 v47,v47,4,v45
v_and_b32 v46,1,v46
v_lshl_add_u32 v33,v46,3,v47
s_mov_b32 s21,454694678
v_bfe_u32 v46,s21,v43,8
v_lshrrev_b32 v47,1,v46
v_xor_b32 v47,v47,v44
v_lshl_add_u32 v47,v47,4,v45
v_and_b32 v46,1,v46
v_lshl_add_u32 v34,v46,3,v47
s_mov_b32 s21,488381724
v_bfe_u32 v46,s21,v43,8
v_lshrrev_b32 v47,1,v46
v_xor_b32 v47,v47,v44
v_lshl_add_u32 v47,v47,4,v45
v_and_b32 v46,1,v46
v_lshl_add_u32 v35,v46,3,v47
WL 0
s_and_b32 s61,s61,65535
s_and_b32 s53,s53,65535
s_and_b32 s57,s57,65535
s_and_b32 s89,s89,65535
s_and_b32 s45,s45,65535
s_and_b32 s49,s49,65535
s_and_b32 s85,s85,65535
s_add_u32 s80,s80,67108864
s_addc_u32 s81,s81,0
s_and_b32 s81,s81,65535
s_lshl_b32 s21,s20,8
v_lshrrev_b32 v40,2,v0
v_add_u32 v40,s21,v40
s_mul_i32 s22,s31,1296
buffer_load_dword v36,v40,s[88:91],s22 offen offset:0
buffer_load_dword v37,v40,s[88:91],s22 offen offset:1024
v_lshlrev_b32 v41,2,v40
v_add_u32 v41,155648,v41
v_mov_b32 v44,0
v_mov_b32 v45,0
v_mov_b32 v46,0
v_mov_b32 v47,0
ds_write_b128 v41,v[44:47] offset:0
v_add_u32 v17,98304,v17
v_add_u32 v18,98304,v18
v_add_u32 v21,131072,v21
v_add_u32 v22,131072,v22
v_add_u32 v23,131072,v23
v_add_u32 v24,131072,v24
v_add_u32 v28,16384,v28
v_add_u32 v29,16384,v29
v_add_u32 v30,16384,v30
v_add_u32 v31,16384,v31
v_add_u32 v32,16384,v32
v_add_u32 v33,16384,v33
v_add_u32 v34,16384,v34
v_add_u32 v35,16384,v35
s_waitcnt vmcnt(33) lgkmcnt(0)
s_barrier
)"
R"(D a[160:163],v17,0
T a[128:129],v21,0
T a[130:131],v21,512
D a[164:167],v17,2048
D a[168:171],v17,4096
D a[172:175],v17,6144
T a[132:133],v22,0
T a[134:135],v22,512
T a[136:137],v23,0
T a[138:139],v23,512
T a[140:141],v24,0
T a[142:143],v24,512
T a[144:145],v21,8192
T a[146:147],v21,8704
T a[148:149],v22,8192
T a[150:151],v22,8704
T a[152:153],v23,8192
T a[154:155],v23,8704
T a[156:157],v24,8192
T a[158:159],v24,8704
s_lshl_b32 s21,s20,6
v_lshrrev_b32 v40,4,v0
v_add_u32 v40,s21,v40
v_add_u32 v41,0,v40
v_cmp_le_u32 vcc,162,v41
s_nop 1
v_cndmask_b32 v42,0,1,vcc
v_mul_u32_u24 v43,162,v42
v_sub_u32 v41,v41,v43
v_mul_u32_u24 v43,57,v41
v_lshrrev_b32 v43,9,v43
v_lshlrev_b32 v44,2,v41
v_lshl_add_u32 v44,v43,2,v44
v_mul_u32_u24 v42,1040,v42
v_add_u32 v44,v44,v42
v_add_u32 v44,156752,v44
WV 1
ds_write_b32 v44,v36 offset:0
v_add_u32 v41,256,v40
v_cmp_le_u32 vcc,162,v41
s_nop 1
v_cndmask_b32 v42,0,1,vcc
v_mul_u32_u24 v43,162,v42
v_sub_u32 v41,v41,v43
v_mul_u32_u24 v43,57,v41
v_lshrrev_b32 v43,9,v43
v_lshlrev_b32 v44,2,v41
v_lshl_add_u32 v44,v43,2,v44
v_mul_u32_u24 v42,1040,v42
v_add_u32 v44,v44,v42
v_add_u32 v44,156752,v44
v_cmp_gt_u32 vcc,162,v41
v_mov_b32 v45,159744
s_nop 0
v_cndmask_b32 v44,v45,v44,vcc
WV 0
ds_write_b32 v44,v37 offset:0
WL 15
M v[128:131],a[128:131],a[160:163],0
D a[176:179],v18,0
M v[160:163],a[128:131],a[164:167],0
D a[180:183],v18,2048
M v[192:195],a[128:131],a[168:171],0
D a[184:187],v18,4096
M v[224:227],a[128:131],a[172:175],0
WL 15
D a[188:191],v18,6144
M v[132:135],a[132:135],a[160:163],0
T a[128:129],v21,4096
M v[164:167],a[132:135],a[164:167],0
T a[130:131],v21,4608
M v[196:199],a[132:135],a[168:171],0
M v[228:231],a[132:135],a[172:175],0
T a[132:133],v22,4096
M v[136:139],a[136:139],a[160:163],0
T a[134:135],v22,4608
M v[168:171],a[136:139],a[164:167],0
WL 6
M v[200:203],a[136:139],a[168:171],0
v_lshrrev_b32 v40,4,v0
M v[232:235],a[136:139],a[172:175],0
T a[136:137],v23,4096
v_lshrrev_b32 v41,4,v40
M v[140:143],a[140:143],a[160:163],0
T a[138:139],v23,4608
v_bfe_u32 v42,v40,2,2
M v[172:175],a[140:143],a[164:167],0
v_and_b32 v43,3,v40
M v[204:207],a[140:143],a[168:171],0
v_and_b32 v44,15,v40
M v[236:239],a[140:143],a[172:175],0
s_mul_i32 s21,s20,3072
T a[140:141],v24,4096
M v[144:147],a[144:147],a[160:163],0
s_add_u32 s21,s21,118784
T a[142:143],v24,4608
v_mul_u32_u24 v56,96,v44
M v[176:179],a[144:147],a[164:167],0
v_add_u32 v56,s21,v56
M v[208:211],a[144:147],a[168:171],0
v_lshl_add_u32 v57,v41,3,v56
M v[240:243],a[144:147],a[172:175],0
T a[144:145],v21,12288
v_and_b32 v45,2,v41
M v[148:151],a[148:151],a[160:163],0
T a[146:147],v21,12800
v_lshl_add_u32 v56,v45,3,v57
M v[180:183],a[148:151],a[164:167],0
v_cmp_eq_u32 s[88:89],1,v41
M v[212:215],a[148:151],a[168:171],0
v_lshrrev_b32 v46,1,v41
M v[244:247],a[148:151],a[172:175],0
s_mov_b32 s22,163840
T a[148:149],v22,12288
v_mul_lo_u32 v58,s22,v46
M v[152:155],a[152:155],a[160:163],0
T a[150:151],v22,12800
v_add_u32 v17,4294868992,v17
M v[184:187],a[152:155],a[164:167],0
v_add_u32 v21,4294868992,v21
M v[216:219],a[152:155],a[168:171],0
v_add_u32 v22,4294868992,v22
M v[248:251],a[152:155],a[172:175],0
T a[152:153],v23,12288
v_lshl_add_u32 v58,v44,4,v58
M v[156:159],a[156:159],a[160:163],0
T a[154:155],v23,12800
v_add_u32 v23,4294868992,v23
M v[188:191],a[156:159],a[164:167],0
WL 4
v_and_b32 v45,1,v41
M v[220:223],a[156:159],a[168:171],0
v_cmp_eq_u32 s[90:91],1,v45
M v[252:255],a[156:159],a[172:175],0
T a[156:157],v24,12288
v_mov_b32 v47,2130706432
M v[128:131],a[128:131],a[176:179],v[128:131]
T a[158:159],v24,12800
v_add_u32 v24,4294868992,v24
M v[160:163],a[128:131],a[180:183],v[160:163]
v_cndmask_b32 v58,v58,v47,s[90:91]
M v[192:195],a[128:131],a[184:187],v[192:195]
v_mul_u32_u24 v59,10240,v46
M v[224:227],a[128:131],a[188:191],v[224:227]
v_mul_u32_u24 v45,80,v44
M v[132:135],a[132:135],a[176:179],v[132:135]
v_add_u32 v59,v59,v45
M v[164:167],a[132:135],a[180:183],v[164:167]
s_mul_i32 s21,s20,2560
M v[196:199],a[132:135],a[184:187],v[196:199]
s_add_u32 s21,s21,98304
WL 0
s_barrier
M v[228:231],a[132:135],a[188:191],v[228:231]
s_add_u32 m0,s67,98304
D a[160:163],v17,0
v_add_u32 v59,s21,v59
M v[136:139],a[136:139],a[176:179],v[136:139]
s_lshl_b32 s28,s31,12
D a[164:167],v17,2048
M v[168:171],a[136:139],a[180:183],v[168:171]
s_lshl_b32 s24,s34,7
D a[168:171],v17,4096
M v[200:203],a[136:139],a[184:187],v[200:203]
s_add_u32 s28,s28,s24
G v1,s[36:39],s64,0
M v[232:235],a[136:139],a[188:191],v[232:235]
D a[172:175],v17,6144
M v[140:143],a[140:143],a[176:179],v[140:143]
s_lshl_b32 s24,s20,5
T a[128:129],v21,0
M v[172:175],a[140:143],a[180:183],v[172:175]
s_add_u32 s28,s28,s24
G v2,s[36:39],s64,1024
M v[204:207],a[140:143],a[184:187],v[204:207]
s_mul_i32 s28,s28,80
T a[130:131],v21,512
v_lshl_add_u32 v45,v41,3,v42
M v[236:239],a[140:143],a[188:191],v[236:239]
T a[132:133],v22,0
M v[144:147],a[144:147],a[176:179],v[144:147]
T a[134:135],v22,512
M v[176:179],a[144:147],a[180:183],v[176:179]
s_lshr_b32 s21,s20,1
G v3,s[36:39],s64,2048
M v[208:211],a[144:147],a[184:187],v[208:211]
s_and_b32 s22,s20,1
T a[136:137],v23,0
M v[240:243],a[144:147],a[188:191],v[240:243]
T a[138:139],v23,512
M v[148:151],a[148:151],a[176:179],v[148:151]
s_mul_i32 s23,s21,10240
G v4,s[36:39],s64,3072
M v[180:183],a[148:151],a[180:183],v[180:183]
s_add_u32 s64,s64,128
T a[140:141],v24,0
M v[212:215],a[148:151],a[184:187],v[212:215]
s_add_u32 m0,s67,102400
T a[142:143],v24,512
M v[244:247],a[148:151],a[188:191],v[244:247]
s_add_u32 s23,s23,98304
T a[144:145],v21,8192
v_mul_u32_u24 v60,80,v45
M v[152:155],a[152:155],a[176:179],v[152:155]
T a[146:147],v21,8704
v_add_u32 v18,4294868992,v18
M v[184:187],a[152:155],a[180:183],v[184:187]
T a[148:149],v22,8192
v_lshl_add_u32 v60,v43,3,v60
M v[216:219],a[152:155],a[184:187],v[216:219]
T a[150:151],v22,8704
v_add_u32 v60,s23,v60
M v[248:251],a[152:155],a[188:191],v[248:251]
s_lshl_b32 s24,s21,2
T a[152:153],v23,8192
v_add_u32 v46,s24,v43
M v[156:159],a[156:159],a[176:179],v[156:159]
G v1,s[36:39],s65,0
M v[188:191],a[156:159],a[180:183],v[188:191]
WL 6
T a[154:155],v23,8704
M v[220:223],a[156:159],a[184:187],v[220:223]
s_add_u32 s24,s24,4
G v2,s[36:39],s65,1024
M v[252:255],a[156:159],a[188:191],v[252:255]
T a[156:157],v24,8192
v_mov_b32 v62,s24
M v[128:131],a[128:131],a[160:163],v[128:131]
T a[158:159],v24,8704
v_cmp_eq_u32 vcc,0,v43
M v[160:163],a[128:131],a[164:167],v[160:163]
D a[176:179],v18,0
v_cndmask_b32 v62,9,v62,vcc
M v[192:195],a[128:131],a[168:171],v[192:195]
s_sub_u32 s98,s22,1
D a[180:183],v18,2048
M v[224:227],a[128:131],a[172:175],v[224:227]
s_mov_b32 s99,s98
G v3,s[36:39],s65,2048
M v[132:135],a[132:135],a[160:163],v[132:135]
D a[184:187],v18,4096
v_cndmask_b32 v46,v62,v46,s[98:99]
M v[164:167],a[132:135],a[164:167],v[164:167]
D a[188:191],v18,6144
v_mul_u32_u24 v61,96,v45
M v[196:199],a[132:135],a[168:171],v[196:199]
G v4,s[36:39],s65,3072
M v[228:231],a[132:135],a[172:175],v[228:231]
s_add_u32 s65,s65,128
T a[128:129],v21,4096
v_lshl_add_u32 v61,v46,3,v61
M v[136:139],a[136:139],a[160:163],v[136:139]
s_add_u32 m0,s68,131072
T a[130:131],v21,4608
v_add_u32 v61,118784,v61
M v[168:171],a[136:139],a[164:167],v[168:171]
WL 5
T a[132:133],v22,4096
v_lshl_add_u32 v62,v41,3,v44
M v[200:203],a[136:139],a[168:171],v[200:203]
T a[134:135],v22,4608
v_lshlrev_b32 v62,4,v62
M v[232:235],a[136:139],a[172:175],v[232:235]
T a[136:137],v23,4096
v_add_u32 v62,2304,v62
M v[140:143],a[140:143],a[160:163],v[140:143]
G v9,s[40:43],s66,0
M v[172:175],a[140:143],a[164:167],v[172:175]
T a[138:139],v23,4608
v_cmp_gt_u32 vcc,8,v44
M v[204:207],a[140:143],a[168:171],v[204:207]
G v10,s[40:43],s66,1024
M v[236:239],a[140:143],a[172:175],v[236:239]
T a[140:141],v24,4096
v_cndmask_b32 v62,v47,v62,vcc
M v[144:147],a[144:147],a[160:163],v[144:147]
T a[142:143],v24,4608
v_cndmask_b32 v62,v62,v0,s[98:99]
M v[176:179],a[144:147],a[164:167],v[176:179]
G v11,s[40:43],s66,2048
M v[208:211],a[144:147],a[168:171],v[208:211]
G v12,s[40:43],s66,3072
M v[240:243],a[144:147],a[172:175],v[240:243]
T a[144:145],v21,12288
v_cmp_eq_u32 vcc,0,v41
M v[148:151],a[148:151],a[160:163],v[148:151]
s_add_u32 s66,s66,524288
s_mul_i32 s15,s14,4096
s_mov_b32 m0,147456
T a[146:147],v21,12800
v_cndmask_b32 v63,v47,v62,vcc
M v[180:183],a[148:151],a[164:167],v[180:183]
s_lshl_b32 s29,s31,1
G v0,s[84:87],s15,0
M v[212:215],a[148:151],a[168:171],v[212:215]
s_add_u32 s29,s29,s21
G v0,s[84:87],s15,1024
M v[244:247],a[148:151],a[172:175],v[244:247]
s_lshl_b32 s29,s29,4
G v0,s[84:87],s15,2048
M v[152:155],a[152:155],a[160:163],v[152:155]
s_add_u32 s29,s29,s34
G v0,s[84:87],s15,3072
M v[184:187],a[152:155],a[164:167],v[184:187]
T a[148:149],v22,12288
v_add_u32 v17,65536,v17
M v[216:219],a[152:155],a[168:171],v[216:219]
s_mul_i32 s29,s29,3456
T a[150:151],v22,12800
v_add_u32 v21,16384,v21
M v[248:251],a[152:155],a[172:175],v[248:251]
s_lshr_b32 s30,1024,s22
T a[152:153],v23,12288
v_add_u32 v22,16384,v22
M v[156:159],a[156:159],a[160:163],v[156:159]
T a[154:155],v23,12800
v_add_u32 v23,16384,v23
M v[188:191],a[156:159],a[164:167],v[188:191]
WL 4
v_lshrrev_b32 v46,1,v41
M v[220:223],a[156:159],a[168:171],v[220:223]
v_mul_u32_u24 v36,960,v46
M v[252:255],a[156:159],a[172:175],v[252:255]
T a[156:157],v24,12288
v_mul_u32_u24 v45,40,v42
M v[128:131],a[128:131],a[176:179],v[128:131]
T a[158:159],v24,12800
v_add_u32 v24,16384,v24
M v[160:163],a[128:131],a[180:183],v[160:163]
v_add_u32 v36,v36,v45
M v[192:195],a[128:131],a[184:187],v[192:195]
v_add_u32 v36,156752,v36
M v[224:227],a[128:131],a[188:191],v[224:227]
v_and_b32 v45,1,v41
M v[132:135],a[132:135],a[176:179],v[132:135]
v_mul_u32_u24 v46,160,v45
M v[164:167],a[132:135],a[180:183],v[164:167]
v_mul_u32_u24 v45,640,v45
M v[196:199],a[132:135],a[184:187],v[196:199]
WL 0
s_barrier
M v[228:231],a[132:135],a[188:191],v[228:231]
s_add_u32 m0,s67,0
D a[160:163],v17,0
v_sub_u32 v37,v36,v45
M v[136:139],a[136:139],a[176:179],v[136:139]
D a[164:167],v17,2048
v_add_u32 v36,v36,v46
M v[168:171],a[136:139],a[180:183],v[168:171]
D a[168:171],v17,4096
v_cmp_eq_u32 vcc,v43,v41
M v[200:203],a[136:139],a[184:187],v[200:203]
G v1,s[36:39],s64,0
M v[232:235],a[136:139],a[188:191],v[232:235]
D a[172:175],v17,6144
v_mov_b32 v45,155648
M v[140:143],a[140:143],a[176:179],v[140:143]
T a[128:129],v21,0
v_cndmask_b32 v36,v45,v36,vcc
M v[172:175],a[140:143],a[180:183],v[172:175]
G v2,s[36:39],s64,1024
M v[204:207],a[140:143],a[184:187],v[204:207]
T a[130:131],v21,512
v_cndmask_b32 v37,v45,v37,vcc
M v[236:239],a[140:143],a[188:191],v[236:239]
T a[132:133],v22,0
v_readfirstlane_b32 s96,v38
M v[144:147],a[144:147],a[176:179],v[144:147]
s_add_u32 s96,s96,1
T a[134:135],v22,512
M v[176:179],a[144:147],a[180:183],v[176:179]
s_xor_b32 s97,s96,1540483477
G v3,s[36:39],s64,2048
M v[208:211],a[144:147],a[184:187],v[208:211]
T a[136:137],v23,0
M v[240:243],a[144:147],a[188:191],v[240:243]
T a[138:139],v23,512
M v[148:151],a[148:151],a[176:179],v[148:151]
G v4,s[36:39],s64,3072
M v[180:183],a[148:151],a[180:183],v[180:183]
s_add_u32 s64,s64,128
T a[140:141],v24,0
M v[212:215],a[148:151],a[184:187],v[212:215]
s_add_u32 m0,s67,4096
T a[142:143],v24,512
M v[244:247],a[148:151],a[188:191],v[244:247]
T a[144:145],v21,8192
M v[152:155],a[152:155],a[176:179],v[152:155]
T a[146:147],v21,8704
v_add_u32 v18,65536,v18
M v[184:187],a[152:155],a[180:183],v[184:187]
T a[148:149],v22,8192
M v[216:219],a[152:155],a[184:187],v[216:219]
T a[150:151],v22,8704
M v[248:251],a[152:155],a[188:191],v[248:251]
T a[152:153],v23,8192
M v[156:159],a[156:159],a[176:179],v[156:159]
G v1,s[36:39],s65,0
M v[188:191],a[156:159],a[180:183],v[188:191]
WL 6
)"
R"(T a[154:155],v23,8704
M v[220:223],a[156:159],a[184:187],v[220:223]
G v2,s[36:39],s65,1024
M v[252:255],a[156:159],a[188:191],v[252:255]
T a[156:157],v24,8192
M v[128:131],a[128:131],a[160:163],v[128:131]
T a[158:159],v24,8704
M v[160:163],a[128:131],a[164:167],v[160:163]
D a[176:179],v18,0
M v[192:195],a[128:131],a[168:171],v[192:195]
D a[180:183],v18,2048
M v[224:227],a[128:131],a[172:175],v[224:227]
G v3,s[36:39],s65,2048
M v[132:135],a[132:135],a[160:163],v[132:135]
D a[184:187],v18,4096
M v[164:167],a[132:135],a[164:167],v[164:167]
D a[188:191],v18,6144
M v[196:199],a[132:135],a[168:171],v[196:199]
G v4,s[36:39],s65,3072
M v[228:231],a[132:135],a[172:175],v[228:231]
s_add_u32 s65,s65,128
T a[128:129],v21,4096
M v[136:139],a[136:139],a[160:163],v[136:139]
s_add_u32 m0,s68,32768
T a[130:131],v21,4608
M v[168:171],a[136:139],a[164:167],v[168:171]
WL 5
T a[132:133],v22,4096
M v[200:203],a[136:139],a[168:171],v[200:203]
T a[134:135],v22,4608
M v[232:235],a[136:139],a[172:175],v[232:235]
T a[136:137],v23,4096
M v[140:143],a[140:143],a[160:163],v[140:143]
G v9,s[40:43],s66,0
M v[172:175],a[140:143],a[164:167],v[172:175]
T a[138:139],v23,4608
M v[204:207],a[140:143],a[168:171],v[204:207]
G v10,s[40:43],s66,1024
M v[236:239],a[140:143],a[172:175],v[236:239]
T a[140:141],v24,4096
M v[144:147],a[144:147],a[160:163],v[144:147]
T a[142:143],v24,4608
M v[176:179],a[144:147],a[164:167],v[176:179]
G v11,s[40:43],s66,2048
M v[208:211],a[144:147],a[168:171],v[208:211]
G v12,s[40:43],s66,3072
M v[240:243],a[144:147],a[172:175],v[240:243]
T a[144:145],v21,12288
M v[148:151],a[148:151],a[160:163],v[148:151]
s_add_u32 s66,s66,524288
s_mul_i32 s15,s14,2304
s_mov_b32 m0,147456
T a[146:147],v21,12800
M v[180:183],a[148:151],a[164:167],v[180:183]
G v0,s[44:47],s15,0
M v[212:215],a[148:151],a[168:171],v[212:215]
G v0,s[44:47],s15,1024
M v[244:247],a[148:151],a[172:175],v[244:247]
G v0,s[44:47],s15,2048
M v[152:155],a[152:155],a[160:163],v[152:155]
T a[148:149],v22,12288
v_add_u32 v17,32768,v17
M v[184:187],a[152:155],a[164:167],v[184:187]
T a[150:151],v22,12800
v_add_u32 v21,81920,v21
M v[216:219],a[152:155],a[168:171],v[216:219]
v_add_u32 v22,81920,v22
M v[248:251],a[152:155],a[172:175],v[248:251]
T a[152:153],v23,12288
M v[156:159],a[156:159],a[160:163],v[156:159]
T a[154:155],v23,12800
v_add_u32 v23,81920,v23
M v[188:191],a[156:159],a[164:167],v[188:191]
WL 4
M v[220:223],a[156:159],a[168:171],v[220:223]
M v[252:255],a[156:159],a[172:175],v[252:255]
T a[156:157],v24,12288
M v[128:131],a[128:131],a[176:179],v[128:131]
T a[158:159],v24,12800
v_add_u32 v24,81920,v24
M v[160:163],a[128:131],a[180:183],v[160:163]
M v[192:195],a[128:131],a[184:187],v[192:195]
M v[224:227],a[128:131],a[188:191],v[224:227]
M v[132:135],a[132:135],a[176:179],v[132:135]
M v[164:167],a[132:135],a[180:183],v[164:167]
M v[196:199],a[132:135],a[184:187],v[196:199]
s_waitcnt vmcnt(19) lgkmcnt(0)
s_barrier
M v[228:231],a[132:135],a[188:191],v[228:231]
s_add_u32 m0,s67,65536
D a[160:163],v17,0
M v[136:139],a[136:139],a[176:179],v[136:139]
D a[164:167],v17,2048
M v[168:171],a[136:139],a[180:183],v[168:171]
D a[168:171],v17,4096
M v[200:203],a[136:139],a[184:187],v[200:203]
G v1,s[36:39],s64,0
M v[232:235],a[136:139],a[188:191],v[232:235]
D a[172:175],v17,6144
M v[140:143],a[140:143],a[176:179],v[140:143]
T a[128:129],v21,0
M v[172:175],a[140:143],a[180:183],v[172:175]
G v2,s[36:39],s64,1024
M v[204:207],a[140:143],a[184:187],v[204:207]
T a[130:131],v21,512
M v[236:239],a[140:143],a[188:191],v[236:239]
T a[132:133],v22,0
M v[144:147],a[144:147],a[176:179],v[144:147]
T a[134:135],v22,512
M v[176:179],a[144:147],a[180:183],v[176:179]
G v3,s[36:39],s64,2048
M v[208:211],a[144:147],a[184:187],v[208:211]
T a[136:137],v23,0
M v[240:243],a[144:147],a[188:191],v[240:243]
T a[138:139],v23,512
M v[148:151],a[148:151],a[176:179],v[148:151]
G v4,s[36:39],s64,3072
M v[180:183],a[148:151],a[180:183],v[180:183]
s_add_u32 s64,s64,128
T a[140:141],v24,0
M v[212:215],a[148:151],a[184:187],v[212:215]
s_add_u32 m0,s67,69632
T a[142:143],v24,512
M v[244:247],a[148:151],a[188:191],v[244:247]
T a[144:145],v21,8192
M v[152:155],a[152:155],a[176:179],v[152:155]
T a[146:147],v21,8704
v_add_u32 v18,32768,v18
M v[184:187],a[152:155],a[180:183],v[184:187]
T a[148:149],v22,8192
M v[216:219],a[152:155],a[184:187],v[216:219]
T a[150:151],v22,8704
M v[248:251],a[152:155],a[188:191],v[248:251]
T a[152:153],v23,8192
M v[156:159],a[156:159],a[176:179],v[156:159]
G v1,s[36:39],s65,0
M v[188:191],a[156:159],a[180:183],v[188:191]
WL 6
T a[154:155],v23,8704
M v[220:223],a[156:159],a[184:187],v[220:223]
G v2,s[36:39],s65,1024
M v[252:255],a[156:159],a[188:191],v[252:255]
T a[156:157],v24,8192
M v[128:131],a[128:131],a[160:163],v[128:131]
T a[158:159],v24,8704
M v[160:163],a[128:131],a[164:167],v[160:163]
D a[176:179],v18,0
M v[192:195],a[128:131],a[168:171],v[192:195]
D a[180:183],v18,2048
M v[224:227],a[128:131],a[172:175],v[224:227]
G v3,s[36:39],s65,2048
M v[132:135],a[132:135],a[160:163],v[132:135]
D a[184:187],v18,4096
M v[164:167],a[132:135],a[164:167],v[164:167]
D a[188:191],v18,6144
M v[196:199],a[132:135],a[168:171],v[196:199]
G v4,s[36:39],s65,3072
M v[228:231],a[132:135],a[172:175],v[228:231]
s_add_u32 s65,s65,128
T a[128:129],v21,4096
M v[136:139],a[136:139],a[160:163],v[136:139]
s_add_u32 m0,s68,49152
T a[130:131],v21,4608
M v[168:171],a[136:139],a[164:167],v[168:171]
WL 5
T a[132:133],v22,4096
M v[200:203],a[136:139],a[168:171],v[200:203]
T a[134:135],v22,4608
M v[232:235],a[136:139],a[172:175],v[232:235]
T a[136:137],v23,4096
M v[140:143],a[140:143],a[160:163],v[140:143]
G v9,s[40:43],s66,0
M v[172:175],a[140:143],a[164:167],v[172:175]
T a[138:139],v23,4608
M v[204:207],a[140:143],a[168:171],v[204:207]
G v10,s[40:43],s66,1024
M v[236:239],a[140:143],a[172:175],v[236:239]
T a[140:141],v24,4096
M v[144:147],a[144:147],a[160:163],v[144:147]
T a[142:143],v24,4608
M v[176:179],a[144:147],a[164:167],v[176:179]
G v11,s[40:43],s66,2048
M v[208:211],a[144:147],a[168:171],v[208:211]
G v12,s[40:43],s66,3072
M v[240:243],a[144:147],a[172:175],v[240:243]
T a[144:145],v21,12288
M v[148:151],a[148:151],a[160:163],v[148:151]
s_add_u32 s66,s66,524288
s_mul_i32 s15,s14,4608
s_mov_b32 m0,147456
T a[146:147],v21,12800
M v[180:183],a[148:151],a[164:167],v[180:183]
G v0,s[48:51],s15,0
M v[212:215],a[148:151],a[168:171],v[212:215]
G v0,s[48:51],s15,1024
M v[244:247],a[148:151],a[172:175],v[244:247]
G v0,s[48:51],s15,2048
M v[152:155],a[152:155],a[160:163],v[152:155]
G v0,s[48:51],s15,3072
M v[184:187],a[152:155],a[164:167],v[184:187]
s_add_u32 s15,s15,4096
T a[148:149],v22,12288
v_add_u32 v17,4294868992,v17
M v[216:219],a[152:155],a[168:171],v[216:219]
T a[150:151],v22,12800
v_add_u32 v21,4294868992,v21
M v[248:251],a[152:155],a[172:175],v[248:251]
G v0,s[48:51],s15,0
M v[156:159],a[156:159],a[160:163],v[156:159]
T a[152:153],v23,12288
v_add_u32 v22,4294868992,v22
M v[188:191],a[156:159],a[164:167],v[188:191]
WL 3
T a[154:155],v23,12800
v_add_u32 v23,4294868992,v23
M v[220:223],a[156:159],a[168:171],v[220:223]
M v[252:255],a[156:159],a[172:175],v[252:255]
T a[156:157],v24,12288
M v[128:131],a[128:131],a[176:179],v[128:131]
T a[158:159],v24,12800
v_add_u32 v24,4294868992,v24
M v[160:163],a[128:131],a[180:183],v[160:163]
M v[192:195],a[128:131],a[184:187],v[192:195]
M v[224:227],a[128:131],a[188:191],v[224:227]
M v[132:135],a[132:135],a[176:179],v[132:135]
M v[164:167],a[132:135],a[180:183],v[164:167]
M v[196:199],a[132:135],a[184:187],v[196:199]
s_waitcnt vmcnt(20) lgkmcnt(0)
s_barrier
M v[228:231],a[132:135],a[188:191],v[228:231]
s_add_u32 m0,s67,98304
D a[160:163],v17,0
M v[136:139],a[136:139],a[176:179],v[136:139]
D a[164:167],v17,2048
M v[168:171],a[136:139],a[180:183],v[168:171]
D a[168:171],v17,4096
M v[200:203],a[136:139],a[184:187],v[200:203]
G v1,s[36:39],s64,0
M v[232:235],a[136:139],a[188:191],v[232:235]
D a[172:175],v17,6144
M v[140:143],a[140:143],a[176:179],v[140:143]
T a[128:129],v21,0
M v[172:175],a[140:143],a[180:183],v[172:175]
G v2,s[36:39],s64,1024
M v[204:207],a[140:143],a[184:187],v[204:207]
T a[130:131],v21,512
M v[236:239],a[140:143],a[188:191],v[236:239]
T a[132:133],v22,0
M v[144:147],a[144:147],a[176:179],v[144:147]
T a[134:135],v22,512
M v[176:179],a[144:147],a[180:183],v[176:179]
G v3,s[36:39],s64,2048
M v[208:211],a[144:147],a[184:187],v[208:211]
T a[136:137],v23,0
M v[240:243],a[144:147],a[188:191],v[240:243]
T a[138:139],v23,512
M v[148:151],a[148:151],a[176:179],v[148:151]
G v4,s[36:39],s64,3072
M v[180:183],a[148:151],a[180:183],v[180:183]
s_add_u32 s64,s64,128
T a[140:141],v24,0
M v[212:215],a[148:151],a[184:187],v[212:215]
s_add_u32 m0,s67,102400
T a[142:143],v24,512
M v[244:247],a[148:151],a[188:191],v[244:247]
T a[144:145],v21,8192
M v[152:155],a[152:155],a[176:179],v[152:155]
T a[146:147],v21,8704
v_add_u32 v18,4294868992,v18
M v[184:187],a[152:155],a[180:183],v[184:187]
T a[148:149],v22,8192
M v[216:219],a[152:155],a[184:187],v[216:219]
T a[150:151],v22,8704
M v[248:251],a[152:155],a[188:191],v[248:251]
T a[152:153],v23,8192
M v[156:159],a[156:159],a[176:179],v[156:159]
G v1,s[36:39],s65,0
M v[188:191],a[156:159],a[180:183],v[188:191]
WL 6
T a[154:155],v23,8704
M v[220:223],a[156:159],a[184:187],v[220:223]
G v2,s[36:39],s65,1024
M v[252:255],a[156:159],a[188:191],v[252:255]
T a[156:157],v24,8192
M v[128:131],a[128:131],a[160:163],v[128:131]
T a[158:159],v24,8704
M v[160:163],a[128:131],a[164:167],v[160:163]
D a[176:179],v18,0
M v[192:195],a[128:131],a[168:171],v[192:195]
D a[180:183],v18,2048
M v[224:227],a[128:131],a[172:175],v[224:227]
G v3,s[36:39],s65,2048
M v[132:135],a[132:135],a[160:163],v[132:135]
D a[184:187],v18,4096
M v[164:167],a[132:135],a[164:167],v[164:167]
D a[188:191],v18,6144
M v[196:199],a[132:135],a[168:171],v[196:199]
G v4,s[36:39],s65,3072
M v[228:231],a[132:135],a[172:175],v[228:231]
s_add_u32 s65,s65,128
T a[128:129],v21,4096
M v[136:139],a[136:139],a[160:163],v[136:139]
s_add_u32 m0,s68,131072
T a[130:131],v21,4608
M v[168:171],a[136:139],a[164:167],v[168:171]
WL 5
T a[132:133],v22,4096
M v[200:203],a[136:139],a[168:171],v[200:203]
T a[134:135],v22,4608
M v[232:235],a[136:139],a[172:175],v[232:235]
T a[136:137],v23,4096
M v[140:143],a[140:143],a[160:163],v[140:143]
G v9,s[40:43],s66,0
M v[172:175],a[140:143],a[164:167],v[172:175]
T a[138:139],v23,4608
M v[204:207],a[140:143],a[168:171],v[204:207]
G v10,s[40:43],s66,1024
M v[236:239],a[140:143],a[172:175],v[236:239]
T a[140:141],v24,4096
M v[144:147],a[144:147],a[160:163],v[144:147]
T a[142:143],v24,4608
M v[176:179],a[144:147],a[164:167],v[176:179]
G v11,s[40:43],s66,2048
M v[208:211],a[144:147],a[168:171],v[208:211]
G v12,s[40:43],s66,3072
M v[240:243],a[144:147],a[172:175],v[240:243]
T a[144:145],v21,12288
M v[148:151],a[148:151],a[160:163],v[148:151]
s_add_u32 s66,s66,524288
T a[146:147],v21,12800
M v[180:183],a[148:151],a[164:167],v[180:183]
M v[212:215],a[148:151],a[168:171],v[212:215]
M v[244:247],a[148:151],a[172:175],v[244:247]
T a[148:149],v22,12288
M v[152:155],a[152:155],a[160:163],v[152:155]
T a[150:151],v22,12800
v_add_u32 v17,65536,v17
M v[184:187],a[152:155],a[164:167],v[184:187]
v_add_u32 v21,16384,v21
M v[216:219],a[152:155],a[168:171],v[216:219]
v_add_u32 v22,16384,v22
M v[248:251],a[152:155],a[172:175],v[248:251]
T a[152:153],v23,12288
M v[156:159],a[156:159],a[160:163],v[156:159]
T a[154:155],v23,12800
v_add_u32 v23,16384,v23
M v[188:191],a[156:159],a[164:167],v[188:191]
WL 4
M v[220:223],a[156:159],a[168:171],v[220:223]
M v[252:255],a[156:159],a[172:175],v[252:255]
T a[156:157],v24,12288
M v[128:131],a[128:131],a[176:179],v[128:131]
T a[158:159],v24,12800
v_add_u32 v24,16384,v24
M v[160:163],a[128:131],a[180:183],v[160:163]
M v[192:195],a[128:131],a[184:187],v[192:195]
M v[224:227],a[128:131],a[188:191],v[224:227]
M v[132:135],a[132:135],a[176:179],v[132:135]
M v[164:167],a[132:135],a[180:183],v[164:167]
M v[196:199],a[132:135],a[184:187],v[196:199]
s_waitcnt vmcnt(17) lgkmcnt(0)
s_barrier
M v[228:231],a[132:135],a[188:191],v[228:231]
s_add_u32 m0,s67,0
D a[160:163],v17,0
M v[136:139],a[136:139],a[176:179],v[136:139]
D a[164:167],v17,2048
M v[168:171],a[136:139],a[180:183],v[168:171]
D a[168:171],v17,4096
M v[200:203],a[136:139],a[184:187],v[200:203]
G v1,s[36:39],s64,0
M v[232:235],a[136:139],a[188:191],v[232:235]
D a[172:175],v17,6144
M v[140:143],a[140:143],a[176:179],v[140:143]
T a[128:129],v21,0
M v[172:175],a[140:143],a[180:183],v[172:175]
G v2,s[36:39],s64,1024
M v[204:207],a[140:143],a[184:187],v[204:207]
T a[130:131],v21,512
M v[236:239],a[140:143],a[188:191],v[236:239]
T a[132:133],v22,0
M v[144:147],a[144:147],a[176:179],v[144:147]
T a[134:135],v22,512
M v[176:179],a[144:147],a[180:183],v[176:179]
G v3,s[36:39],s64,2048
M v[208:211],a[144:147],a[184:187],v[208:211]
T a[136:137],v23,0
M v[240:243],a[144:147],a[188:191],v[240:243]
T a[138:139],v23,512
M v[148:151],a[148:151],a[176:179],v[148:151]
G v4,s[36:39],s64,3072
M v[180:183],a[148:151],a[180:183],v[180:183]
s_add_u32 s64,s64,128
T a[140:141],v24,0
M v[212:215],a[148:151],a[184:187],v[212:215]
s_add_u32 m0,s67,4096
T a[142:143],v24,512
M v[244:247],a[148:151],a[188:191],v[244:247]
T a[144:145],v21,8192
M v[152:155],a[152:155],a[176:179],v[152:155]
)"
R"(T a[146:147],v21,8704
v_add_u32 v18,65536,v18
M v[184:187],a[152:155],a[180:183],v[184:187]
T a[148:149],v22,8192
M v[216:219],a[152:155],a[184:187],v[216:219]
T a[150:151],v22,8704
M v[248:251],a[152:155],a[188:191],v[248:251]
T a[152:153],v23,8192
M v[156:159],a[156:159],a[176:179],v[156:159]
G v1,s[36:39],s65,0
M v[188:191],a[156:159],a[180:183],v[188:191]
WL 6
T a[154:155],v23,8704
M v[220:223],a[156:159],a[184:187],v[220:223]
G v2,s[36:39],s65,1024
M v[252:255],a[156:159],a[188:191],v[252:255]
T a[156:157],v24,8192
M v[128:131],a[128:131],a[160:163],v[128:131]
T a[158:159],v24,8704
M v[160:163],a[128:131],a[164:167],v[160:163]
D a[176:179],v18,0
M v[192:195],a[128:131],a[168:171],v[192:195]
D a[180:183],v18,2048
M v[224:227],a[128:131],a[172:175],v[224:227]
G v3,s[36:39],s65,2048
M v[132:135],a[132:135],a[160:163],v[132:135]
D a[184:187],v18,4096
M v[164:167],a[132:135],a[164:167],v[164:167]
D a[188:191],v18,6144
M v[196:199],a[132:135],a[168:171],v[196:199]
G v4,s[36:39],s65,3072
M v[228:231],a[132:135],a[172:175],v[228:231]
s_add_u32 s65,s65,128
T a[128:129],v21,4096
M v[136:139],a[136:139],a[160:163],v[136:139]
s_add_u32 m0,s68,32768
T a[130:131],v21,4608
M v[168:171],a[136:139],a[164:167],v[168:171]
WL 5
T a[132:133],v22,4096
M v[200:203],a[136:139],a[168:171],v[200:203]
T a[134:135],v22,4608
M v[232:235],a[136:139],a[172:175],v[232:235]
T a[136:137],v23,4096
M v[140:143],a[140:143],a[160:163],v[140:143]
G v9,s[40:43],s66,0
M v[172:175],a[140:143],a[164:167],v[172:175]
T a[138:139],v23,4608
M v[204:207],a[140:143],a[168:171],v[204:207]
G v10,s[40:43],s66,1024
M v[236:239],a[140:143],a[172:175],v[236:239]
T a[140:141],v24,4096
M v[144:147],a[144:147],a[160:163],v[144:147]
T a[142:143],v24,4608
M v[176:179],a[144:147],a[164:167],v[176:179]
G v11,s[40:43],s66,2048
M v[208:211],a[144:147],a[168:171],v[208:211]
G v12,s[40:43],s66,3072
M v[240:243],a[144:147],a[172:175],v[240:243]
T a[144:145],v21,12288
M v[148:151],a[148:151],a[160:163],v[148:151]
s_add_u32 s66,s66,524288
s_mul_i32 s15,s14,2304
s_mov_b32 m0,147456
T a[146:147],v21,12800
M v[180:183],a[148:151],a[164:167],v[180:183]
G v0,s[52:55],s15,0
M v[212:215],a[148:151],a[168:171],v[212:215]
G v0,s[52:55],s15,1024
M v[244:247],a[148:151],a[172:175],v[244:247]
G v0,s[52:55],s15,2048
M v[152:155],a[152:155],a[160:163],v[152:155]
T a[148:149],v22,12288
v_add_u32 v17,32768,v17
M v[184:187],a[152:155],a[164:167],v[184:187]
T a[150:151],v22,12800
v_add_u32 v21,81920,v21
M v[216:219],a[152:155],a[168:171],v[216:219]
v_add_u32 v22,81920,v22
M v[248:251],a[152:155],a[172:175],v[248:251]
T a[152:153],v23,12288
M v[156:159],a[156:159],a[160:163],v[156:159]
T a[154:155],v23,12800
v_add_u32 v23,81920,v23
M v[188:191],a[156:159],a[164:167],v[188:191]
WL 4
M v[220:223],a[156:159],a[168:171],v[220:223]
M v[252:255],a[156:159],a[172:175],v[252:255]
T a[156:157],v24,12288
M v[128:131],a[128:131],a[176:179],v[128:131]
T a[158:159],v24,12800
v_add_u32 v24,81920,v24
M v[160:163],a[128:131],a[180:183],v[160:163]
M v[192:195],a[128:131],a[184:187],v[192:195]
M v[224:227],a[128:131],a[188:191],v[224:227]
M v[132:135],a[132:135],a[176:179],v[132:135]
M v[164:167],a[132:135],a[180:183],v[164:167]
M v[196:199],a[132:135],a[184:187],v[196:199]
s_waitcnt vmcnt(15) lgkmcnt(0)
s_barrier
M v[228:231],a[132:135],a[188:191],v[228:231]
s_sub_u32 s64,s64,1024
D a[160:163],v17,0
M v[136:139],a[136:139],a[176:179],v[136:139]
s_sub_u32 s65,s65,1024
D a[164:167],v17,2048
M v[168:171],a[136:139],a[180:183],v[168:171]
s_add_u32 m0,s67,65536
D a[168:171],v17,4096
M v[200:203],a[136:139],a[184:187],v[200:203]
D a[172:175],v17,6144
M v[232:235],a[136:139],a[188:191],v[232:235]
T a[128:129],v21,0
M v[140:143],a[140:143],a[176:179],v[140:143]
T a[130:131],v21,512
M v[172:175],a[140:143],a[180:183],v[172:175]
T a[132:133],v22,0
M v[204:207],a[140:143],a[184:187],v[204:207]
G v1,s[36:39],s64,0
M v[236:239],a[140:143],a[188:191],v[236:239]
T a[134:135],v22,512
M v[144:147],a[144:147],a[176:179],v[144:147]
T a[136:137],v23,0
M v[176:179],a[144:147],a[180:183],v[176:179]
G v2,s[36:39],s64,1024
M v[208:211],a[144:147],a[184:187],v[208:211]
T a[138:139],v23,512
M v[240:243],a[144:147],a[188:191],v[240:243]
T a[140:141],v24,0
M v[148:151],a[148:151],a[176:179],v[148:151]
G v3,s[36:39],s64,2048
M v[180:183],a[148:151],a[180:183],v[180:183]
T a[142:143],v24,512
M v[212:215],a[148:151],a[184:187],v[212:215]
T a[144:145],v21,8192
M v[244:247],a[148:151],a[188:191],v[244:247]
G v4,s[36:39],s64,3072
M v[152:155],a[152:155],a[176:179],v[152:155]
s_add_u32 s64,s64,128
T a[146:147],v21,8704
v_add_u32 v18,32768,v18
M v[184:187],a[152:155],a[180:183],v[184:187]
s_add_u32 m0,s67,69632
T a[148:149],v22,8192
M v[216:219],a[152:155],a[184:187],v[216:219]
T a[150:151],v22,8704
M v[248:251],a[152:155],a[188:191],v[248:251]
T a[152:153],v23,8192
M v[156:159],a[156:159],a[176:179],v[156:159]
T a[154:155],v23,8704
M v[188:191],a[156:159],a[180:183],v[188:191]
WL 6
G v1,s[36:39],s65,0
M v[220:223],a[156:159],a[184:187],v[220:223]
G v2,s[36:39],s65,1024
M v[252:255],a[156:159],a[188:191],v[252:255]
T a[156:157],v24,8192
M v[128:131],a[128:131],a[160:163],v[128:131]
T a[158:159],v24,8704
M v[160:163],a[128:131],a[164:167],v[160:163]
D a[176:179],v18,0
M v[192:195],a[128:131],a[168:171],v[192:195]
D a[180:183],v18,2048
M v[224:227],a[128:131],a[172:175],v[224:227]
D a[184:187],v18,4096
M v[132:135],a[132:135],a[160:163],v[132:135]
G v3,s[36:39],s65,2048
M v[164:167],a[132:135],a[164:167],v[164:167]
D a[188:191],v18,6144
M v[196:199],a[132:135],a[168:171],v[196:199]
G v4,s[36:39],s65,3072
M v[228:231],a[132:135],a[172:175],v[228:231]
s_add_u32 s65,s65,128
T a[128:129],v21,4096
M v[136:139],a[136:139],a[160:163],v[136:139]
s_sub_u32 s66,s66,4194304
T a[130:131],v21,4608
M v[168:171],a[136:139],a[164:167],v[168:171]
s_add_u32 m0,s68,49152
T a[132:133],v22,4096
M v[200:203],a[136:139],a[168:171],v[200:203]
T a[134:135],v22,4608
M v[232:235],a[136:139],a[172:175],v[232:235]
T a[136:137],v23,4096
M v[140:143],a[140:143],a[160:163],v[140:143]
T a[138:139],v23,4608
M v[172:175],a[140:143],a[164:167],v[172:175]
WL 6
G v9,s[84:87],s66,0
M v[204:207],a[140:143],a[168:171],v[204:207]
G v10,s[84:87],s66,1024
M v[236:239],a[140:143],a[172:175],v[236:239]
T a[140:141],v24,4096
M v[144:147],a[144:147],a[160:163],v[144:147]
T a[142:143],v24,4608
M v[176:179],a[144:147],a[164:167],v[176:179]
G v11,s[84:87],s66,2048
M v[208:211],a[144:147],a[168:171],v[208:211]
G v12,s[84:87],s66,3072
M v[240:243],a[144:147],a[172:175],v[240:243]
T a[144:145],v21,12288
M v[148:151],a[148:151],a[160:163],v[148:151]
s_add_u32 s66,s66,524288
s_mul_i32 s15,s14,1944
s_mov_b32 m0,147456
T a[146:147],v21,12800
M v[180:183],a[148:151],a[164:167],v[180:183]
G v0,s[56:59],s15,0
M v[212:215],a[148:151],a[168:171],v[212:215]
G v0,s[56:59],s15,1024
M v[244:247],a[148:151],a[172:175],v[244:247]
T a[148:149],v22,12288
M v[152:155],a[152:155],a[160:163],v[152:155]
T a[150:151],v22,12800
v_add_u32 v17,4294868992,v17
M v[184:187],a[152:155],a[164:167],v[184:187]
v_add_u32 v21,4294868992,v21
M v[216:219],a[152:155],a[168:171],v[216:219]
v_add_u32 v22,4294868992,v22
M v[248:251],a[152:155],a[172:175],v[248:251]
T a[152:153],v23,12288
M v[156:159],a[156:159],a[160:163],v[156:159]
T a[154:155],v23,12800
v_add_u32 v23,4294868992,v23
M v[188:191],a[156:159],a[164:167],v[188:191]
WL 4
M v[220:223],a[156:159],a[168:171],v[220:223]
M v[252:255],a[156:159],a[172:175],v[252:255]
T a[156:157],v24,12288
M v[128:131],a[128:131],a[176:179],v[128:131]
T a[158:159],v24,12800
v_add_u32 v24,4294868992,v24
M v[160:163],a[128:131],a[180:183],v[160:163]
M v[192:195],a[128:131],a[184:187],v[192:195]
M v[224:227],a[128:131],a[188:191],v[224:227]
M v[132:135],a[132:135],a[176:179],v[132:135]
M v[164:167],a[132:135],a[180:183],v[164:167]
M v[196:199],a[132:135],a[184:187],v[196:199]
s_waitcnt vmcnt(17) lgkmcnt(0)
s_barrier
M v[228:231],a[132:135],a[188:191],v[228:231]
s_add_u32 m0,s67,98304
D a[160:163],v17,0
M v[136:139],a[136:139],a[176:179],v[136:139]
D a[164:167],v17,2048
M v[168:171],a[136:139],a[180:183],v[168:171]
D a[168:171],v17,4096
M v[200:203],a[136:139],a[184:187],v[200:203]
G v1,s[36:39],s64,0
M v[232:235],a[136:139],a[188:191],v[232:235]
D a[172:175],v17,6144
M v[140:143],a[140:143],a[176:179],v[140:143]
T a[128:129],v21,0
M v[172:175],a[140:143],a[180:183],v[172:175]
G v2,s[36:39],s64,1024
M v[204:207],a[140:143],a[184:187],v[204:207]
T a[130:131],v21,512
M v[236:239],a[140:143],a[188:191],v[236:239]
T a[132:133],v22,0
M v[144:147],a[144:147],a[176:179],v[144:147]
T a[134:135],v22,512
M v[176:179],a[144:147],a[180:183],v[176:179]
G v3,s[36:39],s64,2048
M v[208:211],a[144:147],a[184:187],v[208:211]
T a[136:137],v23,0
M v[240:243],a[144:147],a[188:191],v[240:243]
T a[138:139],v23,512
M v[148:151],a[148:151],a[176:179],v[148:151]
G v4,s[36:39],s64,3072
M v[180:183],a[148:151],a[180:183],v[180:183]
s_add_u32 s64,s64,128
T a[140:141],v24,0
M v[212:215],a[148:151],a[184:187],v[212:215]
s_add_u32 m0,s67,102400
T a[142:143],v24,512
M v[244:247],a[148:151],a[188:191],v[244:247]
T a[144:145],v21,8192
M v[152:155],a[152:155],a[176:179],v[152:155]
T a[146:147],v21,8704
v_add_u32 v18,4294868992,v18
M v[184:187],a[152:155],a[180:183],v[184:187]
T a[148:149],v22,8192
M v[216:219],a[152:155],a[184:187],v[216:219]
T a[150:151],v22,8704
M v[248:251],a[152:155],a[188:191],v[248:251]
T a[152:153],v23,8192
M v[156:159],a[156:159],a[176:179],v[156:159]
G v1,s[36:39],s65,0
M v[188:191],a[156:159],a[180:183],v[188:191]
WL 6
T a[154:155],v23,8704
M v[220:223],a[156:159],a[184:187],v[220:223]
G v2,s[36:39],s65,1024
M v[252:255],a[156:159],a[188:191],v[252:255]
T a[156:157],v24,8192
M v[128:131],a[128:131],a[160:163],v[128:131]
T a[158:159],v24,8704
M v[160:163],a[128:131],a[164:167],v[160:163]
D a[176:179],v18,0
M v[192:195],a[128:131],a[168:171],v[192:195]
D a[180:183],v18,2048
M v[224:227],a[128:131],a[172:175],v[224:227]
G v3,s[36:39],s65,2048
M v[132:135],a[132:135],a[160:163],v[132:135]
D a[184:187],v18,4096
M v[164:167],a[132:135],a[164:167],v[164:167]
D a[188:191],v18,6144
M v[196:199],a[132:135],a[168:171],v[196:199]
G v4,s[36:39],s65,3072
M v[228:231],a[132:135],a[172:175],v[228:231]
s_add_u32 s65,s65,128
T a[128:129],v21,4096
M v[136:139],a[136:139],a[160:163],v[136:139]
s_add_u32 m0,s68,131072
T a[130:131],v21,4608
M v[168:171],a[136:139],a[164:167],v[168:171]
WL 5
T a[132:133],v22,4096
M v[200:203],a[136:139],a[168:171],v[200:203]
T a[134:135],v22,4608
M v[232:235],a[136:139],a[172:175],v[232:235]
T a[136:137],v23,4096
M v[140:143],a[140:143],a[160:163],v[140:143]
G v9,s[84:87],s66,0
M v[172:175],a[140:143],a[164:167],v[172:175]
T a[138:139],v23,4608
M v[204:207],a[140:143],a[168:171],v[204:207]
G v10,s[84:87],s66,1024
M v[236:239],a[140:143],a[172:175],v[236:239]
T a[140:141],v24,4096
M v[144:147],a[144:147],a[160:163],v[144:147]
T a[142:143],v24,4608
M v[176:179],a[144:147],a[164:167],v[176:179]
G v11,s[84:87],s66,2048
M v[208:211],a[144:147],a[168:171],v[208:211]
G v12,s[84:87],s66,3072
M v[240:243],a[144:147],a[172:175],v[240:243]
T a[144:145],v21,12288
M v[148:151],a[148:151],a[160:163],v[148:151]
s_add_u32 s66,s66,524288
T a[146:147],v21,12800
M v[180:183],a[148:151],a[164:167],v[180:183]
M v[212:215],a[148:151],a[168:171],v[212:215]
M v[244:247],a[148:151],a[172:175],v[244:247]
T a[148:149],v22,12288
M v[152:155],a[152:155],a[160:163],v[152:155]
T a[150:151],v22,12800
v_add_u32 v17,65536,v17
M v[184:187],a[152:155],a[164:167],v[184:187]
v_add_u32 v21,16384,v21
M v[216:219],a[152:155],a[168:171],v[216:219]
v_add_u32 v22,16384,v22
M v[248:251],a[152:155],a[172:175],v[248:251]
T a[152:153],v23,12288
M v[156:159],a[156:159],a[160:163],v[156:159]
T a[154:155],v23,12800
v_add_u32 v23,16384,v23
M v[188:191],a[156:159],a[164:167],v[188:191]
WL 4
M v[220:223],a[156:159],a[168:171],v[220:223]
M v[252:255],a[156:159],a[172:175],v[252:255]
T a[156:157],v24,12288
M v[128:131],a[128:131],a[176:179],v[128:131]
T a[158:159],v24,12800
v_add_u32 v24,16384,v24
M v[160:163],a[128:131],a[180:183],v[160:163]
M v[192:195],a[128:131],a[184:187],v[192:195]
M v[224:227],a[128:131],a[188:191],v[224:227]
M v[132:135],a[132:135],a[176:179],v[132:135]
M v[164:167],a[132:135],a[180:183],v[164:167]
M v[196:199],a[132:135],a[184:187],v[196:199]
s_waitcnt vmcnt(14) lgkmcnt(0)
s_barrier
M v[228:231],a[132:135],a[188:191],v[228:231]
s_add_u32 m0,s67,0
D a[160:163],v17,0
M v[136:139],a[136:139],a[176:179],v[136:139]
D a[164:167],v17,2048
M v[168:171],a[136:139],a[180:183],v[168:171]
D a[168:171],v17,4096
M v[200:203],a[136:139],a[184:187],v[200:203]
G v1,s[36:39],s64,0
M v[232:235],a[136:139],a[188:191],v[232:235]
D a[172:175],v17,6144
M v[140:143],a[140:143],a[176:179],v[140:143]
T a[128:129],v21,0
M v[172:175],a[140:143],a[180:183],v[172:175]
G v2,s[36:39],s64,1024
M v[204:207],a[140:143],a[184:187],v[204:207]
T a[130:131],v21,512
M v[236:239],a[140:143],a[188:191],v[236:239]
T a[132:133],v22,0
M v[144:147],a[144:147],a[176:179],v[144:147]
T a[134:135],v22,512
M v[176:179],a[144:147],a[180:183],v[176:179]
G v3,s[36:39],s64,2048
M v[208:211],a[144:147],a[184:187],v[208:211]
T a[136:137],v23,0
M v[240:243],a[144:147],a[188:191],v[240:243]
T a[138:139],v23,512
)"
R"(M v[148:151],a[148:151],a[176:179],v[148:151]
G v4,s[36:39],s64,3072
M v[180:183],a[148:151],a[180:183],v[180:183]
s_add_u32 s64,s64,128
T a[140:141],v24,0
M v[212:215],a[148:151],a[184:187],v[212:215]
s_add_u32 m0,s67,4096
T a[142:143],v24,512
M v[244:247],a[148:151],a[188:191],v[244:247]
T a[144:145],v21,8192
M v[152:155],a[152:155],a[176:179],v[152:155]
T a[146:147],v21,8704
v_add_u32 v18,65536,v18
M v[184:187],a[152:155],a[180:183],v[184:187]
T a[148:149],v22,8192
M v[216:219],a[152:155],a[184:187],v[216:219]
T a[150:151],v22,8704
M v[248:251],a[152:155],a[188:191],v[248:251]
T a[152:153],v23,8192
M v[156:159],a[156:159],a[176:179],v[156:159]
G v1,s[36:39],s65,0
M v[188:191],a[156:159],a[180:183],v[188:191]
WL 6
T a[154:155],v23,8704
M v[220:223],a[156:159],a[184:187],v[220:223]
G v2,s[36:39],s65,1024
M v[252:255],a[156:159],a[188:191],v[252:255]
T a[156:157],v24,8192
M a[0:3],a[160:163],a[128:131],0
T a[158:159],v24,8704
M a[32:35],a[164:167],a[128:131],0
D a[176:179],v18,0
M a[64:67],a[168:171],a[128:131],0
D a[180:183],v18,2048
M a[96:99],a[172:175],a[128:131],0
G v3,s[36:39],s65,2048
M a[4:7],a[160:163],a[132:135],0
D a[184:187],v18,4096
M a[36:39],a[164:167],a[132:135],0
D a[188:191],v18,6144
M a[68:71],a[168:171],a[132:135],0
G v4,s[36:39],s65,3072
M a[100:103],a[172:175],a[132:135],0
s_add_u32 s65,s65,128
T a[128:129],v21,4096
M a[8:11],a[160:163],a[136:139],0
s_add_u32 m0,s68,32768
T a[130:131],v21,4608
M a[40:43],a[164:167],a[136:139],0
WL 5
T a[132:133],v22,4096
M a[72:75],a[168:171],a[136:139],0
T a[134:135],v22,4608
v_mul_f32 v128,s71,v128
M a[104:107],a[172:175],a[136:139],0
T a[136:137],v23,4096
v_mul_f32 v129,s71,v129
M a[12:15],a[160:163],a[140:143],0
G v9,s[84:87],s66,0
M a[44:47],a[164:167],a[140:143],0
T a[138:139],v23,4608
v_mul_f32 v130,s71,v130
M a[76:79],a[168:171],a[140:143],0
G v10,s[84:87],s66,1024
M a[108:111],a[172:175],a[140:143],0
T a[140:141],v24,4096
v_mul_f32 v131,s71,v131
M a[16:19],a[160:163],a[144:147],0
T a[142:143],v24,4608
v_mul_f32 v132,s71,v132
M a[48:51],a[164:167],a[144:147],0
G v11,s[84:87],s66,2048
M a[80:83],a[168:171],a[144:147],0
G v12,s[84:87],s66,3072
M a[112:115],a[172:175],a[144:147],0
T a[144:145],v21,12288
v_mul_f32 v133,s71,v133
M a[20:23],a[160:163],a[148:151],0
s_add_u32 s66,s66,524288
T a[146:147],v21,12800
v_mul_f32 v134,s71,v134
M a[52:55],a[164:167],a[148:151],0
v_mul_f32 v135,s71,v135
M a[84:87],a[168:171],a[148:151],0
C v40,v128,v129
M a[116:119],a[172:175],a[148:151],0
T a[148:149],v22,12288
C v41,v130,v131
M a[24:27],a[160:163],a[152:155],0
T a[150:151],v22,12800
v_add_u32 v17,32768,v17
M a[56:59],a[164:167],a[152:155],0
v_add_u32 v21,81920,v21
M a[88:91],a[168:171],a[152:155],0
v_add_u32 v22,81920,v22
M a[120:123],a[172:175],a[152:155],0
T a[152:153],v23,12288
C v42,v132,v133
M a[28:31],a[160:163],a[156:159],0
T a[154:155],v23,12800
v_add_u32 v23,81920,v23
M a[60:63],a[164:167],a[156:159],0
WL 4
C v43,v134,v135
M a[92:95],a[168:171],a[156:159],0
v_mul_f32 v136,s71,v136
M a[124:127],a[172:175],a[156:159],0
T a[156:157],v24,12288
v_mul_f32 v137,s71,v137
M a[0:3],a[176:179],a[128:131],a[0:3]
T a[158:159],v24,12800
v_add_u32 v24,81920,v24
M a[32:35],a[180:183],a[128:131],a[32:35]
buffer_store_dwordx4 v[40:43],v0,s[60:63],s70 offen offset:0 sc1
M a[64:67],a[184:187],a[128:131],a[64:67]
v_mul_f32 v138,s71,v138
M a[96:99],a[188:191],a[128:131],a[96:99]
v_mul_f32 v139,s71,v139
M a[4:7],a[176:179],a[132:135],a[4:7]
M a[36:39],a[180:183],a[132:135],a[36:39]
v_mul_f32 v140,s71,v140
M a[68:71],a[184:187],a[132:135],a[68:71]
s_waitcnt vmcnt(13) lgkmcnt(0)
s_barrier
M a[100:103],a[188:191],a[132:135],a[100:103]
s_add_u32 m0,s67,65536
D a[160:163],v17,0
v_mul_f32 v141,s71,v141
M a[8:11],a[176:179],a[136:139],a[8:11]
D a[164:167],v17,2048
M a[40:43],a[180:183],a[136:139],a[40:43]
D a[168:171],v17,4096
M a[72:75],a[184:187],a[136:139],a[72:75]
G v1,s[36:39],s64,0
M a[104:107],a[188:191],a[136:139],a[104:107]
D a[172:175],v17,6144
v_mul_f32 v142,s71,v142
M a[12:15],a[176:179],a[140:143],a[12:15]
T a[128:129],v21,0
v_mul_f32 v143,s71,v143
M a[44:47],a[180:183],a[140:143],a[44:47]
G v2,s[36:39],s64,1024
M a[76:79],a[184:187],a[140:143],a[76:79]
T a[130:131],v21,512
C v44,v136,v137
M a[108:111],a[188:191],a[140:143],a[108:111]
T a[132:133],v22,0
M a[16:19],a[176:179],a[144:147],a[16:19]
T a[134:135],v22,512
C v45,v138,v139
M a[48:51],a[180:183],a[144:147],a[48:51]
G v3,s[36:39],s64,2048
M a[80:83],a[184:187],a[144:147],a[80:83]
T a[136:137],v23,0
M a[112:115],a[188:191],a[144:147],a[112:115]
T a[138:139],v23,512
C v46,v140,v141
M a[20:23],a[176:179],a[148:151],a[20:23]
G v4,s[36:39],s64,3072
M a[52:55],a[180:183],a[148:151],a[52:55]
s_add_u32 s64,s64,128
T a[140:141],v24,0
C v47,v142,v143
M a[84:87],a[184:187],a[148:151],a[84:87]
s_add_u32 m0,s67,69632
T a[142:143],v24,512
M a[116:119],a[188:191],a[148:151],a[116:119]
T a[144:145],v21,8192
M a[24:27],a[176:179],a[152:155],a[24:27]
T a[146:147],v21,8704
v_add_u32 v18,32768,v18
M a[56:59],a[180:183],a[152:155],a[56:59]
T a[148:149],v22,8192
v_mul_f32 v144,s71,v144
M a[88:91],a[184:187],a[152:155],a[88:91]
T a[150:151],v22,8704
M a[120:123],a[188:191],a[152:155],a[120:123]
T a[152:153],v23,8192
M a[28:31],a[176:179],a[156:159],a[28:31]
G v1,s[36:39],s65,0
M a[60:63],a[180:183],a[156:159],a[60:63]
WL 6
T a[154:155],v23,8704
v_mul_f32 v145,s71,v145
M a[92:95],a[184:187],a[156:159],a[92:95]
G v2,s[36:39],s65,1024
M a[124:127],a[188:191],a[156:159],a[124:127]
T a[156:157],v24,8192
v_mul_f32 v146,s71,v146
M a[0:3],a[160:163],a[128:131],a[0:3]
T a[158:159],v24,8704
v_mul_f32 v147,s71,v147
M a[32:35],a[164:167],a[128:131],a[32:35]
D a[176:179],v18,0
M a[64:67],a[168:171],a[128:131],a[64:67]
D a[180:183],v18,2048
M a[96:99],a[172:175],a[128:131],a[96:99]
G v3,s[36:39],s65,2048
M a[4:7],a[160:163],a[132:135],a[4:7]
D a[184:187],v18,4096
v_mul_f32 v148,s71,v148
M a[36:39],a[164:167],a[132:135],a[36:39]
D a[188:191],v18,6144
v_mul_f32 v149,s71,v149
M a[68:71],a[168:171],a[132:135],a[68:71]
G v4,s[36:39],s65,3072
M a[100:103],a[172:175],a[132:135],a[100:103]
s_add_u32 s65,s65,128
buffer_store_dwordx4 v[44:47],v0,s[60:63],s70 offen offset:1024 sc1
M a[8:11],a[160:163],a[136:139],a[8:11]
s_add_u32 m0,s68,49152
T a[128:129],v21,4096
v_mul_f32 v150,s71,v150
M a[40:43],a[164:167],a[136:139],a[40:43]
WL 4
T a[130:131],v21,4608
v_mul_f32 v151,s71,v151
M a[72:75],a[168:171],a[136:139],a[72:75]
T a[132:133],v22,4096
M a[104:107],a[172:175],a[136:139],a[104:107]
T a[134:135],v22,4608
M a[12:15],a[160:163],a[140:143],a[12:15]
T a[136:137],v23,4096
C v64,v144,v145
M a[44:47],a[164:167],a[140:143],a[44:47]
G v9,s[84:87],s66,0
M a[76:79],a[168:171],a[140:143],a[76:79]
T a[138:139],v23,4608
C v65,v146,v147
M a[108:111],a[172:175],a[140:143],a[108:111]
T a[140:141],v24,4096
M a[16:19],a[160:163],a[144:147],a[16:19]
G v10,s[84:87],s66,1024
M a[48:51],a[164:167],a[144:147],a[48:51]
T a[142:143],v24,4608
C v66,v148,v149
M a[80:83],a[168:171],a[144:147],a[80:83]
G v11,s[84:87],s66,2048
M a[112:115],a[172:175],a[144:147],a[112:115]
T a[144:145],v21,12288
C v67,v150,v151
M a[20:23],a[160:163],a[148:151],a[20:23]
T a[146:147],v21,12800
M a[52:55],a[164:167],a[148:151],a[52:55]
G v12,s[84:87],s66,3072
M a[84:87],a[168:171],a[148:151],a[84:87]
s_add_u32 s66,s66,524288
M a[116:119],a[172:175],a[148:151],a[116:119]
T a[148:149],v22,12288
v_mul_f32 v152,s71,v152
M a[24:27],a[160:163],a[152:155],a[24:27]
T a[150:151],v22,12800
v_add_u32 v17,4294868992,v17
M a[56:59],a[164:167],a[152:155],a[56:59]
v_add_u32 v21,4294868992,v21
M a[88:91],a[168:171],a[152:155],a[88:91]
v_add_u32 v22,4294868992,v22
M a[120:123],a[172:175],a[152:155],a[120:123]
T a[152:153],v23,12288
v_mul_f32 v153,s71,v153
M a[28:31],a[160:163],a[156:159],a[28:31]
T a[154:155],v23,12800
v_add_u32 v23,4294868992,v23
M a[60:63],a[164:167],a[156:159],a[60:63]
WL 4
buffer_store_dwordx4 v[64:67],v0,s[60:63],s70 offen offset:2048 sc1
M a[92:95],a[168:171],a[156:159],a[92:95]
v_mul_f32 v154,s71,v154
M a[124:127],a[172:175],a[156:159],a[124:127]
T a[156:157],v24,12288
v_mul_f32 v155,s71,v155
M a[0:3],a[176:179],a[128:131],a[0:3]
T a[158:159],v24,12800
v_add_u32 v24,4294868992,v24
M a[32:35],a[180:183],a[128:131],a[32:35]
v_mul_f32 v156,s71,v156
M a[64:67],a[184:187],a[128:131],a[64:67]
v_mul_f32 v157,s71,v157
M a[96:99],a[188:191],a[128:131],a[96:99]
M a[4:7],a[176:179],a[132:135],a[4:7]
M a[36:39],a[180:183],a[132:135],a[36:39]
v_mul_f32 v158,s71,v158
M a[68:71],a[184:187],a[132:135],a[68:71]
s_waitcnt vmcnt(15) lgkmcnt(0)
s_barrier
M a[100:103],a[188:191],a[132:135],a[100:103]
s_add_u32 m0,s67,98304
D a[160:163],v17,0
v_mul_f32 v159,s71,v159
M a[8:11],a[176:179],a[136:139],a[8:11]
D a[164:167],v17,2048
M a[40:43],a[180:183],a[136:139],a[40:43]
D a[168:171],v17,4096
C v68,v152,v153
M a[72:75],a[184:187],a[136:139],a[72:75]
G v1,s[36:39],s64,0
M a[104:107],a[188:191],a[136:139],a[104:107]
D a[172:175],v17,6144
C v69,v154,v155
M a[12:15],a[176:179],a[140:143],a[12:15]
T a[128:129],v21,0
M a[44:47],a[180:183],a[140:143],a[44:47]
G v2,s[36:39],s64,1024
M a[76:79],a[184:187],a[140:143],a[76:79]
T a[130:131],v21,512
C v70,v156,v157
M a[108:111],a[188:191],a[140:143],a[108:111]
T a[132:133],v22,0
M a[16:19],a[176:179],a[144:147],a[16:19]
T a[134:135],v22,512
C v71,v158,v159
M a[48:51],a[180:183],a[144:147],a[48:51]
G v3,s[36:39],s64,2048
M a[80:83],a[184:187],a[144:147],a[80:83]
T a[136:137],v23,0
M a[112:115],a[188:191],a[144:147],a[112:115]
T a[138:139],v23,512
M a[20:23],a[176:179],a[148:151],a[20:23]
G v4,s[36:39],s64,3072
M a[52:55],a[180:183],a[148:151],a[52:55]
s_add_u32 s64,s64,128
T a[140:141],v24,0
v_mul_f32 v160,s71,v160
M a[84:87],a[184:187],a[148:151],a[84:87]
s_add_u32 m0,s67,102400
T a[142:143],v24,512
M a[116:119],a[188:191],a[148:151],a[116:119]
T a[144:145],v21,8192
v_mul_f32 v161,s71,v161
M a[24:27],a[176:179],a[152:155],a[24:27]
T a[146:147],v21,8704
v_add_u32 v18,4294868992,v18
M a[56:59],a[180:183],a[152:155],a[56:59]
T a[148:149],v22,8192
v_mul_f32 v162,s71,v162
M a[88:91],a[184:187],a[152:155],a[88:91]
T a[150:151],v22,8704
M a[120:123],a[188:191],a[152:155],a[120:123]
T a[152:153],v23,8192
v_mul_f32 v163,s71,v163
M a[28:31],a[176:179],a[156:159],a[28:31]
G v1,s[36:39],s65,0
M a[60:63],a[180:183],a[156:159],a[60:63]
WL 6
T a[154:155],v23,8704
M a[92:95],a[184:187],a[156:159],a[92:95]
G v2,s[36:39],s65,1024
M a[124:127],a[188:191],a[156:159],a[124:127]
T a[156:157],v24,8192
v_mul_f32 v164,s71,v164
M a[0:3],a[160:163],a[128:131],a[0:3]
T a[158:159],v24,8704
v_mul_f32 v165,s71,v165
M a[32:35],a[164:167],a[128:131],a[32:35]
buffer_store_dwordx4 v[68:71],v0,s[60:63],s70 offen offset:3072 sc1
M a[64:67],a[168:171],a[128:131],a[64:67]
s_add_u32 s70,s70,4096
D a[176:179],v18,0
v_mul_f32 v166,s71,v166
M a[96:99],a[172:175],a[128:131],a[96:99]
D a[180:183],v18,2048
M a[4:7],a[160:163],a[132:135],a[4:7]
G v3,s[36:39],s65,2048
M a[36:39],a[164:167],a[132:135],a[36:39]
D a[184:187],v18,4096
v_mul_f32 v167,s71,v167
M a[68:71],a[168:171],a[132:135],a[68:71]
D a[188:191],v18,6144
M a[100:103],a[172:175],a[132:135],a[100:103]
G v4,s[36:39],s65,3072
M a[8:11],a[160:163],a[136:139],a[8:11]
s_add_u32 s65,s65,128
T a[128:129],v21,4096
C v40,v160,v161
M a[40:43],a[164:167],a[136:139],a[40:43]
WL 5
T a[130:131],v21,4608
C v41,v162,v163
M a[72:75],a[168:171],a[136:139],a[72:75]
s_add_u32 m0,s68,131072
T a[132:133],v22,4096
M a[104:107],a[172:175],a[136:139],a[104:107]
T a[134:135],v22,4608
C v42,v164,v165
M a[12:15],a[160:163],a[140:143],a[12:15]
T a[136:137],v23,4096
M a[44:47],a[164:167],a[140:143],a[44:47]
G v9,s[84:87],s66,0
M a[76:79],a[168:171],a[140:143],a[76:79]
T a[138:139],v23,4608
C v43,v166,v167
M a[108:111],a[172:175],a[140:143],a[108:111]
T a[140:141],v24,4096
M a[16:19],a[160:163],a[144:147],a[16:19]
G v10,s[84:87],s66,1024
M a[48:51],a[164:167],a[144:147],a[48:51]
T a[142:143],v24,4608
M a[80:83],a[168:171],a[144:147],a[80:83]
G v11,s[84:87],s66,2048
M a[112:115],a[172:175],a[144:147],a[112:115]
)"
R"(T a[144:145],v21,12288
v_mul_f32 v168,s71,v168
M a[20:23],a[160:163],a[148:151],a[20:23]
T a[146:147],v21,12800
v_mul_f32 v169,s71,v169
M a[52:55],a[164:167],a[148:151],a[52:55]
G v12,s[84:87],s66,3072
M a[84:87],a[168:171],a[148:151],a[84:87]
s_add_u32 s66,s66,524288
buffer_store_dwordx4 v[40:43],v0,s[60:63],s70 offen offset:0 sc1
M a[116:119],a[172:175],a[148:151],a[116:119]
T a[148:149],v22,12288
v_mul_f32 v170,s71,v170
M a[24:27],a[160:163],a[152:155],a[24:27]
T a[150:151],v22,12800
v_add_u32 v17,65536,v17
M a[56:59],a[164:167],a[152:155],a[56:59]
v_add_u32 v21,16384,v21
M a[88:91],a[168:171],a[152:155],a[88:91]
v_add_u32 v22,16384,v22
M a[120:123],a[172:175],a[152:155],a[120:123]
T a[152:153],v23,12288
v_mul_f32 v171,s71,v171
M a[28:31],a[160:163],a[156:159],a[28:31]
T a[154:155],v23,12800
v_add_u32 v23,16384,v23
M a[60:63],a[164:167],a[156:159],a[60:63]
WL 4
v_mul_f32 v172,s71,v172
M a[92:95],a[168:171],a[156:159],a[92:95]
v_mul_f32 v173,s71,v173
M a[124:127],a[172:175],a[156:159],a[124:127]
T a[156:157],v24,12288
M a[0:3],a[176:179],a[128:131],a[0:3]
T a[158:159],v24,12800
v_add_u32 v24,16384,v24
M a[32:35],a[180:183],a[128:131],a[32:35]
v_mul_f32 v174,s71,v174
M a[64:67],a[184:187],a[128:131],a[64:67]
v_mul_f32 v175,s71,v175
M a[96:99],a[188:191],a[128:131],a[96:99]
M a[4:7],a[176:179],a[132:135],a[4:7]
C v44,v168,v169
M a[36:39],a[180:183],a[132:135],a[36:39]
M a[68:71],a[184:187],a[132:135],a[68:71]
s_waitcnt vmcnt(15) lgkmcnt(0)
s_barrier
M a[100:103],a[188:191],a[132:135],a[100:103]
s_add_u32 m0,s67,0
D a[160:163],v17,0
C v45,v170,v171
M a[8:11],a[176:179],a[136:139],a[8:11]
D a[164:167],v17,2048
M a[40:43],a[180:183],a[136:139],a[40:43]
D a[168:171],v17,4096
C v46,v172,v173
M a[72:75],a[184:187],a[136:139],a[72:75]
G v1,s[36:39],s64,0
M a[104:107],a[188:191],a[136:139],a[104:107]
D a[172:175],v17,6144
C v47,v174,v175
M a[12:15],a[176:179],a[140:143],a[12:15]
T a[128:129],v21,0
M a[44:47],a[180:183],a[140:143],a[44:47]
G v2,s[36:39],s64,1024
M a[76:79],a[184:187],a[140:143],a[76:79]
T a[130:131],v21,512
M a[108:111],a[188:191],a[140:143],a[108:111]
T a[132:133],v22,0
v_mul_f32 v176,s71,v176
M a[16:19],a[176:179],a[144:147],a[16:19]
T a[134:135],v22,512
M a[48:51],a[180:183],a[144:147],a[48:51]
G v3,s[36:39],s64,2048
M a[80:83],a[184:187],a[144:147],a[80:83]
T a[136:137],v23,0
v_mul_f32 v177,s71,v177
M a[112:115],a[188:191],a[144:147],a[112:115]
T a[138:139],v23,512
M a[20:23],a[176:179],a[148:151],a[20:23]
G v4,s[36:39],s64,3072
M a[52:55],a[180:183],a[148:151],a[52:55]
s_add_u32 s64,s64,128
T a[140:141],v24,0
v_mul_f32 v178,s71,v178
M a[84:87],a[184:187],a[148:151],a[84:87]
s_add_u32 m0,s67,4096
T a[142:143],v24,512
v_mul_f32 v179,s71,v179
M a[116:119],a[188:191],a[148:151],a[116:119]
T a[144:145],v21,8192
M a[24:27],a[176:179],a[152:155],a[24:27]
T a[146:147],v21,8704
v_add_u32 v18,65536,v18
M a[56:59],a[180:183],a[152:155],a[56:59]
T a[148:149],v22,8192
v_mul_f32 v180,s71,v180
M a[88:91],a[184:187],a[152:155],a[88:91]
T a[150:151],v22,8704
M a[120:123],a[188:191],a[152:155],a[120:123]
buffer_store_dwordx4 v[44:47],v0,s[60:63],s70 offen offset:1024 sc1
M a[28:31],a[176:179],a[156:159],a[28:31]
T a[152:153],v23,8192
v_mul_f32 v181,s71,v181
M a[60:63],a[180:183],a[156:159],a[60:63]
WL 6
G v1,s[36:39],s65,0
M a[92:95],a[184:187],a[156:159],a[92:95]
T a[154:155],v23,8704
v_mul_f32 v182,s71,v182
M a[124:127],a[188:191],a[156:159],a[124:127]
G v2,s[36:39],s65,1024
M a[0:3],a[160:163],a[128:131],a[0:3]
T a[156:157],v24,8192
v_mul_f32 v183,s71,v183
M a[32:35],a[164:167],a[128:131],a[32:35]
T a[158:159],v24,8704
M a[64:67],a[168:171],a[128:131],a[64:67]
D a[176:179],v18,0
C v64,v176,v177
M a[96:99],a[172:175],a[128:131],a[96:99]
D a[180:183],v18,2048
M a[4:7],a[160:163],a[132:135],a[4:7]
G v3,s[36:39],s65,2048
M a[36:39],a[164:167],a[132:135],a[36:39]
D a[184:187],v18,4096
C v65,v178,v179
M a[68:71],a[168:171],a[132:135],a[68:71]
D a[188:191],v18,6144
C v66,v180,v181
M a[100:103],a[172:175],a[132:135],a[100:103]
G v4,s[36:39],s65,3072
M a[8:11],a[160:163],a[136:139],a[8:11]
s_add_u32 s65,s65,128
T a[128:129],v21,4096
C v67,v182,v183
M a[40:43],a[164:167],a[136:139],a[40:43]
WL 5
T a[130:131],v21,4608
M a[72:75],a[168:171],a[136:139],a[72:75]
s_add_u32 m0,s68,32768
T a[132:133],v22,4096
M a[104:107],a[172:175],a[136:139],a[104:107]
T a[134:135],v22,4608
M a[12:15],a[160:163],a[140:143],a[12:15]
T a[136:137],v23,4096
M a[44:47],a[164:167],a[140:143],a[44:47]
G v9,s[84:87],s66,0
M a[76:79],a[168:171],a[140:143],a[76:79]
T a[138:139],v23,4608
v_mul_f32 v184,s71,v184
M a[108:111],a[172:175],a[140:143],a[108:111]
T a[140:141],v24,4096
v_mul_f32 v185,s71,v185
M a[16:19],a[160:163],a[144:147],a[16:19]
G v10,s[84:87],s66,1024
M a[48:51],a[164:167],a[144:147],a[48:51]
T a[142:143],v24,4608
M a[80:83],a[168:171],a[144:147],a[80:83]
G v11,s[84:87],s66,2048
M a[112:115],a[172:175],a[144:147],a[112:115]
T a[144:145],v21,12288
v_mul_f32 v186,s71,v186
M a[20:23],a[160:163],a[148:151],a[20:23]
T a[146:147],v21,12800
v_mul_f32 v187,s71,v187
M a[52:55],a[164:167],a[148:151],a[52:55]
G v12,s[84:87],s66,3072
M a[84:87],a[168:171],a[148:151],a[84:87]
s_add_u32 s66,s66,524288
buffer_store_dwordx4 v[64:67],v0,s[60:63],s70 offen offset:2048 sc1
M a[116:119],a[172:175],a[148:151],a[116:119]
T a[148:149],v22,12288
v_mul_f32 v188,s71,v188
M a[24:27],a[160:163],a[152:155],a[24:27]
T a[150:151],v22,12800
v_add_u32 v17,32768,v17
M a[56:59],a[164:167],a[152:155],a[56:59]
v_add_u32 v21,81920,v21
M a[88:91],a[168:171],a[152:155],a[88:91]
v_add_u32 v22,81920,v22
M a[120:123],a[172:175],a[152:155],a[120:123]
T a[152:153],v23,12288
v_mul_f32 v189,s71,v189
M a[28:31],a[160:163],a[156:159],a[28:31]
T a[154:155],v23,12800
v_add_u32 v23,81920,v23
M a[60:63],a[164:167],a[156:159],a[60:63]
WL 4
v_mul_f32 v190,s71,v190
M a[92:95],a[168:171],a[156:159],a[92:95]
v_mul_f32 v191,s71,v191
M a[124:127],a[172:175],a[156:159],a[124:127]
T a[156:157],v24,12288
C v68,v184,v185
M a[0:3],a[176:179],a[128:131],a[0:3]
T a[158:159],v24,12800
v_add_u32 v24,81920,v24
M a[32:35],a[180:183],a[128:131],a[32:35]
C v69,v186,v187
M a[64:67],a[184:187],a[128:131],a[64:67]
M a[96:99],a[188:191],a[128:131],a[96:99]
M a[4:7],a[176:179],a[132:135],a[4:7]
C v70,v188,v189
M a[36:39],a[180:183],a[132:135],a[36:39]
M a[68:71],a[184:187],a[132:135],a[68:71]
s_waitcnt vmcnt(15) lgkmcnt(0)
s_barrier
M a[100:103],a[188:191],a[132:135],a[100:103]
s_add_u32 m0,s67,65536
D a[160:163],v17,0
C v71,v190,v191
M a[8:11],a[176:179],a[136:139],a[8:11]
D a[164:167],v17,2048
M a[40:43],a[180:183],a[136:139],a[40:43]
D a[168:171],v17,4096
M a[72:75],a[184:187],a[136:139],a[72:75]
G v1,s[36:39],s64,0
M a[104:107],a[188:191],a[136:139],a[104:107]
D a[172:175],v17,6144
v_mul_f32 v192,s71,v192
M a[12:15],a[176:179],a[140:143],a[12:15]
T a[128:129],v21,0
M a[44:47],a[180:183],a[140:143],a[44:47]
G v2,s[36:39],s64,1024
M a[76:79],a[184:187],a[140:143],a[76:79]
T a[130:131],v21,512
v_mul_f32 v193,s71,v193
M a[108:111],a[188:191],a[140:143],a[108:111]
T a[132:133],v22,0
v_mul_f32 v194,s71,v194
M a[16:19],a[176:179],a[144:147],a[16:19]
T a[134:135],v22,512
M a[48:51],a[180:183],a[144:147],a[48:51]
G v3,s[36:39],s64,2048
M a[80:83],a[184:187],a[144:147],a[80:83]
T a[136:137],v23,0
v_mul_f32 v195,s71,v195
M a[112:115],a[188:191],a[144:147],a[112:115]
T a[138:139],v23,512
M a[20:23],a[176:179],a[148:151],a[20:23]
G v4,s[36:39],s64,3072
M a[52:55],a[180:183],a[148:151],a[52:55]
s_add_u32 s64,s64,128
T a[140:141],v24,0
v_mul_f32 v196,s71,v196
M a[84:87],a[184:187],a[148:151],a[84:87]
s_add_u32 m0,s67,69632
T a[142:143],v24,512
v_mul_f32 v197,s71,v197
M a[116:119],a[188:191],a[148:151],a[116:119]
buffer_store_dwordx4 v[68:71],v0,s[60:63],s70 offen offset:3072 sc1
M a[24:27],a[176:179],a[152:155],a[24:27]
s_add_u32 s70,s70,4096
T a[144:145],v21,8192
v_add_u32 v18,32768,v18
M a[56:59],a[180:183],a[152:155],a[56:59]
T a[146:147],v21,8704
v_mul_f32 v198,s71,v198
M a[88:91],a[184:187],a[152:155],a[88:91]
T a[148:149],v22,8192
v_mul_f32 v199,s71,v199
M a[120:123],a[188:191],a[152:155],a[120:123]
T a[150:151],v22,8704
M a[28:31],a[176:179],a[156:159],a[28:31]
T a[152:153],v23,8192
M a[60:63],a[180:183],a[156:159],a[60:63]
WL 6
G v1,s[36:39],s65,0
M a[92:95],a[184:187],a[156:159],a[92:95]
T a[154:155],v23,8704
C v40,v192,v193
M a[124:127],a[188:191],a[156:159],a[124:127]
G v2,s[36:39],s65,1024
M a[0:3],a[160:163],a[128:131],a[0:3]
T a[156:157],v24,8192
C v41,v194,v195
M a[32:35],a[164:167],a[128:131],a[32:35]
T a[158:159],v24,8704
C v42,v196,v197
M a[64:67],a[168:171],a[128:131],a[64:67]
D a[176:179],v18,0
M a[96:99],a[172:175],a[128:131],a[96:99]
D a[180:183],v18,2048
C v43,v198,v199
M a[4:7],a[160:163],a[132:135],a[4:7]
G v3,s[36:39],s65,2048
M a[36:39],a[164:167],a[132:135],a[36:39]
D a[184:187],v18,4096
M a[68:71],a[168:171],a[132:135],a[68:71]
D a[188:191],v18,6144
M a[100:103],a[172:175],a[132:135],a[100:103]
G v4,s[36:39],s65,3072
M a[8:11],a[160:163],a[136:139],a[8:11]
s_add_u32 s65,s65,128
T a[128:129],v21,4096
v_mul_f32 v200,s71,v200
M a[40:43],a[164:167],a[136:139],a[40:43]
WL 5
T a[130:131],v21,4608
M a[72:75],a[168:171],a[136:139],a[72:75]
s_add_u32 m0,s68,49152
T a[132:133],v22,4096
v_mul_f32 v201,s71,v201
M a[104:107],a[172:175],a[136:139],a[104:107]
T a[134:135],v22,4608
M a[12:15],a[160:163],a[140:143],a[12:15]
T a[136:137],v23,4096
M a[44:47],a[164:167],a[140:143],a[44:47]
G v9,s[84:87],s66,0
M a[76:79],a[168:171],a[140:143],a[76:79]
T a[138:139],v23,4608
v_mul_f32 v202,s71,v202
M a[108:111],a[172:175],a[140:143],a[108:111]
T a[140:141],v24,4096
v_mul_f32 v203,s71,v203
M a[16:19],a[160:163],a[144:147],a[16:19]
G v10,s[84:87],s66,1024
M a[48:51],a[164:167],a[144:147],a[48:51]
T a[142:143],v24,4608
v_mul_f32 v204,s71,v204
M a[80:83],a[168:171],a[144:147],a[80:83]
G v11,s[84:87],s66,2048
M a[112:115],a[172:175],a[144:147],a[112:115]
buffer_store_dwordx4 v[40:43],v0,s[60:63],s70 offen offset:0 sc1
M a[20:23],a[160:163],a[148:151],a[20:23]
T a[144:145],v21,12288
v_mul_f32 v205,s71,v205
M a[52:55],a[164:167],a[148:151],a[52:55]
T a[146:147],v21,12800
M a[84:87],a[168:171],a[148:151],a[84:87]
G v12,s[84:87],s66,3072
M a[116:119],a[172:175],a[148:151],a[116:119]
s_add_u32 s66,s66,524288
T a[148:149],v22,12288
v_mul_f32 v206,s71,v206
M a[24:27],a[160:163],a[152:155],a[24:27]
T a[150:151],v22,12800
v_add_u32 v17,4294868992,v17
M a[56:59],a[164:167],a[152:155],a[56:59]
v_add_u32 v21,4294868992,v21
M a[88:91],a[168:171],a[152:155],a[88:91]
v_add_u32 v22,4294868992,v22
M a[120:123],a[172:175],a[152:155],a[120:123]
T a[152:153],v23,12288
v_mul_f32 v207,s71,v207
M a[28:31],a[160:163],a[156:159],a[28:31]
T a[154:155],v23,12800
v_add_u32 v23,4294868992,v23
M a[60:63],a[164:167],a[156:159],a[60:63]
WL 4
C v44,v200,v201
M a[92:95],a[168:171],a[156:159],a[92:95]
C v45,v202,v203
M a[124:127],a[172:175],a[156:159],a[124:127]
T a[156:157],v24,12288
C v46,v204,v205
M a[0:3],a[176:179],a[128:131],a[0:3]
T a[158:159],v24,12800
v_add_u32 v24,4294868992,v24
M a[32:35],a[180:183],a[128:131],a[32:35]
C v47,v206,v207
M a[64:67],a[184:187],a[128:131],a[64:67]
M a[96:99],a[188:191],a[128:131],a[96:99]
M a[4:7],a[176:179],a[132:135],a[4:7]
M a[36:39],a[180:183],a[132:135],a[36:39]
v_mul_f32 v208,s71,v208
M a[68:71],a[184:187],a[132:135],a[68:71]
s_waitcnt vmcnt(15) lgkmcnt(0)
s_barrier
M a[100:103],a[188:191],a[132:135],a[100:103]
s_add_u32 m0,s67,98304
D a[160:163],v17,0
M a[8:11],a[176:179],a[136:139],a[8:11]
D a[164:167],v17,2048
v_mul_f32 v209,s71,v209
M a[40:43],a[180:183],a[136:139],a[40:43]
D a[168:171],v17,4096
M a[72:75],a[184:187],a[136:139],a[72:75]
G v1,s[36:39],s64,0
M a[104:107],a[188:191],a[136:139],a[104:107]
D a[172:175],v17,6144
v_mul_f32 v210,s71,v210
M a[12:15],a[176:179],a[140:143],a[12:15]
T a[128:129],v21,0
v_mul_f32 v211,s71,v211
M a[44:47],a[180:183],a[140:143],a[44:47]
G v2,s[36:39],s64,1024
M a[76:79],a[184:187],a[140:143],a[76:79]
T a[130:131],v21,512
M a[108:111],a[188:191],a[140:143],a[108:111]
T a[132:133],v22,0
v_mul_f32 v212,s71,v212
M a[16:19],a[176:179],a[144:147],a[16:19]
T a[134:135],v22,512
M a[48:51],a[180:183],a[144:147],a[48:51]
G v3,s[36:39],s64,2048
M a[80:83],a[184:187],a[144:147],a[80:83]
buffer_store_dwordx4 v[44:47],v0,s[60:63],s70 offen offset:1024 sc1
M a[112:115],a[188:191],a[144:147],a[112:115]
)"
R"(T a[136:137],v23,0
v_mul_f32 v213,s71,v213
M a[20:23],a[176:179],a[148:151],a[20:23]
T a[138:139],v23,512
v_mul_f32 v214,s71,v214
M a[52:55],a[180:183],a[148:151],a[52:55]
G v4,s[36:39],s64,3072
M a[84:87],a[184:187],a[148:151],a[84:87]
s_add_u32 s64,s64,128
T a[140:141],v24,0
v_mul_f32 v215,s71,v215
M a[116:119],a[188:191],a[148:151],a[116:119]
s_add_u32 m0,s67,102400
T a[142:143],v24,512
M a[24:27],a[176:179],a[152:155],a[24:27]
T a[144:145],v21,8192
v_add_u32 v18,4294868992,v18
M a[56:59],a[180:183],a[152:155],a[56:59]
T a[146:147],v21,8704
C v64,v208,v209
M a[88:91],a[184:187],a[152:155],a[88:91]
T a[148:149],v22,8192
C v65,v210,v211
M a[120:123],a[188:191],a[152:155],a[120:123]
T a[150:151],v22,8704
M a[28:31],a[176:179],a[156:159],a[28:31]
T a[152:153],v23,8192
C v66,v212,v213
M a[60:63],a[180:183],a[156:159],a[60:63]
WL 7
G v1,s[36:39],s65,0
M a[92:95],a[184:187],a[156:159],a[92:95]
T a[154:155],v23,8704
C v67,v214,v215
M a[124:127],a[188:191],a[156:159],a[124:127]
G v2,s[36:39],s65,1024
M a[0:3],a[160:163],a[128:131],a[0:3]
T a[156:157],v24,8192
M a[32:35],a[164:167],a[128:131],a[32:35]
T a[158:159],v24,8704
M a[64:67],a[168:171],a[128:131],a[64:67]
D a[176:179],v18,0
M a[96:99],a[172:175],a[128:131],a[96:99]
D a[180:183],v18,2048
v_mul_f32 v216,s71,v216
M a[4:7],a[160:163],a[132:135],a[4:7]
G v3,s[36:39],s65,2048
M a[36:39],a[164:167],a[132:135],a[36:39]
D a[184:187],v18,4096
v_mul_f32 v217,s71,v217
M a[68:71],a[168:171],a[132:135],a[68:71]
D a[188:191],v18,6144
M a[100:103],a[172:175],a[132:135],a[100:103]
G v4,s[36:39],s65,3072
M a[8:11],a[160:163],a[136:139],a[8:11]
s_add_u32 s65,s65,128
T a[128:129],v21,4096
v_mul_f32 v218,s71,v218
M a[40:43],a[164:167],a[136:139],a[40:43]
WL 5
T a[130:131],v21,4608
M a[72:75],a[168:171],a[136:139],a[72:75]
s_add_u32 m0,s68,131072
T a[132:133],v22,4096
v_mul_f32 v219,s71,v219
M a[104:107],a[172:175],a[136:139],a[104:107]
T a[134:135],v22,4608
M a[12:15],a[160:163],a[140:143],a[12:15]
T a[136:137],v23,4096
v_mul_f32 v220,s71,v220
M a[44:47],a[164:167],a[140:143],a[44:47]
G v9,s[84:87],s66,0
M a[76:79],a[168:171],a[140:143],a[76:79]
T a[138:139],v23,4608
v_mul_f32 v221,s71,v221
M a[108:111],a[172:175],a[140:143],a[108:111]
buffer_store_dwordx4 v[64:67],v0,s[60:63],s70 offen offset:2048 sc1
M a[16:19],a[160:163],a[144:147],a[16:19]
T a[140:141],v24,4096
M a[48:51],a[164:167],a[144:147],a[48:51]
G v10,s[84:87],s66,1024
M a[80:83],a[168:171],a[144:147],a[80:83]
T a[142:143],v24,4608
v_mul_f32 v222,s71,v222
M a[112:115],a[172:175],a[144:147],a[112:115]
G v11,s[84:87],s66,2048
M a[20:23],a[160:163],a[148:151],a[20:23]
T a[144:145],v21,12288
v_mul_f32 v223,s71,v223
M a[52:55],a[164:167],a[148:151],a[52:55]
T a[146:147],v21,12800
C v68,v216,v217
M a[84:87],a[168:171],a[148:151],a[84:87]
G v12,s[84:87],s66,3072
M a[116:119],a[172:175],a[148:151],a[116:119]
s_add_u32 s66,s66,524288
T a[148:149],v22,12288
C v69,v218,v219
M a[24:27],a[160:163],a[152:155],a[24:27]
T a[150:151],v22,12800
v_add_u32 v17,65536,v17
M a[56:59],a[164:167],a[152:155],a[56:59]
v_add_u32 v21,16384,v21
M a[88:91],a[168:171],a[152:155],a[88:91]
v_add_u32 v22,16384,v22
M a[120:123],a[172:175],a[152:155],a[120:123]
T a[152:153],v23,12288
C v70,v220,v221
M a[28:31],a[160:163],a[156:159],a[28:31]
T a[154:155],v23,12800
v_add_u32 v23,16384,v23
M a[60:63],a[164:167],a[156:159],a[60:63]
WL 4
C v71,v222,v223
M a[92:95],a[168:171],a[156:159],a[92:95]
M a[124:127],a[172:175],a[156:159],a[124:127]
T a[156:157],v24,12288
M a[0:3],a[176:179],a[128:131],a[0:3]
T a[158:159],v24,12800
v_add_u32 v24,16384,v24
M a[32:35],a[180:183],a[128:131],a[32:35]
v_mul_f32 v224,s71,v224
M a[64:67],a[184:187],a[128:131],a[64:67]
M a[96:99],a[188:191],a[128:131],a[96:99]
v_mul_f32 v225,s71,v225
M a[4:7],a[176:179],a[132:135],a[4:7]
buffer_store_dwordx4 v[68:71],v0,s[60:63],s70 offen offset:3072 sc1
M a[36:39],a[180:183],a[132:135],a[36:39]
v_mul_f32 v226,s71,v226
M a[68:71],a[184:187],a[132:135],a[68:71]
s_add_u32 s70,s70,4096
s_waitcnt vmcnt(15) lgkmcnt(0)
s_barrier
M a[100:103],a[188:191],a[132:135],a[100:103]
s_add_u32 m0,s67,0
D a[160:163],v17,0
v_mul_f32 v227,s71,v227
M a[8:11],a[176:179],a[136:139],a[8:11]
D a[164:167],v17,2048
M a[40:43],a[180:183],a[136:139],a[40:43]
D a[168:171],v17,4096
M a[72:75],a[184:187],a[136:139],a[72:75]
G v5,s[44:47],s72,0
M a[104:107],a[188:191],a[136:139],a[104:107]
D a[172:175],v17,6144
v_mul_f32 v228,s71,v228
M a[12:15],a[176:179],a[140:143],a[12:15]
T a[128:129],v21,0
v_mul_f32 v229,s71,v229
M a[44:47],a[180:183],a[140:143],a[44:47]
G v6,s[44:47],s72,1024
M a[76:79],a[184:187],a[140:143],a[76:79]
T a[130:131],v21,512
v_mul_f32 v230,s71,v230
M a[108:111],a[188:191],a[140:143],a[108:111]
T a[132:133],v22,0
M a[16:19],a[176:179],a[144:147],a[16:19]
T a[134:135],v22,512
v_mul_f32 v231,s71,v231
M a[48:51],a[180:183],a[144:147],a[48:51]
G v7,s[44:47],s72,2048
M a[80:83],a[184:187],a[144:147],a[80:83]
T a[136:137],v23,0
M a[112:115],a[188:191],a[144:147],a[112:115]
T a[138:139],v23,512
C v40,v224,v225
M a[20:23],a[176:179],a[148:151],a[20:23]
G v8,s[44:47],s72,3072
M a[52:55],a[180:183],a[148:151],a[52:55]
s_add_u32 s72,s72,128
T a[140:141],v24,0
C v41,v226,v227
M a[84:87],a[184:187],a[148:151],a[84:87]
s_add_u32 m0,s67,4096
T a[142:143],v24,512
M a[116:119],a[188:191],a[148:151],a[116:119]
T a[144:145],v21,8192
C v42,v228,v229
M a[24:27],a[176:179],a[152:155],a[24:27]
T a[146:147],v21,8704
v_add_u32 v18,65536,v18
M a[56:59],a[180:183],a[152:155],a[56:59]
T a[148:149],v22,8192
C v43,v230,v231
M a[88:91],a[184:187],a[152:155],a[88:91]
T a[150:151],v22,8704
M a[120:123],a[188:191],a[152:155],a[120:123]
T a[152:153],v23,8192
M a[28:31],a[176:179],a[156:159],a[28:31]
G v5,s[44:47],s73,0
M a[60:63],a[180:183],a[156:159],a[60:63]
WL 6
T a[154:155],v23,8704
M a[92:95],a[184:187],a[156:159],a[92:95]
G v6,s[44:47],s73,1024
M a[124:127],a[188:191],a[156:159],a[124:127]
T a[156:157],v24,8192
v_mul_f32 v232,s71,v232
M a[0:3],a[160:163],a[128:131],a[0:3]
T a[158:159],v24,8704
v_mul_f32 v233,s71,v233
M a[32:35],a[164:167],a[128:131],a[32:35]
D a[176:179],v18,0
M a[64:67],a[168:171],a[128:131],a[64:67]
D a[180:183],v18,2048
M a[96:99],a[172:175],a[128:131],a[96:99]
G v7,s[44:47],s73,2048
M a[4:7],a[160:163],a[132:135],a[4:7]
D a[184:187],v18,4096
v_mul_f32 v234,s71,v234
M a[36:39],a[164:167],a[132:135],a[36:39]
D a[188:191],v18,6144
v_mul_f32 v235,s71,v235
M a[68:71],a[168:171],a[132:135],a[68:71]
G v8,s[44:47],s73,3072
M a[100:103],a[172:175],a[132:135],a[100:103]
s_add_u32 s73,s73,128
T a[128:129],v21,4096
v_mul_f32 v236,s71,v236
M a[8:11],a[160:163],a[136:139],a[8:11]
s_add_u32 m0,s68,32768
T a[130:131],v21,4608
M a[40:43],a[164:167],a[136:139],a[40:43]
WL 5
buffer_store_dwordx4 v[40:43],v0,s[60:63],s70 offen offset:0 sc1
M a[72:75],a[168:171],a[136:139],a[72:75]
T a[132:133],v22,4096
v_mul_f32 v237,s71,v237
M a[104:107],a[172:175],a[136:139],a[104:107]
T a[134:135],v22,4608
M a[12:15],a[160:163],a[140:143],a[12:15]
T a[136:137],v23,4096
v_mul_f32 v238,s71,v238
M a[44:47],a[164:167],a[140:143],a[44:47]
G v9,s[48:51],s74,0
M a[76:79],a[168:171],a[140:143],a[76:79]
T a[138:139],v23,4608
v_mul_f32 v239,s71,v239
M a[108:111],a[172:175],a[140:143],a[108:111]
T a[140:141],v24,4096
M a[16:19],a[160:163],a[144:147],a[16:19]
G v10,s[48:51],s74,1024
M a[48:51],a[164:167],a[144:147],a[48:51]
T a[142:143],v24,4608
C v44,v232,v233
M a[80:83],a[168:171],a[144:147],a[80:83]
G v11,s[48:51],s74,2048
M a[112:115],a[172:175],a[144:147],a[112:115]
T a[144:145],v21,12288
C v45,v234,v235
M a[20:23],a[160:163],a[148:151],a[20:23]
T a[146:147],v21,12800
M a[52:55],a[164:167],a[148:151],a[52:55]
G v12,s[48:51],s74,3072
M a[84:87],a[168:171],a[148:151],a[84:87]
s_add_u32 s74,s74,524288
C v46,v236,v237
M a[116:119],a[172:175],a[148:151],a[116:119]
T a[148:149],v22,12288
C v47,v238,v239
M a[24:27],a[160:163],a[152:155],a[24:27]
T a[150:151],v22,12800
v_add_u32 v17,32768,v17
M a[56:59],a[164:167],a[152:155],a[56:59]
v_add_u32 v21,81920,v21
M a[88:91],a[168:171],a[152:155],a[88:91]
v_add_u32 v22,81920,v22
M a[120:123],a[172:175],a[152:155],a[120:123]
T a[152:153],v23,12288
v_mul_f32 v240,s71,v240
M a[28:31],a[160:163],a[156:159],a[28:31]
T a[154:155],v23,12800
v_add_u32 v23,81920,v23
M a[60:63],a[164:167],a[156:159],a[60:63]
WL 4
M a[92:95],a[168:171],a[156:159],a[92:95]
v_mul_f32 v241,s71,v241
M a[124:127],a[172:175],a[156:159],a[124:127]
T a[156:157],v24,12288
M a[0:3],a[176:179],a[128:131],a[0:3]
T a[158:159],v24,12800
v_add_u32 v24,81920,v24
M a[32:35],a[180:183],a[128:131],a[32:35]
buffer_store_dwordx4 v[44:47],v0,s[60:63],s70 offen offset:1024 sc1
M a[64:67],a[184:187],a[128:131],a[64:67]
v_mul_f32 v242,s71,v242
M a[96:99],a[188:191],a[128:131],a[96:99]
v_mul_f32 v243,s71,v243
M a[4:7],a[176:179],a[132:135],a[4:7]
M a[36:39],a[180:183],a[132:135],a[36:39]
v_mul_f32 v244,s71,v244
M a[68:71],a[184:187],a[132:135],a[68:71]
s_waitcnt vmcnt(15) lgkmcnt(0)
s_barrier
M a[100:103],a[188:191],a[132:135],a[100:103]
s_add_u32 m0,s67,65536
D a[160:163],v17,0
v_mul_f32 v245,s71,v245
M a[8:11],a[176:179],a[136:139],a[8:11]
D a[164:167],v17,2048
M a[40:43],a[180:183],a[136:139],a[40:43]
D a[168:171],v17,4096
v_mul_f32 v246,s71,v246
M a[72:75],a[184:187],a[136:139],a[72:75]
G v5,s[44:47],s72,0
M a[104:107],a[188:191],a[136:139],a[104:107]
D a[172:175],v17,6144
v_mul_f32 v247,s71,v247
M a[12:15],a[176:179],a[140:143],a[12:15]
T a[128:129],v21,0
M a[44:47],a[180:183],a[140:143],a[44:47]
G v6,s[44:47],s72,1024
M a[76:79],a[184:187],a[140:143],a[76:79]
T a[130:131],v21,512
C v64,v240,v241
M a[108:111],a[188:191],a[140:143],a[108:111]
T a[132:133],v22,0
M a[16:19],a[176:179],a[144:147],a[16:19]
T a[134:135],v22,512
C v65,v242,v243
M a[48:51],a[180:183],a[144:147],a[48:51]
G v7,s[44:47],s72,2048
M a[80:83],a[184:187],a[144:147],a[80:83]
T a[136:137],v23,0
C v66,v244,v245
M a[112:115],a[188:191],a[144:147],a[112:115]
T a[138:139],v23,512
M a[20:23],a[176:179],a[148:151],a[20:23]
G v8,s[44:47],s72,3072
M a[52:55],a[180:183],a[148:151],a[52:55]
s_add_u32 s72,s72,128
T a[140:141],v24,0
C v67,v246,v247
M a[84:87],a[184:187],a[148:151],a[84:87]
s_add_u32 m0,s67,69632
T a[142:143],v24,512
M a[116:119],a[188:191],a[148:151],a[116:119]
T a[144:145],v21,8192
M a[24:27],a[176:179],a[152:155],a[24:27]
T a[146:147],v21,8704
v_add_u32 v18,32768,v18
M a[56:59],a[180:183],a[152:155],a[56:59]
T a[148:149],v22,8192
v_mul_f32 v248,s71,v248
M a[88:91],a[184:187],a[152:155],a[88:91]
T a[150:151],v22,8704
M a[120:123],a[188:191],a[152:155],a[120:123]
T a[152:153],v23,8192
v_mul_f32 v249,s71,v249
M a[28:31],a[176:179],a[156:159],a[28:31]
G v5,s[44:47],s73,0
M a[60:63],a[180:183],a[156:159],a[60:63]
WL 6
T a[154:155],v23,8704
M a[92:95],a[184:187],a[156:159],a[92:95]
G v6,s[44:47],s73,1024
M a[124:127],a[188:191],a[156:159],a[124:127]
T a[156:157],v24,8192
v_mul_f32 v250,s71,v250
M a[0:3],a[160:163],a[128:131],a[0:3]
T a[158:159],v24,8704
v_mul_f32 v251,s71,v251
M a[32:35],a[164:167],a[128:131],a[32:35]
D a[176:179],v18,0
M a[64:67],a[168:171],a[128:131],a[64:67]
D a[180:183],v18,2048
v_mul_f32 v252,s71,v252
M a[96:99],a[172:175],a[128:131],a[96:99]
G v7,s[44:47],s73,2048
M a[4:7],a[160:163],a[132:135],a[4:7]
D a[184:187],v18,4096
v_mul_f32 v253,s71,v253
M a[36:39],a[164:167],a[132:135],a[36:39]
buffer_store_dwordx4 v[64:67],v0,s[60:63],s70 offen offset:2048 sc1
M a[68:71],a[168:171],a[132:135],a[68:71]
D a[188:191],v18,6144
M a[100:103],a[172:175],a[132:135],a[100:103]
G v8,s[44:47],s73,3072
M a[8:11],a[160:163],a[136:139],a[8:11]
s_add_u32 s73,s73,128
T a[128:129],v21,4096
v_mul_f32 v254,s71,v254
M a[40:43],a[164:167],a[136:139],a[40:43]
WL 4
T a[130:131],v21,4608
v_mul_f32 v255,s71,v255
M a[72:75],a[168:171],a[136:139],a[72:75]
s_add_u32 m0,s68,49152
T a[132:133],v22,4096
M a[104:107],a[172:175],a[136:139],a[104:107]
T a[134:135],v22,4608
C v68,v248,v249
M a[12:15],a[160:163],a[140:143],a[12:15]
T a[136:137],v23,4096
M a[44:47],a[164:167],a[140:143],a[44:47]
G v9,s[48:51],s74,0
M a[76:79],a[168:171],a[140:143],a[76:79]
T a[138:139],v23,4608
C v69,v250,v251
M a[108:111],a[172:175],a[140:143],a[108:111]
)"
R"(T a[140:141],v24,4096
M a[16:19],a[160:163],a[144:147],a[16:19]
G v10,s[48:51],s74,1024
M a[48:51],a[164:167],a[144:147],a[48:51]
T a[142:143],v24,4608
C v70,v252,v253
M a[80:83],a[168:171],a[144:147],a[80:83]
G v11,s[48:51],s74,2048
M a[112:115],a[172:175],a[144:147],a[112:115]
T a[144:145],v21,12288
C v71,v254,v255
M a[20:23],a[160:163],a[148:151],a[20:23]
T a[146:147],v21,12800
M a[52:55],a[164:167],a[148:151],a[52:55]
G v12,s[48:51],s74,3072
M a[84:87],a[168:171],a[148:151],a[84:87]
s_add_u32 s74,s74,524288
M a[116:119],a[172:175],a[148:151],a[116:119]
T a[148:149],v22,12288
M a[24:27],a[160:163],a[152:155],a[24:27]
T a[150:151],v22,12800
v_add_u32 v17,4294868992,v17
M a[56:59],a[164:167],a[152:155],a[56:59]
v_add_u32 v21,4294868992,v21
M a[88:91],a[168:171],a[152:155],a[88:91]
buffer_store_dwordx4 v[68:71],v0,s[60:63],s70 offen offset:3072 sc1
M a[120:123],a[172:175],a[152:155],a[120:123]
T a[152:153],v23,12288
v_add_u32 v22,4294868992,v22
M a[28:31],a[160:163],a[156:159],a[28:31]
s_add_u32 s70,s70,16764928
T a[154:155],v23,12800
v_add_u32 v23,4294868992,v23
M a[60:63],a[164:167],a[156:159],a[60:63]
WL 4
M a[92:95],a[168:171],a[156:159],a[92:95]
M a[124:127],a[172:175],a[156:159],a[124:127]
T a[156:157],v24,12288
M a[0:3],a[176:179],a[128:131],a[0:3]
T a[158:159],v24,12800
v_add_u32 v24,4294868992,v24
M a[32:35],a[180:183],a[128:131],a[32:35]
M a[64:67],a[184:187],a[128:131],a[64:67]
M a[96:99],a[188:191],a[128:131],a[96:99]
M a[4:7],a[176:179],a[132:135],a[4:7]
M a[36:39],a[180:183],a[132:135],a[36:39]
M a[68:71],a[184:187],a[132:135],a[68:71]
s_waitcnt vmcnt(15) lgkmcnt(0)
s_barrier
M a[100:103],a[188:191],a[132:135],a[100:103]
s_add_u32 m0,s67,98304
D a[160:163],v17,0
M a[8:11],a[176:179],a[136:139],a[8:11]
D a[164:167],v17,2048
M a[40:43],a[180:183],a[136:139],a[40:43]
D a[168:171],v17,4096
M a[72:75],a[184:187],a[136:139],a[72:75]
G v5,s[44:47],s72,0
M a[104:107],a[188:191],a[136:139],a[104:107]
D a[172:175],v17,6144
M a[12:15],a[176:179],a[140:143],a[12:15]
T a[128:129],v21,0
M a[44:47],a[180:183],a[140:143],a[44:47]
G v6,s[44:47],s72,1024
M a[76:79],a[184:187],a[140:143],a[76:79]
T a[130:131],v21,512
M a[108:111],a[188:191],a[140:143],a[108:111]
T a[132:133],v22,0
M a[16:19],a[176:179],a[144:147],a[16:19]
T a[134:135],v22,512
M a[48:51],a[180:183],a[144:147],a[48:51]
G v7,s[44:47],s72,2048
M a[80:83],a[184:187],a[144:147],a[80:83]
T a[136:137],v23,0
M a[112:115],a[188:191],a[144:147],a[112:115]
T a[138:139],v23,512
M a[20:23],a[176:179],a[148:151],a[20:23]
G v8,s[44:47],s72,3072
M a[52:55],a[180:183],a[148:151],a[52:55]
s_add_u32 s72,s72,128
T a[140:141],v24,0
M a[84:87],a[184:187],a[148:151],a[84:87]
s_add_u32 m0,s67,102400
T a[142:143],v24,512
M a[116:119],a[188:191],a[148:151],a[116:119]
T a[144:145],v21,8192
M a[24:27],a[176:179],a[152:155],a[24:27]
T a[146:147],v21,8704
v_add_u32 v18,4294868992,v18
M a[56:59],a[180:183],a[152:155],a[56:59]
T a[148:149],v22,8192
M a[88:91],a[184:187],a[152:155],a[88:91]
T a[150:151],v22,8704
M a[120:123],a[188:191],a[152:155],a[120:123]
T a[152:153],v23,8192
M a[28:31],a[176:179],a[156:159],a[28:31]
G v5,s[44:47],s73,0
M a[60:63],a[180:183],a[156:159],a[60:63]
WL 6
T a[154:155],v23,8704
M a[92:95],a[184:187],a[156:159],a[92:95]
G v6,s[44:47],s73,1024
M a[124:127],a[188:191],a[156:159],a[124:127]
T a[156:157],v24,8192
M v[128:131],a[128:131],a[160:163],0
T a[158:159],v24,8704
M v[160:163],a[128:131],a[164:167],0
D a[176:179],v18,0
M v[192:195],a[128:131],a[168:171],0
D a[180:183],v18,2048
M v[224:227],a[128:131],a[172:175],0
G v7,s[44:47],s73,2048
M v[132:135],a[132:135],a[160:163],0
D a[184:187],v18,4096
M v[164:167],a[132:135],a[164:167],0
D a[188:191],v18,6144
M v[196:199],a[132:135],a[168:171],0
G v8,s[44:47],s73,3072
M v[228:231],a[132:135],a[172:175],0
s_add_u32 s73,s73,128
T a[128:129],v21,4096
M v[136:139],a[136:139],a[160:163],0
s_add_u32 m0,s68,131072
T a[130:131],v21,4608
M v[168:171],a[136:139],a[164:167],0
WL 5
T a[132:133],v22,4096
M v[200:203],a[136:139],a[168:171],0
T a[134:135],v22,4608
AR v48,a0
M v[232:235],a[136:139],a[172:175],0
T a[136:137],v23,4096
AR v49,a1
M v[140:143],a[140:143],a[160:163],0
G v9,s[48:51],s74,0
M v[172:175],a[140:143],a[164:167],0
T a[138:139],v23,4608
AR v50,a2
M v[204:207],a[140:143],a[168:171],0
G v10,s[48:51],s74,1024
M v[236:239],a[140:143],a[172:175],0
T a[140:141],v24,4096
AR v51,a3
M v[144:147],a[144:147],a[160:163],0
T a[142:143],v24,4608
AR v52,a32
M v[176:179],a[144:147],a[164:167],0
G v11,s[48:51],s74,2048
M v[208:211],a[144:147],a[168:171],0
G v12,s[48:51],s74,3072
M v[240:243],a[144:147],a[172:175],0
T a[144:145],v21,12288
AR v53,a33
M v[148:151],a[148:151],a[160:163],0
s_add_u32 s74,s74,524288
T a[146:147],v21,12800
AR v54,a34
M v[180:183],a[148:151],a[164:167],0
AR v55,a35
M v[212:215],a[148:151],a[168:171],0
C v40,v48,v49
M v[244:247],a[148:151],a[172:175],0
T a[148:149],v22,12288
C v41,v50,v51
M v[152:155],a[152:155],a[160:163],0
T a[150:151],v22,12800
v_add_u32 v17,65536,v17
M v[184:187],a[152:155],a[164:167],0
v_add_u32 v21,16384,v21
M v[216:219],a[152:155],a[168:171],0
v_add_u32 v22,16384,v22
M v[248:251],a[152:155],a[172:175],0
T a[152:153],v23,12288
C v42,v52,v53
M v[156:159],a[156:159],a[160:163],0
T a[154:155],v23,12800
v_add_u32 v23,16384,v23
M v[188:191],a[156:159],a[164:167],0
WL 4
C v43,v54,v55
M v[220:223],a[156:159],a[168:171],0
AR v48,a4
M v[252:255],a[156:159],a[172:175],0
T a[156:157],v24,12288
AR v49,a5
M v[128:131],a[128:131],a[176:179],v[128:131]
T a[158:159],v24,12800
v_add_u32 v24,16384,v24
M v[160:163],a[128:131],a[180:183],v[160:163]
buffer_store_dwordx4 v[40:43],v0,s[60:63],s70 offen offset:0 sc1
M v[192:195],a[128:131],a[184:187],v[192:195]
AR v50,a6
M v[224:227],a[128:131],a[188:191],v[224:227]
AR v51,a7
M v[132:135],a[132:135],a[176:179],v[132:135]
AR v52,a36
M v[164:167],a[132:135],a[180:183],v[164:167]
M v[196:199],a[132:135],a[184:187],v[196:199]
s_waitcnt vmcnt(14) lgkmcnt(0)
s_barrier
M v[228:231],a[132:135],a[188:191],v[228:231]
s_add_u32 m0,s67,0
D a[160:163],v17,0
AR v53,a37
M v[136:139],a[136:139],a[176:179],v[136:139]
D a[164:167],v17,2048
M v[168:171],a[136:139],a[180:183],v[168:171]
D a[168:171],v17,4096
AR v54,a38
M v[200:203],a[136:139],a[184:187],v[200:203]
G v5,s[44:47],s72,0
M v[232:235],a[136:139],a[188:191],v[232:235]
D a[172:175],v17,6144
AR v55,a39
M v[140:143],a[140:143],a[176:179],v[140:143]
T a[128:129],v21,0
M v[172:175],a[140:143],a[180:183],v[172:175]
G v6,s[44:47],s72,1024
M v[204:207],a[140:143],a[184:187],v[204:207]
T a[130:131],v21,512
C v44,v48,v49
M v[236:239],a[140:143],a[188:191],v[236:239]
T a[132:133],v22,0
C v45,v50,v51
M v[144:147],a[144:147],a[176:179],v[144:147]
T a[134:135],v22,512
M v[176:179],a[144:147],a[180:183],v[176:179]
G v7,s[44:47],s72,2048
M v[208:211],a[144:147],a[184:187],v[208:211]
T a[136:137],v23,0
C v46,v52,v53
M v[240:243],a[144:147],a[188:191],v[240:243]
T a[138:139],v23,512
M v[148:151],a[148:151],a[176:179],v[148:151]
G v8,s[44:47],s72,3072
M v[180:183],a[148:151],a[180:183],v[180:183]
s_add_u32 s72,s72,128
T a[140:141],v24,0
C v47,v54,v55
M v[212:215],a[148:151],a[184:187],v[212:215]
s_add_u32 m0,s67,4096
T a[142:143],v24,512
M v[244:247],a[148:151],a[188:191],v[244:247]
T a[144:145],v21,8192
M v[152:155],a[152:155],a[176:179],v[152:155]
T a[146:147],v21,8704
v_add_u32 v18,65536,v18
M v[184:187],a[152:155],a[180:183],v[184:187]
T a[148:149],v22,8192
AR v48,a8
M v[216:219],a[152:155],a[184:187],v[216:219]
T a[150:151],v22,8704
AR v49,a9
M v[248:251],a[152:155],a[188:191],v[248:251]
T a[152:153],v23,8192
M v[156:159],a[156:159],a[176:179],v[156:159]
G v5,s[44:47],s73,0
M v[188:191],a[156:159],a[180:183],v[188:191]
WL 6
T a[154:155],v23,8704
AR v50,a10
M v[220:223],a[156:159],a[184:187],v[220:223]
G v6,s[44:47],s73,1024
M v[252:255],a[156:159],a[188:191],v[252:255]
T a[156:157],v24,8192
AR v51,a11
M v[128:131],a[128:131],a[160:163],v[128:131]
T a[158:159],v24,8704
M v[160:163],a[128:131],a[164:167],v[160:163]
D a[176:179],v18,0
AR v52,a40
M v[192:195],a[128:131],a[168:171],v[192:195]
D a[180:183],v18,2048
M v[224:227],a[128:131],a[172:175],v[224:227]
G v7,s[44:47],s73,2048
M v[132:135],a[132:135],a[160:163],v[132:135]
D a[184:187],v18,4096
AR v53,a41
M v[164:167],a[132:135],a[164:167],v[164:167]
buffer_store_dwordx4 v[44:47],v0,s[60:63],s70 offen offset:1024 sc1
M v[196:199],a[132:135],a[168:171],v[196:199]
D a[188:191],v18,6144
AR v54,a42
M v[228:231],a[132:135],a[172:175],v[228:231]
G v8,s[44:47],s73,3072
M v[136:139],a[136:139],a[160:163],v[136:139]
s_add_u32 s73,s73,128
T a[128:129],v21,4096
AR v55,a43
M v[168:171],a[136:139],a[164:167],v[168:171]
WL 4
T a[130:131],v21,4608
M v[200:203],a[136:139],a[168:171],v[200:203]
s_add_u32 m0,s68,32768
T a[132:133],v22,4096
C v64,v48,v49
M v[232:235],a[136:139],a[172:175],v[232:235]
T a[134:135],v22,4608
M v[140:143],a[140:143],a[160:163],v[140:143]
T a[136:137],v23,4096
C v65,v50,v51
M v[172:175],a[140:143],a[164:167],v[172:175]
G v9,s[48:51],s74,0
M v[204:207],a[140:143],a[168:171],v[204:207]
T a[138:139],v23,4608
C v66,v52,v53
M v[236:239],a[140:143],a[172:175],v[236:239]
T a[140:141],v24,4096
M v[144:147],a[144:147],a[160:163],v[144:147]
G v10,s[48:51],s74,1024
M v[176:179],a[144:147],a[164:167],v[176:179]
T a[142:143],v24,4608
C v67,v54,v55
M v[208:211],a[144:147],a[168:171],v[208:211]
G v11,s[48:51],s74,2048
M v[240:243],a[144:147],a[172:175],v[240:243]
T a[144:145],v21,12288
M v[148:151],a[148:151],a[160:163],v[148:151]
T a[146:147],v21,12800
M v[180:183],a[148:151],a[164:167],v[180:183]
G v12,s[48:51],s74,3072
M v[212:215],a[148:151],a[168:171],v[212:215]
s_add_u32 s74,s74,524288
AR v48,a12
M v[244:247],a[148:151],a[172:175],v[244:247]
T a[148:149],v22,12288
AR v49,a13
M v[152:155],a[152:155],a[160:163],v[152:155]
T a[150:151],v22,12800
v_add_u32 v17,32768,v17
M v[184:187],a[152:155],a[164:167],v[184:187]
buffer_store_dwordx4 v[64:67],v0,s[60:63],s70 offen offset:2048 sc1
M v[216:219],a[152:155],a[168:171],v[216:219]
v_add_u32 v21,81920,v21
M v[248:251],a[152:155],a[172:175],v[248:251]
T a[152:153],v23,12288
v_add_u32 v22,81920,v22
M v[156:159],a[156:159],a[160:163],v[156:159]
T a[154:155],v23,12800
v_add_u32 v23,81920,v23
M v[188:191],a[156:159],a[164:167],v[188:191]
WL 4
AR v50,a14
M v[220:223],a[156:159],a[168:171],v[220:223]
AR v51,a15
M v[252:255],a[156:159],a[172:175],v[252:255]
T a[156:157],v24,12288
AR v52,a44
M v[128:131],a[128:131],a[176:179],v[128:131]
T a[158:159],v24,12800
v_add_u32 v24,81920,v24
M v[160:163],a[128:131],a[180:183],v[160:163]
AR v53,a45
M v[192:195],a[128:131],a[184:187],v[192:195]
AR v54,a46
M v[224:227],a[128:131],a[188:191],v[224:227]
M v[132:135],a[132:135],a[176:179],v[132:135]
AR v55,a47
M v[164:167],a[132:135],a[180:183],v[164:167]
M v[196:199],a[132:135],a[184:187],v[196:199]
s_waitcnt vmcnt(15) lgkmcnt(0)
s_barrier
M v[228:231],a[132:135],a[188:191],v[228:231]
s_add_u32 m0,s67,65536
D a[160:163],v17,0
C v68,v48,v49
M v[136:139],a[136:139],a[176:179],v[136:139]
D a[164:167],v17,2048
M v[168:171],a[136:139],a[180:183],v[168:171]
D a[168:171],v17,4096
C v69,v50,v51
M v[200:203],a[136:139],a[184:187],v[200:203]
G v5,s[44:47],s72,0
M v[232:235],a[136:139],a[188:191],v[232:235]
D a[172:175],v17,6144
C v70,v52,v53
M v[140:143],a[140:143],a[176:179],v[140:143]
T a[128:129],v21,0
M v[172:175],a[140:143],a[180:183],v[172:175]
G v6,s[44:47],s72,1024
M v[204:207],a[140:143],a[184:187],v[204:207]
T a[130:131],v21,512
C v71,v54,v55
M v[236:239],a[140:143],a[188:191],v[236:239]
T a[132:133],v22,0
M v[144:147],a[144:147],a[176:179],v[144:147]
T a[134:135],v22,512
M v[176:179],a[144:147],a[180:183],v[176:179]
G v7,s[44:47],s72,2048
M v[208:211],a[144:147],a[184:187],v[208:211]
T a[136:137],v23,0
AR v48,a16
M v[240:243],a[144:147],a[188:191],v[240:243]
T a[138:139],v23,512
M v[148:151],a[148:151],a[176:179],v[148:151]
G v8,s[44:47],s72,3072
M v[180:183],a[148:151],a[180:183],v[180:183]
s_add_u32 s72,s72,128
T a[140:141],v24,0
AR v49,a17
)"
R"(M v[212:215],a[148:151],a[184:187],v[212:215]
s_add_u32 m0,s67,69632
T a[142:143],v24,512
AR v50,a18
M v[244:247],a[148:151],a[188:191],v[244:247]
T a[144:145],v21,8192
M v[152:155],a[152:155],a[176:179],v[152:155]
T a[146:147],v21,8704
v_add_u32 v18,32768,v18
M v[184:187],a[152:155],a[180:183],v[184:187]
T a[148:149],v22,8192
AR v51,a19
M v[216:219],a[152:155],a[184:187],v[216:219]
T a[150:151],v22,8704
AR v52,a48
M v[248:251],a[152:155],a[188:191],v[248:251]
T a[152:153],v23,8192
M v[156:159],a[156:159],a[176:179],v[156:159]
G v5,s[44:47],s73,0
M v[188:191],a[156:159],a[180:183],v[188:191]
WL 6
T a[154:155],v23,8704
AR v53,a49
M v[220:223],a[156:159],a[184:187],v[220:223]
buffer_store_dwordx4 v[68:71],v0,s[60:63],s70 offen offset:3072 sc1
M v[252:255],a[156:159],a[188:191],v[252:255]
s_add_u32 s70,s70,4096
G v6,s[44:47],s73,1024
M v[128:131],a[128:131],a[160:163],v[128:131]
T a[156:157],v24,8192
AR v54,a50
M v[160:163],a[128:131],a[164:167],v[160:163]
T a[158:159],v24,8704
AR v55,a51
M v[192:195],a[128:131],a[168:171],v[192:195]
D a[176:179],v18,0
M v[224:227],a[128:131],a[172:175],v[224:227]
D a[180:183],v18,2048
C v40,v48,v49
M v[132:135],a[132:135],a[160:163],v[132:135]
G v7,s[44:47],s73,2048
M v[164:167],a[132:135],a[164:167],v[164:167]
D a[184:187],v18,4096
C v41,v50,v51
M v[196:199],a[132:135],a[168:171],v[196:199]
D a[188:191],v18,6144
M v[228:231],a[132:135],a[172:175],v[228:231]
G v8,s[44:47],s73,3072
M v[136:139],a[136:139],a[160:163],v[136:139]
s_add_u32 s73,s73,128
T a[128:129],v21,4096
C v42,v52,v53
M v[168:171],a[136:139],a[164:167],v[168:171]
WL 5
T a[130:131],v21,4608
M v[200:203],a[136:139],a[168:171],v[200:203]
s_add_u32 m0,s68,49152
T a[132:133],v22,4096
C v43,v54,v55
M v[232:235],a[136:139],a[172:175],v[232:235]
T a[134:135],v22,4608
M v[140:143],a[140:143],a[160:163],v[140:143]
T a[136:137],v23,4096
M v[172:175],a[140:143],a[164:167],v[172:175]
G v9,s[48:51],s74,0
M v[204:207],a[140:143],a[168:171],v[204:207]
T a[138:139],v23,4608
AR v48,a20
M v[236:239],a[140:143],a[172:175],v[236:239]
T a[140:141],v24,4096
M v[144:147],a[144:147],a[160:163],v[144:147]
G v10,s[48:51],s74,1024
M v[176:179],a[144:147],a[164:167],v[176:179]
T a[142:143],v24,4608
AR v49,a21
M v[208:211],a[144:147],a[168:171],v[208:211]
G v11,s[48:51],s74,2048
M v[240:243],a[144:147],a[172:175],v[240:243]
T a[144:145],v21,12288
AR v50,a22
M v[148:151],a[148:151],a[160:163],v[148:151]
T a[146:147],v21,12800
M v[180:183],a[148:151],a[164:167],v[180:183]
G v12,s[48:51],s74,3072
M v[212:215],a[148:151],a[168:171],v[212:215]
s_add_u32 s74,s74,524288
buffer_store_dwordx4 v[40:43],v0,s[60:63],s70 offen offset:0 sc1
M v[244:247],a[148:151],a[172:175],v[244:247]
T a[148:149],v22,12288
AR v51,a23
M v[152:155],a[152:155],a[160:163],v[152:155]
T a[150:151],v22,12800
v_add_u32 v17,4294868992,v17
M v[184:187],a[152:155],a[164:167],v[184:187]
v_add_u32 v21,4294868992,v21
M v[216:219],a[152:155],a[168:171],v[216:219]
v_add_u32 v22,4294868992,v22
M v[248:251],a[152:155],a[172:175],v[248:251]
T a[152:153],v23,12288
AR v52,a52
M v[156:159],a[156:159],a[160:163],v[156:159]
T a[154:155],v23,12800
v_add_u32 v23,4294868992,v23
M v[188:191],a[156:159],a[164:167],v[188:191]
WL 4
AR v53,a53
M v[220:223],a[156:159],a[168:171],v[220:223]
AR v54,a54
M v[252:255],a[156:159],a[172:175],v[252:255]
T a[156:157],v24,12288
AR v55,a55
M v[128:131],a[128:131],a[176:179],v[128:131]
T a[158:159],v24,12800
v_add_u32 v24,4294868992,v24
M v[160:163],a[128:131],a[180:183],v[160:163]
C v44,v48,v49
M v[192:195],a[128:131],a[184:187],v[192:195]
C v45,v50,v51
M v[224:227],a[128:131],a[188:191],v[224:227]
M v[132:135],a[132:135],a[176:179],v[132:135]
C v46,v52,v53
M v[164:167],a[132:135],a[180:183],v[164:167]
M v[196:199],a[132:135],a[184:187],v[196:199]
s_waitcnt vmcnt(15) lgkmcnt(0)
s_barrier
M v[228:231],a[132:135],a[188:191],v[228:231]
s_add_u32 m0,s67,98304
D a[160:163],v17,0
C v47,v54,v55
M v[136:139],a[136:139],a[176:179],v[136:139]
D a[164:167],v17,2048
M v[168:171],a[136:139],a[180:183],v[168:171]
D a[168:171],v17,4096
M v[200:203],a[136:139],a[184:187],v[200:203]
G v5,s[44:47],s72,0
M v[232:235],a[136:139],a[188:191],v[232:235]
D a[172:175],v17,6144
AR v48,a24
M v[140:143],a[140:143],a[176:179],v[140:143]
T a[128:129],v21,0
M v[172:175],a[140:143],a[180:183],v[172:175]
G v6,s[44:47],s72,1024
M v[204:207],a[140:143],a[184:187],v[204:207]
T a[130:131],v21,512
AR v49,a25
M v[236:239],a[140:143],a[188:191],v[236:239]
T a[132:133],v22,0
AR v50,a26
M v[144:147],a[144:147],a[176:179],v[144:147]
T a[134:135],v22,512
M v[176:179],a[144:147],a[180:183],v[176:179]
G v7,s[44:47],s72,2048
M v[208:211],a[144:147],a[184:187],v[208:211]
T a[136:137],v23,0
AR v51,a27
M v[240:243],a[144:147],a[188:191],v[240:243]
T a[138:139],v23,512
M v[148:151],a[148:151],a[176:179],v[148:151]
G v8,s[44:47],s72,3072
M v[180:183],a[148:151],a[180:183],v[180:183]
s_add_u32 s72,s72,128
T a[140:141],v24,0
AR v52,a56
M v[212:215],a[148:151],a[184:187],v[212:215]
s_add_u32 m0,s67,102400
T a[142:143],v24,512
AR v53,a57
M v[244:247],a[148:151],a[188:191],v[244:247]
T a[144:145],v21,8192
M v[152:155],a[152:155],a[176:179],v[152:155]
buffer_store_dwordx4 v[44:47],v0,s[60:63],s70 offen offset:1024 sc1
M v[184:187],a[152:155],a[180:183],v[184:187]
T a[146:147],v21,8704
v_add_u32 v18,4294868992,v18
M v[216:219],a[152:155],a[184:187],v[216:219]
T a[148:149],v22,8192
AR v54,a58
M v[248:251],a[152:155],a[188:191],v[248:251]
T a[150:151],v22,8704
AR v55,a59
M v[156:159],a[156:159],a[176:179],v[156:159]
T a[152:153],v23,8192
M v[188:191],a[156:159],a[180:183],v[188:191]
WL 6
G v5,s[44:47],s73,0
M v[220:223],a[156:159],a[184:187],v[220:223]
T a[154:155],v23,8704
C v64,v48,v49
M v[252:255],a[156:159],a[188:191],v[252:255]
G v6,s[44:47],s73,1024
M v[128:131],a[128:131],a[160:163],v[128:131]
T a[156:157],v24,8192
C v65,v50,v51
M v[160:163],a[128:131],a[164:167],v[160:163]
T a[158:159],v24,8704
C v66,v52,v53
M v[192:195],a[128:131],a[168:171],v[192:195]
D a[176:179],v18,0
M v[224:227],a[128:131],a[172:175],v[224:227]
D a[180:183],v18,2048
C v67,v54,v55
M v[132:135],a[132:135],a[160:163],v[132:135]
G v7,s[44:47],s73,2048
M v[164:167],a[132:135],a[164:167],v[164:167]
D a[184:187],v18,4096
M v[196:199],a[132:135],a[168:171],v[196:199]
D a[188:191],v18,6144
M v[228:231],a[132:135],a[172:175],v[228:231]
G v8,s[44:47],s73,3072
M v[136:139],a[136:139],a[160:163],v[136:139]
s_add_u32 s73,s73,128
T a[128:129],v21,4096
AR v48,a28
M v[168:171],a[136:139],a[164:167],v[168:171]
WL 5
T a[130:131],v21,4608
M v[200:203],a[136:139],a[168:171],v[200:203]
s_add_u32 m0,s68,131072
T a[132:133],v22,4096
AR v49,a29
M v[232:235],a[136:139],a[172:175],v[232:235]
T a[134:135],v22,4608
M v[140:143],a[140:143],a[160:163],v[140:143]
T a[136:137],v23,4096
AR v50,a30
M v[172:175],a[140:143],a[164:167],v[172:175]
G v9,s[48:51],s74,0
M v[204:207],a[140:143],a[168:171],v[204:207]
T a[138:139],v23,4608
AR v51,a31
M v[236:239],a[140:143],a[172:175],v[236:239]
T a[140:141],v24,4096
M v[144:147],a[144:147],a[160:163],v[144:147]
G v10,s[48:51],s74,1024
M v[176:179],a[144:147],a[164:167],v[176:179]
T a[142:143],v24,4608
AR v52,a60
M v[208:211],a[144:147],a[168:171],v[208:211]
buffer_store_dwordx4 v[64:67],v0,s[60:63],s70 offen offset:2048 sc1
M v[240:243],a[144:147],a[172:175],v[240:243]
G v11,s[48:51],s74,2048
M v[148:151],a[148:151],a[160:163],v[148:151]
T a[144:145],v21,12288
AR v53,a61
M v[180:183],a[148:151],a[164:167],v[180:183]
T a[146:147],v21,12800
AR v54,a62
M v[212:215],a[148:151],a[168:171],v[212:215]
G v12,s[48:51],s74,3072
M v[244:247],a[148:151],a[172:175],v[244:247]
s_add_u32 s74,s74,524288
T a[148:149],v22,12288
AR v55,a63
M v[152:155],a[152:155],a[160:163],v[152:155]
T a[150:151],v22,12800
v_add_u32 v17,65536,v17
M v[184:187],a[152:155],a[164:167],v[184:187]
v_add_u32 v21,16384,v21
M v[216:219],a[152:155],a[168:171],v[216:219]
v_add_u32 v22,16384,v22
M v[248:251],a[152:155],a[172:175],v[248:251]
T a[152:153],v23,12288
C v68,v48,v49
M v[156:159],a[156:159],a[160:163],v[156:159]
T a[154:155],v23,12800
v_add_u32 v23,16384,v23
M v[188:191],a[156:159],a[164:167],v[188:191]
WL 4
C v69,v50,v51
M v[220:223],a[156:159],a[168:171],v[220:223]
C v70,v52,v53
M v[252:255],a[156:159],a[172:175],v[252:255]
T a[156:157],v24,12288
M v[128:131],a[128:131],a[176:179],v[128:131]
T a[158:159],v24,12800
v_add_u32 v24,16384,v24
M v[160:163],a[128:131],a[180:183],v[160:163]
C v71,v54,v55
M v[192:195],a[128:131],a[184:187],v[192:195]
M v[224:227],a[128:131],a[188:191],v[224:227]
M v[132:135],a[132:135],a[176:179],v[132:135]
AR v48,a64
M v[164:167],a[132:135],a[180:183],v[164:167]
M v[196:199],a[132:135],a[184:187],v[196:199]
s_waitcnt vmcnt(15) lgkmcnt(0)
s_barrier
M v[228:231],a[132:135],a[188:191],v[228:231]
s_add_u32 m0,s67,0
D a[160:163],v17,0
AR v49,a65
M v[136:139],a[136:139],a[176:179],v[136:139]
D a[164:167],v17,2048
M v[168:171],a[136:139],a[180:183],v[168:171]
D a[168:171],v17,4096
AR v50,a66
M v[200:203],a[136:139],a[184:187],v[200:203]
G v5,s[44:47],s72,0
M v[232:235],a[136:139],a[188:191],v[232:235]
D a[172:175],v17,6144
AR v51,a67
M v[140:143],a[140:143],a[176:179],v[140:143]
T a[128:129],v21,0
M v[172:175],a[140:143],a[180:183],v[172:175]
G v6,s[44:47],s72,1024
M v[204:207],a[140:143],a[184:187],v[204:207]
T a[130:131],v21,512
AR v52,a96
M v[236:239],a[140:143],a[188:191],v[236:239]
T a[132:133],v22,0
M v[144:147],a[144:147],a[176:179],v[144:147]
buffer_store_dwordx4 v[68:71],v0,s[60:63],s70 offen offset:3072 sc1
M v[176:179],a[144:147],a[180:183],v[176:179]
s_add_u32 s70,s70,4096
T a[134:135],v22,512
AR v53,a97
M v[208:211],a[144:147],a[184:187],v[208:211]
G v7,s[44:47],s72,2048
M v[240:243],a[144:147],a[188:191],v[240:243]
T a[136:137],v23,0
AR v54,a98
M v[148:151],a[148:151],a[176:179],v[148:151]
T a[138:139],v23,512
AR v55,a99
M v[180:183],a[148:151],a[180:183],v[180:183]
G v8,s[44:47],s72,3072
M v[212:215],a[148:151],a[184:187],v[212:215]
s_add_u32 s72,s72,128
T a[140:141],v24,0
C v40,v48,v49
M v[244:247],a[148:151],a[188:191],v[244:247]
s_add_u32 m0,s67,4096
T a[142:143],v24,512
M v[152:155],a[152:155],a[176:179],v[152:155]
T a[144:145],v21,8192
v_add_u32 v18,65536,v18
M v[184:187],a[152:155],a[180:183],v[184:187]
T a[146:147],v21,8704
C v41,v50,v51
M v[216:219],a[152:155],a[184:187],v[216:219]
T a[148:149],v22,8192
M v[248:251],a[152:155],a[188:191],v[248:251]
T a[150:151],v22,8704
C v42,v52,v53
M v[156:159],a[156:159],a[176:179],v[156:159]
T a[152:153],v23,8192
M v[188:191],a[156:159],a[180:183],v[188:191]
WL 7
G v5,s[44:47],s73,0
M v[220:223],a[156:159],a[184:187],v[220:223]
T a[154:155],v23,8704
C v43,v54,v55
M v[252:255],a[156:159],a[188:191],v[252:255]
G v6,s[44:47],s73,1024
M v[128:131],a[128:131],a[160:163],v[128:131]
T a[156:157],v24,8192
M v[160:163],a[128:131],a[164:167],v[160:163]
T a[158:159],v24,8704
AR v48,a68
M v[192:195],a[128:131],a[168:171],v[192:195]
D a[176:179],v18,0
M v[224:227],a[128:131],a[172:175],v[224:227]
D a[180:183],v18,2048
AR v49,a69
M v[132:135],a[132:135],a[160:163],v[132:135]
G v7,s[44:47],s73,2048
M v[164:167],a[132:135],a[164:167],v[164:167]
D a[184:187],v18,4096
M v[196:199],a[132:135],a[168:171],v[196:199]
D a[188:191],v18,6144
AR v50,a70
M v[228:231],a[132:135],a[172:175],v[228:231]
G v8,s[44:47],s73,3072
M v[136:139],a[136:139],a[160:163],v[136:139]
s_add_u32 s73,s73,128
T a[128:129],v21,4096
AR v51,a71
M v[168:171],a[136:139],a[164:167],v[168:171]
WL 5
T a[130:131],v21,4608
M v[200:203],a[136:139],a[168:171],v[200:203]
s_add_u32 m0,s68,32768
T a[132:133],v22,4096
AR v52,a100
M v[232:235],a[136:139],a[172:175],v[232:235]
T a[134:135],v22,4608
M v[140:143],a[140:143],a[160:163],v[140:143]
buffer_store_dwordx4 v[40:43],v0,s[60:63],s70 offen offset:0 sc1
M v[172:175],a[140:143],a[164:167],v[172:175]
T a[136:137],v23,4096
AR v53,a101
M v[204:207],a[140:143],a[168:171],v[204:207]
G v9,s[48:51],s74,0
M v[236:239],a[140:143],a[172:175],v[236:239]
T a[138:139],v23,4608
AR v54,a102
M v[144:147],a[144:147],a[160:163],v[144:147]
T a[140:141],v24,4096
M v[176:179],a[144:147],a[164:167],v[176:179]
G v10,s[48:51],s74,1024
M v[208:211],a[144:147],a[168:171],v[208:211]
)"
R"(T a[142:143],v24,4608
AR v55,a103
M v[240:243],a[144:147],a[172:175],v[240:243]
G v11,s[48:51],s74,2048
M v[148:151],a[148:151],a[160:163],v[148:151]
T a[144:145],v21,12288
C v44,v48,v49
M v[180:183],a[148:151],a[164:167],v[180:183]
T a[146:147],v21,12800
C v45,v50,v51
M v[212:215],a[148:151],a[168:171],v[212:215]
G v12,s[48:51],s74,3072
M v[244:247],a[148:151],a[172:175],v[244:247]
s_add_u32 s74,s74,524288
T a[148:149],v22,12288
C v46,v52,v53
M v[152:155],a[152:155],a[160:163],v[152:155]
T a[150:151],v22,12800
v_add_u32 v17,32768,v17
M v[184:187],a[152:155],a[164:167],v[184:187]
v_add_u32 v21,81920,v21
M v[216:219],a[152:155],a[168:171],v[216:219]
v_add_u32 v22,81920,v22
M v[248:251],a[152:155],a[172:175],v[248:251]
T a[152:153],v23,12288
C v47,v54,v55
M v[156:159],a[156:159],a[160:163],v[156:159]
T a[154:155],v23,12800
v_add_u32 v23,81920,v23
M v[188:191],a[156:159],a[164:167],v[188:191]
WL 4
M v[220:223],a[156:159],a[168:171],v[220:223]
AR v48,a72
M v[252:255],a[156:159],a[172:175],v[252:255]
T a[156:157],v24,12288
M v[128:131],a[128:131],a[176:179],v[128:131]
T a[158:159],v24,12800
v_add_u32 v24,81920,v24
M v[160:163],a[128:131],a[180:183],v[160:163]
AR v49,a73
M v[192:195],a[128:131],a[184:187],v[192:195]
buffer_store_dwordx4 v[44:47],v0,s[60:63],s70 offen offset:1024 sc1
M v[224:227],a[128:131],a[188:191],v[224:227]
AR v50,a74
M v[132:135],a[132:135],a[176:179],v[132:135]
M v[164:167],a[132:135],a[180:183],v[164:167]
AR v51,a75
M v[196:199],a[132:135],a[184:187],v[196:199]
s_waitcnt vmcnt(15) lgkmcnt(0)
s_barrier
M v[228:231],a[132:135],a[188:191],v[228:231]
s_add_u32 m0,s67,65536
D a[160:163],v17,0
AR v52,a104
M v[136:139],a[136:139],a[176:179],v[136:139]
D a[164:167],v17,2048
M v[168:171],a[136:139],a[180:183],v[168:171]
D a[168:171],v17,4096
AR v53,a105
M v[200:203],a[136:139],a[184:187],v[200:203]
G v5,s[44:47],s72,0
M v[232:235],a[136:139],a[188:191],v[232:235]
D a[172:175],v17,6144
AR v54,a106
M v[140:143],a[140:143],a[176:179],v[140:143]
T a[128:129],v21,0
M v[172:175],a[140:143],a[180:183],v[172:175]
G v6,s[44:47],s72,1024
M v[204:207],a[140:143],a[184:187],v[204:207]
T a[130:131],v21,512
AR v55,a107
M v[236:239],a[140:143],a[188:191],v[236:239]
T a[132:133],v22,0
M v[144:147],a[144:147],a[176:179],v[144:147]
T a[134:135],v22,512
C v64,v48,v49
M v[176:179],a[144:147],a[180:183],v[176:179]
G v7,s[44:47],s72,2048
M v[208:211],a[144:147],a[184:187],v[208:211]
T a[136:137],v23,0
C v65,v50,v51
M v[240:243],a[144:147],a[188:191],v[240:243]
T a[138:139],v23,512
M v[148:151],a[148:151],a[176:179],v[148:151]
G v8,s[44:47],s72,3072
M v[180:183],a[148:151],a[180:183],v[180:183]
s_add_u32 s72,s72,128
T a[140:141],v24,0
C v66,v52,v53
M v[212:215],a[148:151],a[184:187],v[212:215]
s_add_u32 m0,s67,69632
T a[142:143],v24,512
C v67,v54,v55
M v[244:247],a[148:151],a[188:191],v[244:247]
T a[144:145],v21,8192
M v[152:155],a[152:155],a[176:179],v[152:155]
T a[146:147],v21,8704
v_add_u32 v18,32768,v18
M v[184:187],a[152:155],a[180:183],v[184:187]
T a[148:149],v22,8192
M v[216:219],a[152:155],a[184:187],v[216:219]
T a[150:151],v22,8704
M v[248:251],a[152:155],a[188:191],v[248:251]
T a[152:153],v23,8192
AR v48,a76
M v[156:159],a[156:159],a[176:179],v[156:159]
G v5,s[44:47],s73,0
M v[188:191],a[156:159],a[180:183],v[188:191]
WL 6
T a[154:155],v23,8704
AR v49,a77
M v[220:223],a[156:159],a[184:187],v[220:223]
G v6,s[44:47],s73,1024
M v[252:255],a[156:159],a[188:191],v[252:255]
T a[156:157],v24,8192
AR v50,a78
M v[128:131],a[128:131],a[160:163],v[128:131]
T a[158:159],v24,8704
M v[160:163],a[128:131],a[164:167],v[160:163]
D a[176:179],v18,0
AR v51,a79
M v[192:195],a[128:131],a[168:171],v[192:195]
D a[180:183],v18,2048
M v[224:227],a[128:131],a[172:175],v[224:227]
G v7,s[44:47],s73,2048
M v[132:135],a[132:135],a[160:163],v[132:135]
D a[184:187],v18,4096
AR v52,a108
M v[164:167],a[132:135],a[164:167],v[164:167]
D a[188:191],v18,6144
M v[196:199],a[132:135],a[168:171],v[196:199]
G v8,s[44:47],s73,3072
M v[228:231],a[132:135],a[172:175],v[228:231]
s_add_u32 s73,s73,128
buffer_store_dwordx4 v[64:67],v0,s[60:63],s70 offen offset:2048 sc1
M v[136:139],a[136:139],a[160:163],v[136:139]
s_add_u32 m0,s68,49152
T a[128:129],v21,4096
AR v53,a109
M v[168:171],a[136:139],a[164:167],v[168:171]
WL 4
T a[130:131],v21,4608
AR v54,a110
M v[200:203],a[136:139],a[168:171],v[200:203]
T a[132:133],v22,4096
AR v55,a111
M v[232:235],a[136:139],a[172:175],v[232:235]
T a[134:135],v22,4608
M v[140:143],a[140:143],a[160:163],v[140:143]
T a[136:137],v23,4096
C v68,v48,v49
M v[172:175],a[140:143],a[164:167],v[172:175]
G v9,s[48:51],s74,0
M v[204:207],a[140:143],a[168:171],v[204:207]
T a[138:139],v23,4608
M v[236:239],a[140:143],a[172:175],v[236:239]
T a[140:141],v24,4096
C v69,v50,v51
M v[144:147],a[144:147],a[160:163],v[144:147]
G v10,s[48:51],s74,1024
M v[176:179],a[144:147],a[164:167],v[176:179]
T a[142:143],v24,4608
C v70,v52,v53
M v[208:211],a[144:147],a[168:171],v[208:211]
G v11,s[48:51],s74,2048
M v[240:243],a[144:147],a[172:175],v[240:243]
T a[144:145],v21,12288
C v71,v54,v55
M v[148:151],a[148:151],a[160:163],v[148:151]
T a[146:147],v21,12800
M v[180:183],a[148:151],a[164:167],v[180:183]
G v12,s[48:51],s74,3072
M v[212:215],a[148:151],a[168:171],v[212:215]
s_add_u32 s74,s74,524288
M v[244:247],a[148:151],a[172:175],v[244:247]
T a[148:149],v22,12288
AR v48,a80
M v[152:155],a[152:155],a[160:163],v[152:155]
T a[150:151],v22,12800
v_add_u32 v17,4294868992,v17
M v[184:187],a[152:155],a[164:167],v[184:187]
v_add_u32 v21,4294868992,v21
M v[216:219],a[152:155],a[168:171],v[216:219]
v_add_u32 v22,4294868992,v22
M v[248:251],a[152:155],a[172:175],v[248:251]
T a[152:153],v23,12288
AR v49,a81
M v[156:159],a[156:159],a[160:163],v[156:159]
T a[154:155],v23,12800
v_add_u32 v23,4294868992,v23
M v[188:191],a[156:159],a[164:167],v[188:191]
WL 4
buffer_store_dwordx4 v[68:71],v0,s[60:63],s70 offen offset:3072 sc1
M v[220:223],a[156:159],a[168:171],v[220:223]
s_add_u32 s70,s70,4096
AR v50,a82
M v[252:255],a[156:159],a[172:175],v[252:255]
T a[156:157],v24,12288
AR v51,a83
M v[128:131],a[128:131],a[176:179],v[128:131]
T a[158:159],v24,12800
v_add_u32 v24,4294868992,v24
M v[160:163],a[128:131],a[180:183],v[160:163]
AR v52,a112
M v[192:195],a[128:131],a[184:187],v[192:195]
M v[224:227],a[128:131],a[188:191],v[224:227]
AR v53,a113
M v[132:135],a[132:135],a[176:179],v[132:135]
M v[164:167],a[132:135],a[180:183],v[164:167]
AR v54,a114
M v[196:199],a[132:135],a[184:187],v[196:199]
s_waitcnt vmcnt(15) lgkmcnt(0)
s_barrier
M v[228:231],a[132:135],a[188:191],v[228:231]
s_add_u32 m0,s67,98304
D a[160:163],v17,0
AR v55,a115
M v[136:139],a[136:139],a[176:179],v[136:139]
D a[164:167],v17,2048
M v[168:171],a[136:139],a[180:183],v[168:171]
D a[168:171],v17,4096
C v40,v48,v49
M v[200:203],a[136:139],a[184:187],v[200:203]
G v5,s[44:47],s72,0
M v[232:235],a[136:139],a[188:191],v[232:235]
D a[172:175],v17,6144
C v41,v50,v51
M v[140:143],a[140:143],a[176:179],v[140:143]
T a[128:129],v21,0
M v[172:175],a[140:143],a[180:183],v[172:175]
G v6,s[44:47],s72,1024
M v[204:207],a[140:143],a[184:187],v[204:207]
T a[130:131],v21,512
C v42,v52,v53
M v[236:239],a[140:143],a[188:191],v[236:239]
T a[132:133],v22,0
M v[144:147],a[144:147],a[176:179],v[144:147]
T a[134:135],v22,512
C v43,v54,v55
M v[176:179],a[144:147],a[180:183],v[176:179]
G v7,s[44:47],s72,2048
M v[208:211],a[144:147],a[184:187],v[208:211]
T a[136:137],v23,0
M v[240:243],a[144:147],a[188:191],v[240:243]
T a[138:139],v23,512
M v[148:151],a[148:151],a[176:179],v[148:151]
G v8,s[44:47],s72,3072
M v[180:183],a[148:151],a[180:183],v[180:183]
s_add_u32 s72,s72,128
T a[140:141],v24,0
AR v48,a84
M v[212:215],a[148:151],a[184:187],v[212:215]
s_add_u32 m0,s67,102400
T a[142:143],v24,512
AR v49,a85
M v[244:247],a[148:151],a[188:191],v[244:247]
T a[144:145],v21,8192
M v[152:155],a[152:155],a[176:179],v[152:155]
T a[146:147],v21,8704
v_add_u32 v18,4294868992,v18
M v[184:187],a[152:155],a[180:183],v[184:187]
T a[148:149],v22,8192
AR v50,a86
M v[216:219],a[152:155],a[184:187],v[216:219]
T a[150:151],v22,8704
M v[248:251],a[152:155],a[188:191],v[248:251]
T a[152:153],v23,8192
AR v51,a87
M v[156:159],a[156:159],a[176:179],v[156:159]
G v5,s[44:47],s73,0
M v[188:191],a[156:159],a[180:183],v[188:191]
WL 6
T a[154:155],v23,8704
AR v52,a116
M v[220:223],a[156:159],a[184:187],v[220:223]
G v6,s[44:47],s73,1024
M v[252:255],a[156:159],a[188:191],v[252:255]
buffer_store_dwordx4 v[40:43],v0,s[60:63],s70 offen offset:0 sc1
M v[128:131],a[128:131],a[160:163],v[128:131]
T a[156:157],v24,8192
AR v53,a117
M v[160:163],a[128:131],a[164:167],v[160:163]
T a[158:159],v24,8704
M v[192:195],a[128:131],a[168:171],v[192:195]
D a[176:179],v18,0
AR v54,a118
M v[224:227],a[128:131],a[172:175],v[224:227]
D a[180:183],v18,2048
M v[132:135],a[132:135],a[160:163],v[132:135]
G v7,s[44:47],s73,2048
M v[164:167],a[132:135],a[164:167],v[164:167]
D a[184:187],v18,4096
AR v55,a119
M v[196:199],a[132:135],a[168:171],v[196:199]
D a[188:191],v18,6144
C v44,v48,v49
M v[228:231],a[132:135],a[172:175],v[228:231]
G v8,s[44:47],s73,3072
M v[136:139],a[136:139],a[160:163],v[136:139]
s_add_u32 s73,s73,128
T a[128:129],v21,4096
C v45,v50,v51
M v[168:171],a[136:139],a[164:167],v[168:171]
WL 5
T a[130:131],v21,4608
M v[200:203],a[136:139],a[168:171],v[200:203]
s_add_u32 m0,s68,131072
T a[132:133],v22,4096
C v46,v52,v53
M v[232:235],a[136:139],a[172:175],v[232:235]
T a[134:135],v22,4608
M v[140:143],a[140:143],a[160:163],v[140:143]
T a[136:137],v23,4096
M v[172:175],a[140:143],a[164:167],v[172:175]
G v9,s[48:51],s74,0
M v[204:207],a[140:143],a[168:171],v[204:207]
T a[138:139],v23,4608
C v47,v54,v55
M v[236:239],a[140:143],a[172:175],v[236:239]
T a[140:141],v24,4096
M v[144:147],a[144:147],a[160:163],v[144:147]
G v10,s[48:51],s74,1024
M v[176:179],a[144:147],a[164:167],v[176:179]
T a[142:143],v24,4608
AR v48,a88
M v[208:211],a[144:147],a[168:171],v[208:211]
G v11,s[48:51],s74,2048
M v[240:243],a[144:147],a[172:175],v[240:243]
T a[144:145],v21,12288
AR v49,a89
M v[148:151],a[148:151],a[160:163],v[148:151]
T a[146:147],v21,12800
M v[180:183],a[148:151],a[164:167],v[180:183]
G v12,s[48:51],s74,3072
M v[212:215],a[148:151],a[168:171],v[212:215]
s_add_u32 s74,s74,524288
buffer_store_dwordx4 v[44:47],v0,s[60:63],s70 offen offset:1024 sc1
M v[244:247],a[148:151],a[172:175],v[244:247]
T a[148:149],v22,12288
AR v50,a90
M v[152:155],a[152:155],a[160:163],v[152:155]
T a[150:151],v22,12800
v_add_u32 v17,65536,v17
M v[184:187],a[152:155],a[164:167],v[184:187]
v_add_u32 v21,16384,v21
M v[216:219],a[152:155],a[168:171],v[216:219]
v_add_u32 v22,16384,v22
M v[248:251],a[152:155],a[172:175],v[248:251]
T a[152:153],v23,12288
AR v51,a91
M v[156:159],a[156:159],a[160:163],v[156:159]
T a[154:155],v23,12800
v_add_u32 v23,16384,v23
M v[188:191],a[156:159],a[164:167],v[188:191]
WL 4
AR v52,a120
M v[220:223],a[156:159],a[168:171],v[220:223]
AR v53,a121
M v[252:255],a[156:159],a[172:175],v[252:255]
T a[156:157],v24,12288
AR v54,a122
M v[128:131],a[128:131],a[176:179],v[128:131]
T a[158:159],v24,12800
v_add_u32 v24,16384,v24
M v[160:163],a[128:131],a[180:183],v[160:163]
AR v55,a123
M v[192:195],a[128:131],a[184:187],v[192:195]
M v[224:227],a[128:131],a[188:191],v[224:227]
C v64,v48,v49
M v[132:135],a[132:135],a[176:179],v[132:135]
M v[164:167],a[132:135],a[180:183],v[164:167]
C v65,v50,v51
M v[196:199],a[132:135],a[184:187],v[196:199]
s_waitcnt vmcnt(15) lgkmcnt(0)
s_barrier
M v[228:231],a[132:135],a[188:191],v[228:231]
s_add_u32 m0,s68,0
D a[160:163],v17,0
C v66,v52,v53
M v[136:139],a[136:139],a[176:179],v[136:139]
D a[164:167],v17,2048
M v[168:171],a[136:139],a[180:183],v[168:171]
D a[168:171],v17,4096
C v67,v54,v55
M v[200:203],a[136:139],a[184:187],v[200:203]
D a[172:175],v17,6144
M v[232:235],a[136:139],a[188:191],v[232:235]
G v5,s[52:55],s77,0
M v[140:143],a[140:143],a[176:179],v[140:143]
T a[128:129],v21,0
M v[172:175],a[140:143],a[180:183],v[172:175]
T a[130:131],v21,512
M v[204:207],a[140:143],a[184:187],v[204:207]
T a[132:133],v22,0
AR v48,a92
M v[236:239],a[140:143],a[188:191],v[236:239]
T a[134:135],v22,512
M v[144:147],a[144:147],a[176:179],v[144:147]
G v6,s[52:55],s77,1024
M v[176:179],a[144:147],a[180:183],v[176:179]
)"
R"(T a[136:137],v23,0
AR v49,a93
M v[208:211],a[144:147],a[184:187],v[208:211]
T a[138:139],v23,512
AR v50,a94
M v[240:243],a[144:147],a[188:191],v[240:243]
T a[140:141],v24,0
M v[148:151],a[148:151],a[176:179],v[148:151]
G v7,s[52:55],s77,2048
M v[180:183],a[148:151],a[180:183],v[180:183]
T a[142:143],v24,512
AR v51,a95
M v[212:215],a[148:151],a[184:187],v[212:215]
T a[144:145],v21,8192
M v[244:247],a[148:151],a[188:191],v[244:247]
T a[146:147],v21,8704
AR v52,a124
M v[152:155],a[152:155],a[176:179],v[152:155]
T a[148:149],v22,8192
v_add_u32 v18,65536,v18
M v[184:187],a[152:155],a[180:183],v[184:187]
buffer_store_dwordx4 v[64:67],v0,s[60:63],s70 offen offset:2048 sc1
M v[216:219],a[152:155],a[184:187],v[216:219]
G v8,s[52:55],s77,3072
M v[248:251],a[152:155],a[188:191],v[248:251]
s_add_u32 s77,s77,128
T a[150:151],v22,8704
AR v53,a125
M v[156:159],a[156:159],a[176:179],v[156:159]
s_add_u32 m0,s68,16384
T a[152:153],v23,8192
AR v54,a126
M v[188:191],a[156:159],a[180:183],v[188:191]
WL 5
T a[154:155],v23,8704
AR v55,a127
M v[220:223],a[156:159],a[184:187],v[220:223]
M v[252:255],a[156:159],a[188:191],v[252:255]
T a[156:157],v24,8192
C v68,v48,v49
M v[128:131],a[128:131],a[160:163],v[128:131]
T a[158:159],v24,8704
M v[160:163],a[128:131],a[164:167],v[160:163]
D a[176:179],v18,0
M v[192:195],a[128:131],a[168:171],v[192:195]
D a[180:183],v18,2048
C v69,v50,v51
M v[224:227],a[128:131],a[172:175],v[224:227]
D a[184:187],v18,4096
M v[132:135],a[132:135],a[160:163],v[132:135]
D a[188:191],v18,6144
C v70,v52,v53
M v[164:167],a[132:135],a[164:167],v[164:167]
G v13,s[56:59],s78,0
M v[196:199],a[132:135],a[168:171],v[196:199]
T a[128:129],v21,4096
C v71,v54,v55
M v[228:231],a[132:135],a[172:175],v[228:231]
T a[130:131],v21,4608
M v[136:139],a[136:139],a[160:163],v[136:139]
T a[132:133],v22,4096
M v[168:171],a[136:139],a[164:167],v[168:171]
G v14,s[56:59],s78,1024
M v[200:203],a[136:139],a[168:171],v[200:203]
T a[134:135],v22,4608
M v[232:235],a[136:139],a[172:175],v[232:235]
T a[136:137],v23,4096
M v[140:143],a[140:143],a[160:163],v[140:143]
T a[138:139],v23,4608
M v[172:175],a[140:143],a[164:167],v[172:175]
WL 6
G v15,s[56:59],s78,2048
M v[204:207],a[140:143],a[168:171],v[204:207]
G v16,s[56:59],s78,3072
M v[236:239],a[140:143],a[172:175],v[236:239]
T a[140:141],v24,4096
M v[144:147],a[144:147],a[160:163],v[144:147]
T a[142:143],v24,4608
M v[176:179],a[144:147],a[164:167],v[176:179]
buffer_store_dwordx4 v[68:71],v0,s[60:63],s70 offen offset:3072 sc1
M v[208:211],a[144:147],a[168:171],v[208:211]
s_add_u32 s70,s70,16764928
M v[240:243],a[144:147],a[172:175],v[240:243]
s_add_u32 s78,s78,221184
T a[144:145],v21,12288
M v[148:151],a[148:151],a[160:163],v[148:151]
T a[146:147],v21,12800
M v[180:183],a[148:151],a[164:167],v[180:183]
M v[212:215],a[148:151],a[168:171],v[212:215]
M v[244:247],a[148:151],a[172:175],v[244:247]
T a[148:149],v22,12288
M v[152:155],a[152:155],a[160:163],v[152:155]
T a[150:151],v22,12800
v_add_u32 v17,32768,v17
M v[184:187],a[152:155],a[164:167],v[184:187]
v_add_u32 v21,81920,v21
M v[216:219],a[152:155],a[168:171],v[216:219]
v_add_u32 v22,81920,v22
M v[248:251],a[152:155],a[172:175],v[248:251]
T a[152:153],v23,12288
M v[156:159],a[156:159],a[160:163],v[156:159]
T a[154:155],v23,12800
v_add_u32 v23,81920,v23
M v[188:191],a[156:159],a[164:167],v[188:191]
WL 4
M v[220:223],a[156:159],a[168:171],v[220:223]
M v[252:255],a[156:159],a[172:175],v[252:255]
T a[156:157],v24,12288
M v[128:131],a[128:131],a[176:179],v[128:131]
T a[158:159],v24,12800
v_add_u32 v24,81920,v24
M v[160:163],a[128:131],a[180:183],v[160:163]
M v[192:195],a[128:131],a[184:187],v[192:195]
M v[224:227],a[128:131],a[188:191],v[224:227]
M v[132:135],a[132:135],a[176:179],v[132:135]
M v[164:167],a[132:135],a[180:183],v[164:167]
M v[196:199],a[132:135],a[184:187],v[196:199]
s_waitcnt vmcnt(11) lgkmcnt(0)
s_barrier
M v[228:231],a[132:135],a[188:191],v[228:231]
s_add_u32 m0,s68,32768
D a[160:163],v17,0
M v[136:139],a[136:139],a[176:179],v[136:139]
D a[164:167],v17,2048
M v[168:171],a[136:139],a[180:183],v[168:171]
D a[168:171],v17,4096
M v[200:203],a[136:139],a[184:187],v[200:203]
D a[172:175],v17,6144
M v[232:235],a[136:139],a[188:191],v[232:235]
G v5,s[52:55],s77,0
M v[140:143],a[140:143],a[176:179],v[140:143]
T a[128:129],v21,0
M v[172:175],a[140:143],a[180:183],v[172:175]
T a[130:131],v21,512
M v[204:207],a[140:143],a[184:187],v[204:207]
T a[132:133],v22,0
M v[236:239],a[140:143],a[188:191],v[236:239]
T a[134:135],v22,512
M v[144:147],a[144:147],a[176:179],v[144:147]
G v6,s[52:55],s77,1024
M v[176:179],a[144:147],a[180:183],v[176:179]
T a[136:137],v23,0
M v[208:211],a[144:147],a[184:187],v[208:211]
T a[138:139],v23,512
M v[240:243],a[144:147],a[188:191],v[240:243]
T a[140:141],v24,0
M v[148:151],a[148:151],a[176:179],v[148:151]
G v7,s[52:55],s77,2048
M v[180:183],a[148:151],a[180:183],v[180:183]
T a[142:143],v24,512
M v[212:215],a[148:151],a[184:187],v[212:215]
T a[144:145],v21,8192
M v[244:247],a[148:151],a[188:191],v[244:247]
T a[146:147],v21,8704
M v[152:155],a[152:155],a[176:179],v[152:155]
T a[148:149],v22,8192
v_add_u32 v18,32768,v18
M v[184:187],a[152:155],a[180:183],v[184:187]
G v8,s[52:55],s77,3072
M v[216:219],a[152:155],a[184:187],v[216:219]
s_add_u32 s77,s77,128
T a[150:151],v22,8704
M v[248:251],a[152:155],a[188:191],v[248:251]
T a[152:153],v23,8192
M v[156:159],a[156:159],a[176:179],v[156:159]
s_add_u32 m0,s68,49152
T a[154:155],v23,8704
M v[188:191],a[156:159],a[180:183],v[188:191]
WL 6
M v[220:223],a[156:159],a[184:187],v[220:223]
M v[252:255],a[156:159],a[188:191],v[252:255]
T a[156:157],v24,8192
M v[128:131],a[128:131],a[160:163],v[128:131]
T a[158:159],v24,8704
M v[160:163],a[128:131],a[164:167],v[160:163]
T a[0:1],v36,0
M v[192:195],a[128:131],a[168:171],v[192:195]
D a[176:179],v18,0
M v[224:227],a[128:131],a[172:175],v[224:227]
T a[2:3],v36,160
M v[132:135],a[132:135],a[160:163],v[132:135]
D a[180:183],v18,2048
M v[164:167],a[132:135],a[164:167],v[164:167]
T a[4:5],v36,320
M v[196:199],a[132:135],a[168:171],v[196:199]
D a[184:187],v18,4096
M v[228:231],a[132:135],a[172:175],v[228:231]
T a[6:7],v36,480
M v[136:139],a[136:139],a[160:163],v[136:139]
D a[188:191],v18,6144
M v[168:171],a[136:139],a[164:167],v[168:171]
T a[8:9],v37,640
AW a10,0
M v[200:203],a[136:139],a[168:171],v[200:203]
G v13,s[56:59],s78,0
M v[232:235],a[136:139],a[172:175],v[232:235]
T a[128:129],v21,4096
T a[130:131],v21,4608
AW a11,0
M v[140:143],a[140:143],a[160:163],v[140:143]
T a[132:133],v22,4096
M v[172:175],a[140:143],a[164:167],v[172:175]
WL 7
T a[12:13],v36,8
M v[204:207],a[140:143],a[168:171],v[204:207]
G v14,s[56:59],s78,1024
M v[236:239],a[140:143],a[172:175],v[236:239]
T a[134:135],v22,4608
T a[14:15],v36,168
M v[144:147],a[144:147],a[160:163],v[144:147]
T a[136:137],v23,4096
T a[16:17],v36,328
M v[176:179],a[144:147],a[164:167],v[176:179]
T a[18:19],v36,488
M v[208:211],a[144:147],a[168:171],v[208:211]
T a[138:139],v23,4608
T a[20:21],v37,648
AW a22,0
M v[240:243],a[144:147],a[172:175],v[240:243]
G v15,s[56:59],s78,2048
M v[148:151],a[148:151],a[160:163],v[148:151]
T a[140:141],v24,4096
AW a23,0
M v[180:183],a[148:151],a[164:167],v[180:183]
T a[142:143],v24,4608
M v[212:215],a[148:151],a[168:171],v[212:215]
G v16,s[56:59],s78,3072
M v[244:247],a[148:151],a[172:175],v[244:247]
s_add_u32 s78,s78,221184
T a[144:145],v21,12288
T a[24:25],v36,16
M v[152:155],a[152:155],a[160:163],v[152:155]
T a[146:147],v21,12800
M v[184:187],a[152:155],a[164:167],v[184:187]
T a[26:27],v36,176
M v[216:219],a[152:155],a[168:171],v[216:219]
T a[28:29],v36,336
M v[248:251],a[152:155],a[172:175],v[248:251]
T a[148:149],v22,12288
T a[150:151],v22,12800
M v[156:159],a[156:159],a[160:163],v[156:159]
T a[30:31],v36,496
M v[188:191],a[156:159],a[164:167],v[188:191]
WL 8
T a[32:33],v37,656
AW a34,0
M v[220:223],a[156:159],a[168:171],v[220:223]
T a[152:153],v23,12288
AW a35,0
M v[252:255],a[156:159],a[172:175],v[252:255]
T a[154:155],v23,12800
M v[128:131],a[128:131],a[176:179],v[128:131]
T a[36:37],v36,24
M v[160:163],a[128:131],a[180:183],v[160:163]
T a[38:39],v36,184
M v[192:195],a[128:131],a[184:187],v[192:195]
T a[156:157],v24,12288
M v[224:227],a[128:131],a[188:191],v[224:227]
T a[158:159],v24,12800
M v[132:135],a[132:135],a[176:179],v[132:135]
T a[40:41],v36,344
M v[164:167],a[132:135],a[180:183],v[164:167]
T a[42:43],v36,504
M v[196:199],a[132:135],a[184:187],v[196:199]
T a[44:45],v37,664
AW a46,0
M v[228:231],a[132:135],a[188:191],v[228:231]
s_waitcnt vmcnt(9) lgkmcnt(0)
s_barrier
M v[136:139],a[136:139],a[176:179],v[136:139]
s_add_u32 m0,s68,65536
D a[160:163],v19,0
AW a47,0
M v[168:171],a[136:139],a[180:183],v[168:171]
T a[48:49],v36,32
M v[200:203],a[136:139],a[184:187],v[200:203]
D a[164:167],v19,2048
M v[232:235],a[136:139],a[188:191],v[232:235]
T a[50:51],v36,192
M v[140:143],a[140:143],a[176:179],v[140:143]
T a[128:129],v28,0
M v[172:175],a[140:143],a[180:183],v[172:175]
T a[52:53],v36,352
M v[204:207],a[140:143],a[184:187],v[204:207]
G v5,s[52:55],s77,0
M v[236:239],a[140:143],a[188:191],v[236:239]
T a[130:131],v28,1024
M v[144:147],a[144:147],a[176:179],v[144:147]
T a[54:55],v36,512
M v[176:179],a[144:147],a[180:183],v[176:179]
T a[132:133],v29,0
T a[56:57],v37,672
AW a58,0
M v[208:211],a[144:147],a[184:187],v[208:211]
G v6,s[52:55],s77,1024
M v[240:243],a[144:147],a[188:191],v[240:243]
T a[134:135],v29,1024
T a[136:137],v30,0
AW a59,0
M v[148:151],a[148:151],a[176:179],v[148:151]
G v7,s[52:55],s77,2048
M v[180:183],a[148:151],a[180:183],v[180:183]
T a[138:139],v30,1024
M v[212:215],a[148:151],a[184:187],v[212:215]
T a[140:141],v31,0
M v[244:247],a[148:151],a[188:191],v[244:247]
G v8,s[52:55],s77,3072
M v[152:155],a[152:155],a[176:179],v[152:155]
s_add_u32 s77,s77,128
T a[142:143],v31,1024
M v[184:187],a[152:155],a[180:183],v[184:187]
s_add_u32 m0,s68,81920
T a[144:145],v32,0
M v[216:219],a[152:155],a[184:187],v[216:219]
T a[146:147],v32,1024
M v[248:251],a[152:155],a[188:191],v[248:251]
T a[148:149],v33,0
M v[156:159],a[156:159],a[176:179],v[156:159]
T a[150:151],v33,1024
M v[188:191],a[156:159],a[180:183],v[188:191]
WL 6
T a[152:153],v34,0
M v[220:223],a[156:159],a[184:187],v[220:223]
T a[154:155],v34,1024
M v[252:255],a[156:159],a[188:191],v[252:255]
G v13,s[56:59],s78,0
M v[64:67],a[128:131],a[160:163],0
G v14,s[56:59],s78,1024
M v[96:99],a[128:131],a[164:167],0
T a[156:157],v35,0
M v[68:71],a[132:135],a[160:163],0
s_mov_b32 s36,s60
T a[158:159],v35,1024
M v[100:103],a[132:135],a[164:167],0
WL 5
D a[176:179],v20,0
M v[72:75],a[136:139],a[160:163],0
s_mov_b32 s37,s61
D a[180:183],v20,2048
M v[104:107],a[136:139],a[164:167],0
s_lshl_b32 s69,s35,19
T a[128:129],v28,8192
M v[76:79],a[140:143],a[160:163],0
s_add_u32 s69,s69,0
T a[130:131],v28,9216
T a[132:133],v29,8192
M v[108:111],a[140:143],a[164:167],0
WL 7
G v15,s[56:59],s78,2048
M v[80:83],a[144:147],a[160:163],0
s_add_u32 s36,s36,s69
s_addc_u32 s37,s37,0
T a[134:135],v29,9216
v_add_u32 v19,32768,v19
M v[112:115],a[144:147],a[164:167],0
s_add_u32 s40,s36,16777216
s_addc_u32 s41,s37,0
G v16,s[56:59],s78,3072
M v[84:87],a[148:151],a[160:163],0
s_add_u32 s78,s78,221184
T a[136:137],v30,8192
T a[138:139],v30,9216
v_add_u32 v28,32768,v28
M v[116:119],a[148:151],a[164:167],0
WL 7
T a[140:141],v31,8192
T a[142:143],v31,9216
v_add_u32 v29,32768,v29
M v[88:91],a[152:155],a[160:163],0
s_and_b32 s37,s37,65535
T a[144:145],v32,8192
v_add_u32 v30,32768,v30
v_add_u32 v31,32768,v31
M v[120:123],a[152:155],a[164:167],0
s_and_b32 s41,s41,65535
T a[146:147],v32,9216
v_add_u32 v32,32768,v32
C v40,v128,v129
M v[92:95],a[156:159],a[160:163],0
WL 7
T a[148:149],v33,8192
C v41,v130,v131
C v42,v132,v133
M v[124:127],a[156:159],a[164:167],0
s_mov_b32 s39,131072
T a[150:151],v33,9216
v_add_u32 v33,32768,v33
C v43,v134,v135
M v[64:67],a[128:131],a[176:179],v[64:67]
WL 8
T a[152:153],v34,8192
AW a64,v40
M v[96:99],a[128:131],a[180:183],v[96:99]
s_mov_b32 s43,131072
T a[154:155],v34,9216
v_add_u32 v34,32768,v34
M v[68:71],a[132:135],a[176:179],v[68:71]
WL 8
T a[156:157],v35,8192
AW a65,v41
)"
R"(M v[100:103],a[132:135],a[180:183],v[100:103]
WL 7
T a[158:159],v35,9216
v_add_u32 v35,32768,v35
M v[72:75],a[136:139],a[176:179],v[72:75]
s_sub_u32 s79,16,s27
s_waitcnt vmcnt(8) lgkmcnt(0)
s_barrier
M v[104:107],a[136:139],a[180:183],v[104:107]
s_lshl_b32 s38,s79,15
s_cmp_lg_u32 s20,0
s_cbranch_scc1 f_pb1
s_lshl_b32 s92,%2,6
s_add_u32 s92,s92,0
v_mov_b32 v25,s92
v_mov_b32 v26,s96
v_mov_b32 v27,s97
s_mov_b64 exec,1
global_store_dwordx2 v25,v[26:27],s[94:95] sc1
s_mov_b64 exec,-1
f_pb1:
M v[76:79],a[140:143],a[176:179],v[76:79]
s_mov_b32 s42,s38
D a[160:163],v19,0
AW a66,v42
M v[108:111],a[140:143],a[180:183],v[108:111]
s_lshl_b32 s75,s20,12
D a[164:167],v19,2048
AW a67,v43
M v[80:83],a[144:147],a[176:179],v[80:83]
s_mov_b32 s76,s75
T a[128:129],v28,0
v_add_u32 v20,32768,v20
M v[112:115],a[144:147],a[180:183],v[112:115]
s_add_u32 m0,s68,98304
T a[130:131],v28,1024
C v44,v136,v137
M v[84:87],a[148:151],a[176:179],v[84:87]
s_add_u32 s84,s80,8388608
s_addc_u32 s85,s81,0
G v5,s[52:55],s77,0
M v[116:119],a[148:151],a[180:183],v[116:119]
s_mov_b32 s86,1769472
T a[132:133],v29,0
C v45,v138,v139
T a[134:135],v29,1024
M v[88:91],a[152:155],a[176:179],v[88:91]
s_lshr_b32 s22,s35,2
T a[136:137],v30,0
C v46,v140,v141
M v[120:123],a[152:155],a[180:183],v[120:123]
s_and_b32 s23,s35,3
T a[138:139],v30,1024
C v47,v142,v143
M v[92:95],a[156:159],a[176:179],v[92:95]
s_lshl_b32 s23,s23,6
G v6,s[52:55],s77,1024
M v[124:127],a[156:159],a[180:183],v[124:127]
WL 4
T a[140:141],v31,0
T a[142:143],v31,1024
AW a68,v44
M v[64:67],a[128:131],a[160:163],v[64:67]
s_add_u32 s22,s22,s23
T a[144:145],v32,0
T a[146:147],v32,1024
AW a69,v45
M v[96:99],a[128:131],a[164:167],v[96:99]
WL 6
G v7,s[52:55],s77,2048
M v[68:71],a[132:135],a[160:163],v[68:71]
s_lshl_b32 s22,s22,6
T a[148:149],v33,0
T a[150:151],v33,1024
v_and_b32 v37,112,v0
M v[100:103],a[132:135],a[164:167],v[100:103]
WL 6
T a[152:153],v34,0
T a[154:155],v34,1024
AW a70,v46
M v[72:75],a[136:139],a[160:163],v[72:75]
G v8,s[52:55],s77,3072
M v[104:107],a[136:139],a[164:167],v[104:107]
WL 6
T a[156:157],v35,0
T a[158:159],v35,1024
v_lshlrev_b32 v37,5,v37
M v[76:79],a[140:143],a[160:163],v[76:79]
s_add_u32 s77,s77,128
s_add_u32 m0,s68,114688
D a[176:179],v20,0
D a[180:183],v20,2048
v_add_u32 v37,s22,v37
M v[108:111],a[140:143],a[164:167],v[108:111]
WL 8
T a[128:129],v28,8192
T a[130:131],v28,9216
AW a71,v47
M v[80:83],a[144:147],a[160:163],v[80:83]
G v13,s[56:59],s78,0
M v[112:115],a[144:147],a[164:167],v[112:115]
WL 8
T a[132:133],v29,8192
T a[134:135],v29,9216
v_add_u32 v19,32768,v19
M v[84:87],a[148:151],a[160:163],v[84:87]
G v14,s[56:59],s78,1024
M v[116:119],a[148:151],a[164:167],v[116:119]
WL 8
T a[136:137],v30,8192
T a[138:139],v30,9216
v_add_u32 v28,32768,v28
M v[88:91],a[152:155],a[160:163],v[88:91]
G v15,s[56:59],s78,2048
M v[120:123],a[152:155],a[164:167],v[120:123]
WL 8
T a[140:141],v31,8192
T a[142:143],v31,9216
v_add_u32 v29,32768,v29
v_add_u32 v30,32768,v30
M v[92:95],a[156:159],a[160:163],v[92:95]
G v16,s[56:59],s78,3072
M v[124:127],a[156:159],a[164:167],v[124:127]
WL 6
T a[144:145],v32,8192
T a[146:147],v32,9216
v_add_u32 v31,32768,v31
v_add_u32 v32,32768,v32
M v[64:67],a[128:131],a[176:179],v[64:67]
s_add_u32 s78,s78,221184
T a[148:149],v33,8192
T a[150:151],v33,9216
v_add_u32 v33,32768,v33
C v40,v144,v145
M v[96:99],a[128:131],a[180:183],v[96:99]
WL 8
T a[152:153],v34,8192
T a[154:155],v34,9216
v_add_u32 v34,32768,v34
C v41,v146,v147
M v[68:71],a[132:135],a[176:179],v[68:71]
T a[156:157],v35,8192
C v42,v148,v149
M v[100:103],a[132:135],a[180:183],v[100:103]
WL 9
T a[158:159],v35,9216
v_add_u32 v35,32768,v35
M v[72:75],a[136:139],a[176:179],v[72:75]
s_waitcnt vmcnt(8) lgkmcnt(0)
s_barrier
M v[104:107],a[136:139],a[180:183],v[104:107]
D a[160:163],v19,0
C v43,v150,v151
M v[76:79],a[140:143],a[176:179],v[76:79]
s_add_u32 m0,s68,0
D a[164:167],v19,2048
AW a72,v40
M v[108:111],a[140:143],a[180:183],v[108:111]
T a[128:129],v28,0
AW a73,v41
M v[80:83],a[144:147],a[176:179],v[80:83]
T a[130:131],v28,1024
AW a74,v42
M v[112:115],a[144:147],a[180:183],v[112:115]
G v5,s[52:55],s77,0
M v[84:87],a[148:151],a[176:179],v[84:87]
T a[132:133],v29,0
AW a75,v43
M v[116:119],a[148:151],a[180:183],v[116:119]
T a[134:135],v29,1024
v_add_u32 v20,32768,v20
M v[88:91],a[152:155],a[176:179],v[88:91]
T a[136:137],v30,0
C v44,v152,v153
M v[120:123],a[152:155],a[180:183],v[120:123]
T a[138:139],v30,1024
C v45,v154,v155
M v[92:95],a[156:159],a[176:179],v[92:95]
G v6,s[52:55],s77,1024
M v[124:127],a[156:159],a[180:183],v[124:127]
WL 4
T a[140:141],v31,0
T a[142:143],v31,1024
C v46,v156,v157
M v[64:67],a[128:131],a[160:163],v[64:67]
T a[144:145],v32,0
T a[146:147],v32,1024
C v47,v158,v159
M v[96:99],a[128:131],a[164:167],v[96:99]
WL 6
G v7,s[52:55],s77,2048
M v[68:71],a[132:135],a[160:163],v[68:71]
T a[148:149],v33,0
T a[150:151],v33,1024
AW a76,v44
M v[100:103],a[132:135],a[164:167],v[100:103]
WL 6
T a[152:153],v34,0
T a[154:155],v34,1024
AW a77,v45
M v[72:75],a[136:139],a[160:163],v[72:75]
G v8,s[52:55],s77,3072
M v[104:107],a[136:139],a[164:167],v[104:107]
WL 6
T a[156:157],v35,0
T a[158:159],v35,1024
AW a78,v46
M v[76:79],a[140:143],a[160:163],v[76:79]
s_add_u32 s77,s77,128
s_add_u32 m0,s68,16384
D a[176:179],v20,0
D a[180:183],v20,2048
AW a79,v47
M v[108:111],a[140:143],a[164:167],v[108:111]
WL 8
T a[128:129],v28,8192
T a[130:131],v28,9216
C v40,v160,v161
M v[80:83],a[144:147],a[160:163],v[80:83]
G v13,s[56:59],s78,0
M v[112:115],a[144:147],a[164:167],v[112:115]
WL 8
T a[132:133],v29,8192
T a[134:135],v29,9216
v_add_u32 v19,32768,v19
M v[84:87],a[148:151],a[160:163],v[84:87]
G v14,s[56:59],s78,1024
M v[116:119],a[148:151],a[164:167],v[116:119]
WL 8
T a[136:137],v30,8192
T a[138:139],v30,9216
v_add_u32 v28,32768,v28
M v[88:91],a[152:155],a[160:163],v[88:91]
G v15,s[56:59],s78,2048
M v[120:123],a[152:155],a[164:167],v[120:123]
WL 8
T a[140:141],v31,8192
T a[142:143],v31,9216
v_add_u32 v29,32768,v29
v_add_u32 v30,32768,v30
M v[92:95],a[156:159],a[160:163],v[92:95]
G v16,s[56:59],s78,3072
M v[124:127],a[156:159],a[164:167],v[124:127]
WL 6
T a[144:145],v32,8192
T a[146:147],v32,9216
v_add_u32 v31,32768,v31
v_add_u32 v32,32768,v32
M v[64:67],a[128:131],a[176:179],v[64:67]
s_add_u32 s78,s78,221184
T a[148:149],v33,8192
T a[150:151],v33,9216
v_add_u32 v33,32768,v33
C v41,v162,v163
M v[96:99],a[128:131],a[180:183],v[96:99]
WL 8
T a[152:153],v34,8192
T a[154:155],v34,9216
v_add_u32 v34,32768,v34
C v42,v164,v165
M v[68:71],a[132:135],a[176:179],v[68:71]
T a[156:157],v35,8192
C v43,v166,v167
M v[100:103],a[132:135],a[180:183],v[100:103]
WL 9
T a[158:159],v35,9216
v_add_u32 v35,32768,v35
M v[72:75],a[136:139],a[176:179],v[72:75]
s_waitcnt vmcnt(8) lgkmcnt(0)
s_barrier
M v[104:107],a[136:139],a[180:183],v[104:107]
D a[160:163],v19,0
AW a80,v40
M v[76:79],a[140:143],a[176:179],v[76:79]
s_add_u32 m0,s68,32768
D a[164:167],v19,2048
AW a81,v41
M v[108:111],a[140:143],a[180:183],v[108:111]
T a[128:129],v28,0
AW a82,v42
M v[80:83],a[144:147],a[176:179],v[80:83]
T a[130:131],v28,1024
AW a83,v43
M v[112:115],a[144:147],a[180:183],v[112:115]
G v5,s[52:55],s77,0
M v[84:87],a[148:151],a[176:179],v[84:87]
T a[132:133],v29,0
C v44,v168,v169
M v[116:119],a[148:151],a[180:183],v[116:119]
T a[134:135],v29,1024
v_add_u32 v20,32768,v20
M v[88:91],a[152:155],a[176:179],v[88:91]
T a[136:137],v30,0
C v45,v170,v171
M v[120:123],a[152:155],a[180:183],v[120:123]
T a[138:139],v30,1024
C v46,v172,v173
M v[92:95],a[156:159],a[176:179],v[92:95]
G v6,s[52:55],s77,1024
M v[124:127],a[156:159],a[180:183],v[124:127]
WL 4
T a[140:141],v31,0
T a[142:143],v31,1024
C v47,v174,v175
M v[64:67],a[128:131],a[160:163],v[64:67]
T a[144:145],v32,0
T a[146:147],v32,1024
AW a84,v44
M v[96:99],a[128:131],a[164:167],v[96:99]
WL 6
G v7,s[52:55],s77,2048
M v[68:71],a[132:135],a[160:163],v[68:71]
T a[148:149],v33,0
T a[150:151],v33,1024
AW a85,v45
M v[100:103],a[132:135],a[164:167],v[100:103]
WL 6
T a[152:153],v34,0
T a[154:155],v34,1024
AW a86,v46
M v[72:75],a[136:139],a[160:163],v[72:75]
G v8,s[52:55],s77,3072
M v[104:107],a[136:139],a[164:167],v[104:107]
WL 6
T a[156:157],v35,0
T a[158:159],v35,1024
AW a87,v47
M v[76:79],a[140:143],a[160:163],v[76:79]
s_add_u32 s77,s77,128
s_add_u32 m0,s68,49152
D a[176:179],v20,0
D a[180:183],v20,2048
C v40,v176,v177
M v[108:111],a[140:143],a[164:167],v[108:111]
WL 8
T a[128:129],v28,8192
T a[130:131],v28,9216
C v41,v178,v179
M v[80:83],a[144:147],a[160:163],v[80:83]
G v13,s[56:59],s78,0
M v[112:115],a[144:147],a[164:167],v[112:115]
WL 8
T a[132:133],v29,8192
T a[134:135],v29,9216
v_add_u32 v19,4294868992,v19
M v[84:87],a[148:151],a[160:163],v[84:87]
G v14,s[56:59],s78,1024
M v[116:119],a[148:151],a[164:167],v[116:119]
WL 8
T a[136:137],v30,8192
T a[138:139],v30,9216
v_add_u32 v28,4294868992,v28
M v[88:91],a[152:155],a[160:163],v[88:91]
G v15,s[56:59],s78,2048
M v[120:123],a[152:155],a[164:167],v[120:123]
WL 8
T a[140:141],v31,8192
T a[142:143],v31,9216
v_add_u32 v29,4294868992,v29
v_add_u32 v30,4294868992,v30
M v[92:95],a[156:159],a[160:163],v[92:95]
G v16,s[56:59],s78,3072
M v[124:127],a[156:159],a[164:167],v[124:127]
WL 6
T a[144:145],v32,8192
T a[146:147],v32,9216
v_add_u32 v31,4294868992,v31
v_add_u32 v32,4294868992,v32
M v[64:67],a[128:131],a[176:179],v[64:67]
s_add_u32 s78,s78,221184
T a[148:149],v33,8192
T a[150:151],v33,9216
v_add_u32 v33,4294868992,v33
C v42,v180,v181
M v[96:99],a[128:131],a[180:183],v[96:99]
WL 8
T a[152:153],v34,8192
T a[154:155],v34,9216
v_add_u32 v34,4294868992,v34
C v43,v182,v183
M v[68:71],a[132:135],a[176:179],v[68:71]
T a[156:157],v35,8192
AW a88,v40
M v[100:103],a[132:135],a[180:183],v[100:103]
WL 9
T a[158:159],v35,9216
v_add_u32 v35,4294868992,v35
M v[72:75],a[136:139],a[176:179],v[72:75]
s_waitcnt vmcnt(8) lgkmcnt(0)
s_barrier
M v[104:107],a[136:139],a[180:183],v[104:107]
s_cmp_lg_u32 s20,0
s_cbranch_scc1 f_pl1
global_load_dwordx2 v[26:27],v37,s[94:95] sc1
f_pl1:
M v[76:79],a[140:143],a[176:179],v[76:79]
s_add_u32 m0,s68,65536
D a[160:163],v19,0
AW a89,v41
M v[108:111],a[140:143],a[180:183],v[108:111]
)"
R"(D a[164:167],v19,2048
AW a90,v42
M v[80:83],a[144:147],a[176:179],v[80:83]
T a[128:129],v28,0
AW a91,v43
M v[112:115],a[144:147],a[180:183],v[112:115]
T a[130:131],v28,1024
C v44,v184,v185
M v[84:87],a[148:151],a[176:179],v[84:87]
G v5,s[52:55],s77,0
M v[116:119],a[148:151],a[180:183],v[116:119]
T a[132:133],v29,0
v_add_u32 v20,4294868992,v20
T a[134:135],v29,1024
M v[88:91],a[152:155],a[176:179],v[88:91]
T a[136:137],v30,0
C v45,v186,v187
M v[120:123],a[152:155],a[180:183],v[120:123]
T a[138:139],v30,1024
C v46,v188,v189
M v[92:95],a[156:159],a[176:179],v[92:95]
G v6,s[52:55],s77,1024
M v[124:127],a[156:159],a[180:183],v[124:127]
WL 4
T a[140:141],v31,0
T a[142:143],v31,1024
C v47,v190,v191
M v[64:67],a[128:131],a[160:163],v[64:67]
T a[144:145],v32,0
T a[146:147],v32,1024
AW a92,v44
M v[96:99],a[128:131],a[164:167],v[96:99]
WL 6
G v7,s[52:55],s77,2048
M v[68:71],a[132:135],a[160:163],v[68:71]
T a[148:149],v33,0
T a[150:151],v33,1024
AW a93,v45
M v[100:103],a[132:135],a[164:167],v[100:103]
WL 6
T a[152:153],v34,0
T a[154:155],v34,1024
AW a94,v46
M v[72:75],a[136:139],a[160:163],v[72:75]
G v8,s[52:55],s77,3072
M v[104:107],a[136:139],a[164:167],v[104:107]
WL 6
T a[156:157],v35,0
T a[158:159],v35,1024
AW a95,v47
M v[76:79],a[140:143],a[160:163],v[76:79]
s_add_u32 s77,s77,128
s_add_u32 m0,s68,81920
D a[176:179],v20,0
D a[180:183],v20,2048
C v40,v192,v193
M v[108:111],a[140:143],a[164:167],v[108:111]
WL 8
T a[128:129],v28,8192
T a[130:131],v28,9216
C v41,v194,v195
M v[80:83],a[144:147],a[160:163],v[80:83]
G v13,s[56:59],s78,0
M v[112:115],a[144:147],a[164:167],v[112:115]
WL 8
T a[132:133],v29,8192
T a[134:135],v29,9216
v_add_u32 v19,32768,v19
M v[84:87],a[148:151],a[160:163],v[84:87]
G v14,s[56:59],s78,1024
M v[116:119],a[148:151],a[164:167],v[116:119]
WL 8
T a[136:137],v30,8192
T a[138:139],v30,9216
v_add_u32 v28,32768,v28
M v[88:91],a[152:155],a[160:163],v[88:91]
G v15,s[56:59],s78,2048
M v[120:123],a[152:155],a[164:167],v[120:123]
WL 8
T a[140:141],v31,8192
T a[142:143],v31,9216
v_add_u32 v29,32768,v29
v_add_u32 v30,32768,v30
M v[92:95],a[156:159],a[160:163],v[92:95]
G v16,s[56:59],s78,3072
M v[124:127],a[156:159],a[164:167],v[124:127]
WL 6
T a[144:145],v32,8192
T a[146:147],v32,9216
v_add_u32 v31,32768,v31
v_add_u32 v32,32768,v32
M v[64:67],a[128:131],a[176:179],v[64:67]
s_add_u32 s78,s78,221184
T a[148:149],v33,8192
T a[150:151],v33,9216
v_add_u32 v33,32768,v33
C v42,v196,v197
M v[96:99],a[128:131],a[180:183],v[96:99]
WL 8
T a[152:153],v34,8192
T a[154:155],v34,9216
v_add_u32 v34,32768,v34
C v43,v198,v199
M v[68:71],a[132:135],a[176:179],v[68:71]
T a[156:157],v35,8192
AW a96,v40
M v[100:103],a[132:135],a[180:183],v[100:103]
WL 9
T a[158:159],v35,9216
v_add_u32 v35,32768,v35
M v[72:75],a[136:139],a[176:179],v[72:75]
s_waitcnt vmcnt(8) lgkmcnt(0)
s_barrier
M v[104:107],a[136:139],a[180:183],v[104:107]
D a[160:163],v19,0
AW a97,v41
M v[76:79],a[140:143],a[176:179],v[76:79]
s_add_u32 m0,s68,98304
D a[164:167],v19,2048
AW a98,v42
M v[108:111],a[140:143],a[180:183],v[108:111]
T a[128:129],v28,0
AW a99,v43
M v[80:83],a[144:147],a[176:179],v[80:83]
s_cmp_lg_u32 s20,0
s_cbranch_scc1 f_pc1
WV 8
v_cmp_ne_u32 vcc,s96,v26
v_cmp_ne_u32 s[92:93],s97,v27
s_nop 1
s_or_b64 vcc,vcc,s[92:93]
s_cbranch_vccz f_c1_ok
s_mov_b32 s98,200000
f_c1_spin:
s_sleep 2
global_load_dwordx2 v[26:27],v37,s[94:95] sc1
WV 0
v_cmp_ne_u32 vcc,s96,v26
v_cmp_ne_u32 s[92:93],s97,v27
s_nop 1
s_or_b64 vcc,vcc,s[92:93]
s_cbranch_vccz f_c1_ok
s_sub_u32 s98,s98,1
s_cbranch_scc0 f_c1_spin
f_c1_ok:
f_pc1:
M v[112:115],a[144:147],a[180:183],v[112:115]
T a[130:131],v28,1024
C v44,v200,v201
M v[84:87],a[148:151],a[176:179],v[84:87]
G v5,s[52:55],s77,0
M v[116:119],a[148:151],a[180:183],v[116:119]
T a[132:133],v29,0
C v45,v202,v203
T a[134:135],v29,1024
M v[88:91],a[152:155],a[176:179],v[88:91]
T a[136:137],v30,0
v_add_u32 v20,32768,v20
M v[120:123],a[152:155],a[180:183],v[120:123]
T a[138:139],v30,1024
C v46,v204,v205
M v[92:95],a[156:159],a[176:179],v[92:95]
G v6,s[52:55],s77,1024
M v[124:127],a[156:159],a[180:183],v[124:127]
WL 4
T a[140:141],v31,0
T a[142:143],v31,1024
C v47,v206,v207
M v[64:67],a[128:131],a[160:163],v[64:67]
T a[144:145],v32,0
T a[146:147],v32,1024
AW a100,v44
M v[96:99],a[128:131],a[164:167],v[96:99]
WL 6
G v7,s[52:55],s77,2048
M v[68:71],a[132:135],a[160:163],v[68:71]
T a[148:149],v33,0
T a[150:151],v33,1024
AW a101,v45
M v[100:103],a[132:135],a[164:167],v[100:103]
WL 6
T a[152:153],v34,0
T a[154:155],v34,1024
AW a102,v46
M v[72:75],a[136:139],a[160:163],v[72:75]
G v8,s[52:55],s77,3072
M v[104:107],a[136:139],a[164:167],v[104:107]
WL 6
T a[156:157],v35,0
T a[158:159],v35,1024
AW a103,v47
M v[76:79],a[140:143],a[160:163],v[76:79]
s_add_u32 s77,s77,128
s_add_u32 m0,s68,114688
D a[176:179],v20,0
D a[180:183],v20,2048
C v40,v208,v209
M v[108:111],a[140:143],a[164:167],v[108:111]
WL 8
T a[128:129],v28,8192
T a[130:131],v28,9216
C v41,v210,v211
M v[80:83],a[144:147],a[160:163],v[80:83]
G v13,s[56:59],s78,0
M v[112:115],a[144:147],a[164:167],v[112:115]
WL 8
T a[132:133],v29,8192
T a[134:135],v29,9216
v_add_u32 v19,32768,v19
M v[84:87],a[148:151],a[160:163],v[84:87]
G v14,s[56:59],s78,1024
M v[116:119],a[148:151],a[164:167],v[116:119]
WL 8
T a[136:137],v30,8192
T a[138:139],v30,9216
v_add_u32 v28,32768,v28
M v[88:91],a[152:155],a[160:163],v[88:91]
G v15,s[56:59],s78,2048
M v[120:123],a[152:155],a[164:167],v[120:123]
WL 8
T a[140:141],v31,8192
T a[142:143],v31,9216
v_add_u32 v29,32768,v29
v_add_u32 v30,32768,v30
M v[92:95],a[156:159],a[160:163],v[92:95]
G v16,s[56:59],s78,3072
M v[124:127],a[156:159],a[164:167],v[124:127]
WL 6
T a[144:145],v32,8192
T a[146:147],v32,9216
v_add_u32 v31,32768,v31
v_add_u32 v32,32768,v32
M v[64:67],a[128:131],a[176:179],v[64:67]
s_add_u32 s78,s78,221184
T a[148:149],v33,8192
T a[150:151],v33,9216
v_add_u32 v33,32768,v33
C v42,v212,v213
M v[96:99],a[128:131],a[180:183],v[96:99]
WL 8
T a[152:153],v34,8192
T a[154:155],v34,9216
v_add_u32 v34,32768,v34
C v43,v214,v215
M v[68:71],a[132:135],a[176:179],v[68:71]
T a[156:157],v35,8192
AW a104,v40
M v[100:103],a[132:135],a[180:183],v[100:103]
WL 9
T a[158:159],v35,9216
v_add_u32 v35,32768,v35
M v[72:75],a[136:139],a[176:179],v[72:75]
s_waitcnt vmcnt(8) lgkmcnt(0)
s_barrier
M v[104:107],a[136:139],a[180:183],v[104:107]
D a[160:163],v19,0
AW a105,v41
M v[76:79],a[140:143],a[176:179],v[76:79]
s_add_u32 m0,s68,131072
D a[164:167],v19,2048
AW a106,v42
M v[108:111],a[140:143],a[180:183],v[108:111]
T a[128:129],v28,0
AW a107,v43
M v[80:83],a[144:147],a[176:179],v[80:83]
T a[130:131],v28,1024
C v44,v216,v217
M v[112:115],a[144:147],a[180:183],v[112:115]
G v5,s[52:55],s77,0
M v[84:87],a[148:151],a[176:179],v[84:87]
T a[132:133],v29,0
C v45,v218,v219
M v[116:119],a[148:151],a[180:183],v[116:119]
T a[134:135],v29,1024
v_add_u32 v20,32768,v20
M v[88:91],a[152:155],a[176:179],v[88:91]
T a[136:137],v30,0
C v46,v220,v221
M v[120:123],a[152:155],a[180:183],v[120:123]
T a[138:139],v30,1024
T a[140:141],v31,0
C v47,v222,v223
M v[92:95],a[156:159],a[176:179],v[92:95]
G v6,s[52:55],s77,1024
M v[124:127],a[156:159],a[180:183],v[124:127]
WL 5
T a[142:143],v31,1024
T a[144:145],v32,0
AW a108,v44
M v[64:67],a[128:131],a[160:163],v[64:67]
T a[146:147],v32,1024
T a[148:149],v33,0
AW a109,v45
M v[96:99],a[128:131],a[164:167],v[96:99]
WL 7
G v7,s[52:55],s77,2048
M v[68:71],a[132:135],a[160:163],v[68:71]
T a[150:151],v33,1024
T a[152:153],v34,0
AW a110,v46
M v[100:103],a[132:135],a[164:167],v[100:103]
WL 7
G v8,s[52:55],s77,3072
M v[72:75],a[136:139],a[160:163],v[72:75]
s_add_u32 s77,s77,128
s_add_u32 m0,s68,49152
T a[154:155],v34,1024
T a[156:157],v35,0
AW a111,v47
M v[104:107],a[136:139],a[164:167],v[104:107]
WL 7
T a[158:159],v35,1024
D a[176:179],v20,0
C v40,v224,v225
M v[76:79],a[140:143],a[160:163],v[76:79]
D a[180:183],v20,2048
T a[128:129],v28,8192
C v41,v226,v227
M v[108:111],a[140:143],a[164:167],v[108:111]
WL 9
T a[130:131],v28,9216
T a[132:133],v29,8192
C v42,v228,v229
M v[80:83],a[144:147],a[160:163],v[80:83]
G v13,s[56:59],s78,0
M v[112:115],a[144:147],a[164:167],v[112:115]
WL 9
T a[134:135],v29,9216
T a[136:137],v30,8192
v_add_u32 v19,32768,v19
M v[84:87],a[148:151],a[160:163],v[84:87]
G v14,s[56:59],s78,1024
M v[116:119],a[148:151],a[164:167],v[116:119]
WL 9
T a[138:139],v30,9216
T a[140:141],v31,8192
v_add_u32 v28,32768,v28
M v[88:91],a[152:155],a[160:163],v[88:91]
G v15,s[56:59],s78,2048
M v[120:123],a[152:155],a[164:167],v[120:123]
WL 8
T a[142:143],v31,9216
T a[144:145],v32,8192
v_add_u32 v29,32768,v29
v_add_u32 v30,32768,v30
M v[92:95],a[156:159],a[160:163],v[92:95]
G v16,s[56:59],s78,3072
M v[124:127],a[156:159],a[164:167],v[124:127]
WL 6
T a[146:147],v32,9216
T a[148:149],v33,8192
v_add_u32 v31,32768,v31
v_add_u32 v32,32768,v32
M v[64:67],a[128:131],a[176:179],v[64:67]
s_add_u32 s78,s78,221184
s_and_b32 m0,s75,65535
T a[150:151],v33,9216
T a[152:153],v34,8192
v_add_u32 v33,32768,v33
C v43,v230,v231
M v[96:99],a[128:131],a[180:183],v[96:99]
WL 8
GS v0,s[36:39],s75,0
M v[68:71],a[132:135],a[176:179],v[68:71]
GS v0,s[36:39],s75,1024
M v[100:103],a[132:135],a[180:183],v[100:103]
WL 6
GS v0,s[36:39],s75,2048
M v[72:75],a[136:139],a[176:179],v[72:75]
GS v0,s[36:39],s75,3072
M v[104:107],a[136:139],a[180:183],v[104:107]
WL 4
T a[154:155],v34,9216
T a[156:157],v35,8192
v_add_u32 v34,32768,v34
AW a112,v40
M v[76:79],a[140:143],a[176:179],v[76:79]
s_add_u32 s75,s75,16384
s_and_b32 m0,s75,65535
T a[158:159],v35,9216
v_add_u32 v35,32768,v35
AW a113,v41
M v[108:111],a[140:143],a[180:183],v[108:111]
WL 5
GS v0,s[36:39],s75,0
s_waitcnt vmcnt(13) lgkmcnt(0)
s_barrier
M v[80:83],a[144:147],a[176:179],v[80:83]
GS v0,s[36:39],s75,1024
M v[112:115],a[144:147],a[180:183],v[112:115]
GS v0,s[36:39],s75,2048
D a[160:163],v19,0
D a[164:167],v19,2048
T a[128:129],v28,0
T a[130:131],v28,1024
M v[84:87],a[148:151],a[176:179],v[84:87]
GS v0,s[36:39],s75,3072
M v[116:119],a[148:151],a[180:183],v[116:119]
s_add_u32 s75,s75,16384
s_and_b32 m0,s75,65535
)"
R"(T a[132:133],v29,0
T a[134:135],v29,1024
AW a114,v42
AW a115,v43
M v[88:91],a[152:155],a[176:179],v[88:91]
GS v0,s[36:39],s75,0
M v[120:123],a[152:155],a[180:183],v[120:123]
GS v0,s[36:39],s75,1024
T a[136:137],v30,0
T a[138:139],v30,1024
M v[92:95],a[156:159],a[176:179],v[92:95]
GS v0,s[36:39],s75,2048
M v[124:127],a[156:159],a[180:183],v[124:127]
WL 4
GS v0,s[36:39],s75,3072
T a[140:141],v31,0
T a[142:143],v31,1024
M v[64:67],a[128:131],a[160:163],v[64:67]
s_add_u32 s75,s75,16384
s_and_b32 s74,s76,65535
s_or_b32 m0,s74,65536
T a[144:145],v32,0
T a[146:147],v32,1024
C v44,v232,v233
C v45,v234,v235
M v[96:99],a[128:131],a[164:167],v[96:99]
WL 6
GS v0,s[40:43],s76,0
M v[68:71],a[132:135],a[160:163],v[68:71]
T a[148:149],v33,0
T a[150:151],v33,1024
C v46,v236,v237
v_add_u32 v20,32768,v20
M v[100:103],a[132:135],a[164:167],v[100:103]
WL 6
GS v0,s[40:43],s76,1024
M v[72:75],a[136:139],a[160:163],v[72:75]
T a[152:153],v34,0
T a[154:155],v34,1024
C v47,v238,v239
AW a116,v44
M v[104:107],a[136:139],a[164:167],v[104:107]
WL 6
T a[156:157],v35,0
T a[158:159],v35,1024
AW a117,v45
M v[76:79],a[140:143],a[160:163],v[76:79]
GS v0,s[40:43],s76,2048
M v[108:111],a[140:143],a[164:167],v[108:111]
WL 6
D a[176:179],v20,0
D a[180:183],v20,2048
AW a118,v46
AW a119,v47
M v[80:83],a[144:147],a[160:163],v[80:83]
T a[128:129],v28,8192
T a[130:131],v28,9216
C v40,v240,v241
M v[112:115],a[144:147],a[164:167],v[112:115]
WL 8
GS v0,s[40:43],s76,3072
M v[84:87],a[148:151],a[160:163],v[84:87]
s_add_u32 s76,s76,16384
s_and_b32 s74,s76,65535
T a[132:133],v29,8192
T a[134:135],v29,9216
C v41,v242,v243
M v[116:119],a[148:151],a[164:167],v[116:119]
WL 8
T a[136:137],v30,8192
T a[138:139],v30,9216
C v42,v244,v245
M v[88:91],a[152:155],a[160:163],v[88:91]
s_or_b32 m0,s74,65536
T a[140:141],v31,8192
T a[142:143],v31,9216
v_add_u32 v19,32768,v19
v_add_u32 v28,4294901760,v28
M v[120:123],a[152:155],a[164:167],v[120:123]
WL 10
T a[144:145],v32,8192
T a[146:147],v32,9216
v_add_u32 v29,4294901760,v29
v_add_u32 v30,4294901760,v30
M v[92:95],a[156:159],a[160:163],v[92:95]
T a[148:149],v33,8192
v_add_u32 v31,4294901760,v31
v_add_u32 v32,4294901760,v32
M v[124:127],a[156:159],a[164:167],v[124:127]
WL 9
T a[150:151],v33,9216
v_add_u32 v33,4294901760,v33
C v43,v246,v247
M v[64:67],a[128:131],a[176:179],v[64:67]
T a[152:153],v34,8192
AW a120,v40
AW a121,v41
M v[96:99],a[128:131],a[180:183],v[96:99]
WL 9
T a[154:155],v34,9216
v_add_u32 v34,4294901760,v34
M v[68:71],a[132:135],a[176:179],v[68:71]
GS v0,s[40:43],s76,0
M v[100:103],a[132:135],a[180:183],v[100:103]
WL 8
T a[156:157],v35,8192
T a[158:159],v35,9216
v_add_u32 v35,4294901760,v35
M v[72:75],a[136:139],a[176:179],v[72:75]
WL 8
GS v0,s[40:43],s76,1024
M v[104:107],a[136:139],a[180:183],v[104:107]
WL 6
s_waitcnt vmcnt(18) lgkmcnt(0)
s_barrier
M v[76:79],a[140:143],a[176:179],v[76:79]
GS v0,s[40:43],s76,2048
M v[108:111],a[140:143],a[180:183],v[108:111]
GS v0,s[40:43],s76,3072
M v[80:83],a[144:147],a[176:179],v[80:83]
s_add_u32 s76,s76,16384
D a[160:163],v19,0
AW a122,v42
AW a123,v43
M v[112:115],a[144:147],a[180:183],v[112:115]
D a[164:167],v19,2048
C v44,v248,v249
T a[128:129],v28,0
T a[130:131],v28,1024
M v[84:87],a[148:151],a[176:179],v[84:87]
T a[132:133],v29,0
C v45,v250,v251
M v[116:119],a[148:151],a[180:183],v[116:119]
T a[134:135],v29,1024
C v46,v252,v253
M v[88:91],a[152:155],a[176:179],v[88:91]
T a[136:137],v30,0
v_add_u32 v20,32768,v20
M v[120:123],a[152:155],a[180:183],v[120:123]
T a[138:139],v30,1024
C v47,v254,v255
M v[92:95],a[156:159],a[176:179],v[92:95]
T a[140:141],v31,0
AW a124,v44
M v[124:127],a[156:159],a[180:183],v[124:127]
WL 5
T a[142:143],v31,1024
AW a125,v45
M v[64:67],a[128:131],a[160:163],v[64:67]
T a[144:145],v32,0
T a[146:147],v32,1024
AW a126,v46
M v[96:99],a[128:131],a[164:167],v[96:99]
WL 6
T a[148:149],v33,0
T a[150:151],v33,1024
AW a127,v47
M v[68:71],a[132:135],a[160:163],v[68:71]
T a[152:153],v34,0
T a[154:155],v34,1024
M v[100:103],a[132:135],a[164:167],v[100:103]
WL 8
T a[156:157],v35,0
T a[158:159],v35,1024
M v[72:75],a[136:139],a[160:163],v[72:75]
D a[176:179],v20,0
D a[180:183],v20,2048
M v[104:107],a[136:139],a[164:167],v[104:107]
WL 10
T a[128:129],v28,8192
M v[76:79],a[140:143],a[160:163],v[76:79]
T a[130:131],v28,9216
M v[108:111],a[140:143],a[164:167],v[108:111]
WL 10
T a[132:133],v29,8192
M v[80:83],a[144:147],a[160:163],v[80:83]
T a[134:135],v29,9216
M v[112:115],a[144:147],a[164:167],v[112:115]
WL 10
T a[136:137],v30,8192
M v[84:87],a[148:151],a[160:163],v[84:87]
WL 9
T a[138:139],v30,9216
M v[116:119],a[148:151],a[164:167],v[116:119]
WL 8
T a[140:141],v31,8192
T a[142:143],v31,9216
M v[88:91],a[152:155],a[160:163],v[88:91]
T a[144:145],v32,8192
M v[120:123],a[152:155],a[164:167],v[120:123]
T a[146:147],v32,9216
M v[92:95],a[156:159],a[160:163],v[92:95]
WL 8
T a[148:149],v33,8192
M v[124:127],a[156:159],a[164:167],v[124:127]
T a[150:151],v33,9216
M v[64:67],a[128:131],a[176:179],v[64:67]
WL 8
T a[152:153],v34,8192
M v[96:99],a[128:131],a[180:183],v[96:99]
T a[154:155],v34,9216
M v[68:71],a[132:135],a[176:179],v[68:71]
WL 8
T a[156:157],v35,8192
M v[100:103],a[132:135],a[180:183],v[100:103]
WL 7
T a[158:159],v35,9216
M v[72:75],a[136:139],a[176:179],v[72:75]
WL 0
s_barrier
M v[104:107],a[136:139],a[180:183],v[104:107]
M v[76:79],a[140:143],a[176:179],v[76:79]
M v[108:111],a[140:143],a[180:183],v[108:111]
M v[80:83],a[144:147],a[176:179],v[80:83]
M v[112:115],a[144:147],a[180:183],v[112:115]
M v[84:87],a[148:151],a[176:179],v[84:87]
M v[116:119],a[148:151],a[180:183],v[116:119]
M v[88:91],a[152:155],a[176:179],v[88:91]
M v[120:123],a[152:155],a[180:183],v[120:123]
M v[92:95],a[156:159],a[176:179],v[92:95]
M v[124:127],a[156:159],a[180:183],v[124:127]
s_nop 7
v_mov_b32 v40,16256
s_mov_b32 s64,1069066811
s_mov_b32 s65,1069066811
C v192,v64,v65
C v193,v66,v67
C v194,v68,v69
C v195,v70,v71
C v196,v72,v73
C v197,v74,v75
C v198,v76,v77
C v199,v78,v79
C v200,v80,v81
C v201,v82,v83
v_mov_b32 v202,0
v_mov_b32 v203,0
C v204,v96,v97
C v205,v98,v99
C v206,v100,v101
C v207,v102,v103
C v208,v104,v105
C v209,v106,v107
C v210,v108,v109
C v211,v110,v111
C v212,v112,v113
C v213,v114,v115
v_mov_b32 v214,0
v_mov_b32 v215,0
M v[216:219],a[0:3],v[192:195],0
C v84,v84,v85
M v[220:223],a[12:15],v[192:195],0
C v85,v86,v87
M v[224:227],a[24:27],v[192:195],0
ds_write_b64 v56,v[84:85] offset:0
M v[228:231],a[36:39],v[192:195],0
C v88,v88,v89
M v[232:235],a[48:51],v[192:195],0
C v89,v90,v91
M v[236:239],a[0:3],v[204:207],0
ds_write_b64 v56,v[88:89] offset:16
M v[240:243],a[12:15],v[204:207],0
C v92,v92,v93
M v[244:247],a[24:27],v[204:207],0
C v93,v94,v95
M v[248:251],a[36:39],v[204:207],0
v_cndmask_b32 v92,v92,v40,s[88:89]
M v[252:255],a[48:51],v[204:207],0
v_cndmask_b32 v93,v93,0,s[88:89]
M v[216:219],a[4:7],v[196:199],v[216:219]
ds_write_b64 v57,v[92:93] offset:64
M v[220:223],a[16:19],v[196:199],v[220:223]
C v116,v116,v117
M v[224:227],a[28:31],v[196:199],v[224:227]
C v117,v118,v119
M v[228:231],a[40:43],v[196:199],v[228:231]
ds_write_b64 v56,v[116:117] offset:1536
M v[232:235],a[52:55],v[196:199],v[232:235]
C v120,v120,v121
M v[236:239],a[4:7],v[208:211],v[236:239]
C v121,v122,v123
M v[240:243],a[16:19],v[208:211],v[240:243]
ds_write_b64 v56,v[120:121] offset:1552
M v[244:247],a[28:31],v[208:211],v[244:247]
C v124,v124,v125
M v[248:251],a[40:43],v[208:211],v[248:251]
C v125,v126,v127
M v[252:255],a[52:55],v[208:211],v[252:255]
v_cndmask_b32 v124,v124,v40,s[88:89]
M v[216:219],a[8:11],v[200:203],v[216:219]
v_cndmask_b32 v125,v125,0,s[88:89]
M v[220:223],a[20:23],v[200:203],v[220:223]
ds_write_b64 v57,v[124:125] offset:1600
M v[224:227],a[32:35],v[200:203],v[224:227]
M v[228:231],a[44:47],v[200:203],v[228:231]
M v[232:235],a[56:59],v[200:203],v[232:235]
M v[236:239],a[8:11],v[212:215],v[236:239]
M v[240:243],a[20:23],v[212:215],v[240:243]
M v[244:247],a[32:35],v[212:215],v[244:247]
M v[248:251],a[44:47],v[212:215],v[248:251]
M v[252:255],a[56:59],v[212:215],v[252:255]
v_max3_f32 v42,v216,v217,v218
v_min3_f32 v44,v216,v217,v218
v_max3_f32 v42,v42,v219,v220
v_min3_f32 v44,v44,v219,v220
v_max3_f32 v42,v42,v221,v222
v_min3_f32 v44,v44,v221,v222
v_max3_f32 v42,v42,v223,v224
v_min3_f32 v44,v44,v223,v224
v_max3_f32 v42,v42,v225,v226
v_min3_f32 v44,v44,v225,v226
v_max3_f32 v42,v42,v227,v228
v_min3_f32 v44,v44,v227,v228
v_max3_f32 v42,v42,v229,v230
v_min3_f32 v44,v44,v229,v230
v_max3_f32 v42,v42,v231,v232
v_min3_f32 v44,v44,v231,v232
v_max_f32 v42,v42,v233
v_min_f32 v44,v44,v233
v_mul_f32 v42,s64,v42
v_mul_f32 v44,s64,v44
v_pk_fma_f32 v[128:129],v[216:217],s[64:65],v[42:43] op_sel_hi:[1,1,0] neg_lo:[0,0,1] neg_hi:[0,0,1]
v_pk_fma_f32 v[146:147],v[216:217],s[64:65],v[44:45] op_sel_hi:[1,1,0] neg_lo:[1,0,0] neg_hi:[1,0,0]
v_pk_fma_f32 v[130:131],v[218:219],s[64:65],v[42:43] op_sel_hi:[1,1,0] neg_lo:[0,0,1] neg_hi:[0,0,1]
v_pk_fma_f32 v[148:149],v[218:219],s[64:65],v[44:45] op_sel_hi:[1,1,0] neg_lo:[1,0,0] neg_hi:[1,0,0]
v_pk_fma_f32 v[132:133],v[220:221],s[64:65],v[42:43] op_sel_hi:[1,1,0] neg_lo:[0,0,1] neg_hi:[0,0,1]
v_pk_fma_f32 v[150:151],v[220:221],s[64:65],v[44:45] op_sel_hi:[1,1,0] neg_lo:[1,0,0] neg_hi:[1,0,0]
v_pk_fma_f32 v[134:135],v[222:223],s[64:65],v[42:43] op_sel_hi:[1,1,0] neg_lo:[0,0,1] neg_hi:[0,0,1]
v_pk_fma_f32 v[152:153],v[222:223],s[64:65],v[44:45] op_sel_hi:[1,1,0] neg_lo:[1,0,0] neg_hi:[1,0,0]
v_pk_fma_f32 v[136:137],v[224:225],s[64:65],v[42:43] op_sel_hi:[1,1,0] neg_lo:[0,0,1] neg_hi:[0,0,1]
v_pk_fma_f32 v[154:155],v[224:225],s[64:65],v[44:45] op_sel_hi:[1,1,0] neg_lo:[1,0,0] neg_hi:[1,0,0]
v_pk_fma_f32 v[138:139],v[226:227],s[64:65],v[42:43] op_sel_hi:[1,1,0] neg_lo:[0,0,1] neg_hi:[0,0,1]
v_pk_fma_f32 v[156:157],v[226:227],s[64:65],v[44:45] op_sel_hi:[1,1,0] neg_lo:[1,0,0] neg_hi:[1,0,0]
v_pk_fma_f32 v[140:141],v[228:229],s[64:65],v[42:43] op_sel_hi:[1,1,0] neg_lo:[0,0,1] neg_hi:[0,0,1]
v_pk_fma_f32 v[158:159],v[228:229],s[64:65],v[44:45] op_sel_hi:[1,1,0] neg_lo:[1,0,0] neg_hi:[1,0,0]
v_pk_fma_f32 v[142:143],v[230:231],s[64:65],v[42:43] op_sel_hi:[1,1,0] neg_lo:[0,0,1] neg_hi:[0,0,1]
v_pk_fma_f32 v[160:161],v[230:231],s[64:65],v[44:45] op_sel_hi:[1,1,0] neg_lo:[1,0,0] neg_hi:[1,0,0]
v_pk_fma_f32 v[144:145],v[232:233],s[64:65],v[42:43] op_sel_hi:[1,1,0] neg_lo:[0,0,1] neg_hi:[0,0,1]
v_pk_fma_f32 v[162:163],v[232:233],s[64:65],v[44:45] op_sel_hi:[1,1,0] neg_lo:[1,0,0] neg_hi:[1,0,0]
E v128,v128
E v146,v146
E v129,v129
E v147,v147
E v130,v130
E v148,v148
E v131,v131
E v149,v149
E v132,v132
E v150,v150
E v133,v133
E v151,v151
E v134,v134
E v152,v152
E v135,v135
E v153,v153
E v136,v136
E v154,v154
E v137,v137
E v155,v155
E v138,v138
E v156,v156
E v139,v139
E v157,v157
E v140,v140
E v158,v158
E v141,v141
E v159,v159
E v142,v142
E v160,v160
E v143,v143
E v161,v161
E v144,v144
E v162,v162
E v145,v145
E v163,v163
v_pk_add_f32 v[46:47],v[128:129],v[130:131]
v_pk_add_f32 v[48:49],v[146:147],v[148:149]
v_pk_add_f32 v[46:47],v[46:47],v[132:133]
v_pk_add_f32 v[48:49],v[48:49],v[150:151]
v_pk_add_f32 v[46:47],v[46:47],v[134:135]
v_pk_add_f32 v[48:49],v[48:49],v[152:153]
v_pk_add_f32 v[46:47],v[46:47],v[136:137]
v_pk_add_f32 v[48:49],v[48:49],v[154:155]
v_pk_add_f32 v[46:47],v[46:47],v[138:139]
v_pk_add_f32 v[48:49],v[48:49],v[156:157]
v_pk_add_f32 v[46:47],v[46:47],v[140:141]
v_pk_add_f32 v[48:49],v[48:49],v[158:159]
v_pk_add_f32 v[46:47],v[46:47],v[142:143]
v_pk_add_f32 v[48:49],v[48:49],v[160:161]
v_pk_add_f32 v[46:47],v[46:47],v[144:145]
v_pk_add_f32 v[48:49],v[48:49],v[162:163]
v_add_f32 v46,v46,v47
v_add_f32 v48,v48,v49
v_rcp_f32 v46,v46
v_rcp_f32 v48,v48
v_pk_mul_f32 v[128:129],v[128:129],v[46:47] op_sel_hi:[1,0]
v_pk_mul_f32 v[146:147],v[146:147],v[48:49] op_sel_hi:[1,0]
v_pk_mul_f32 v[130:131],v[130:131],v[46:47] op_sel_hi:[1,0]
v_pk_mul_f32 v[148:149],v[148:149],v[48:49] op_sel_hi:[1,0]
v_pk_mul_f32 v[132:133],v[132:133],v[46:47] op_sel_hi:[1,0]
)"
R"(v_pk_mul_f32 v[150:151],v[150:151],v[48:49] op_sel_hi:[1,0]
v_pk_mul_f32 v[134:135],v[134:135],v[46:47] op_sel_hi:[1,0]
v_pk_mul_f32 v[152:153],v[152:153],v[48:49] op_sel_hi:[1,0]
v_pk_mul_f32 v[136:137],v[136:137],v[46:47] op_sel_hi:[1,0]
v_pk_mul_f32 v[154:155],v[154:155],v[48:49] op_sel_hi:[1,0]
v_pk_mul_f32 v[138:139],v[138:139],v[46:47] op_sel_hi:[1,0]
v_pk_mul_f32 v[156:157],v[156:157],v[48:49] op_sel_hi:[1,0]
v_pk_mul_f32 v[140:141],v[140:141],v[46:47] op_sel_hi:[1,0]
v_pk_mul_f32 v[158:159],v[158:159],v[48:49] op_sel_hi:[1,0]
v_pk_mul_f32 v[142:143],v[142:143],v[46:47] op_sel_hi:[1,0]
v_pk_mul_f32 v[160:161],v[160:161],v[48:49] op_sel_hi:[1,0]
v_pk_mul_f32 v[144:145],v[144:145],v[46:47] op_sel_hi:[1,0]
v_pk_mul_f32 v[162:163],v[162:163],v[48:49] op_sel_hi:[1,0]
C v164,v128,v129
C v173,v146,v147
C v165,v130,v131
C v174,v148,v149
C v166,v132,v133
C v175,v150,v151
C v167,v134,v135
C v176,v152,v153
C v168,v136,v137
C v177,v154,v155
C v169,v138,v139
C v178,v156,v157
C v170,v140,v141
C v179,v158,v159
C v171,v142,v143
C v180,v160,v161
C v172,v144,v145
C v181,v162,v163
v_mov_b32 v182,0
v_mov_b32 v183,0
s_mov_b64 exec,s[90:91]
ds_write_b128 v59,v[164:167] offset:0
ds_write_b128 v59,v[168:171] offset:16
ds_write_b128 v59,v[172:175] offset:32
ds_write_b128 v59,v[176:179] offset:48
ds_write_b128 v59,v[180:183] offset:64
s_mov_b64 exec,-1
buffer_store_dwordx4 v[164:167],v58,s[80:83],s28 offen offset:0 sc1
buffer_store_dwordx4 v[168:171],v58,s[80:83],s28 offen offset:256 sc1
buffer_store_dwordx4 v[172:175],v58,s[80:83],s28 offen offset:512 sc1
buffer_store_dwordx4 v[176:179],v58,s[80:83],s28 offen offset:768 sc1
buffer_store_dwordx4 v[180:183],v58,s[80:83],s28 offen offset:1024 sc1
v_max3_f32 v42,v236,v237,v238
v_min3_f32 v44,v236,v237,v238
v_max3_f32 v42,v42,v239,v240
v_min3_f32 v44,v44,v239,v240
v_max3_f32 v42,v42,v241,v242
v_min3_f32 v44,v44,v241,v242
v_max3_f32 v42,v42,v243,v244
v_min3_f32 v44,v44,v243,v244
v_max3_f32 v42,v42,v245,v246
v_min3_f32 v44,v44,v245,v246
v_max3_f32 v42,v42,v247,v248
v_min3_f32 v44,v44,v247,v248
v_max3_f32 v42,v42,v249,v250
v_min3_f32 v44,v44,v249,v250
v_max3_f32 v42,v42,v251,v252
v_min3_f32 v44,v44,v251,v252
v_max_f32 v42,v42,v253
v_min_f32 v44,v44,v253
v_mul_f32 v42,s64,v42
v_mul_f32 v44,s64,v44
v_pk_fma_f32 v[128:129],v[236:237],s[64:65],v[42:43] op_sel_hi:[1,1,0] neg_lo:[0,0,1] neg_hi:[0,0,1]
v_pk_fma_f32 v[146:147],v[236:237],s[64:65],v[44:45] op_sel_hi:[1,1,0] neg_lo:[1,0,0] neg_hi:[1,0,0]
v_pk_fma_f32 v[130:131],v[238:239],s[64:65],v[42:43] op_sel_hi:[1,1,0] neg_lo:[0,0,1] neg_hi:[0,0,1]
v_pk_fma_f32 v[148:149],v[238:239],s[64:65],v[44:45] op_sel_hi:[1,1,0] neg_lo:[1,0,0] neg_hi:[1,0,0]
v_pk_fma_f32 v[132:133],v[240:241],s[64:65],v[42:43] op_sel_hi:[1,1,0] neg_lo:[0,0,1] neg_hi:[0,0,1]
v_pk_fma_f32 v[150:151],v[240:241],s[64:65],v[44:45] op_sel_hi:[1,1,0] neg_lo:[1,0,0] neg_hi:[1,0,0]
v_pk_fma_f32 v[134:135],v[242:243],s[64:65],v[42:43] op_sel_hi:[1,1,0] neg_lo:[0,0,1] neg_hi:[0,0,1]
v_pk_fma_f32 v[152:153],v[242:243],s[64:65],v[44:45] op_sel_hi:[1,1,0] neg_lo:[1,0,0] neg_hi:[1,0,0]
v_pk_fma_f32 v[136:137],v[244:245],s[64:65],v[42:43] op_sel_hi:[1,1,0] neg_lo:[0,0,1] neg_hi:[0,0,1]
v_pk_fma_f32 v[154:155],v[244:245],s[64:65],v[44:45] op_sel_hi:[1,1,0] neg_lo:[1,0,0] neg_hi:[1,0,0]
v_pk_fma_f32 v[138:139],v[246:247],s[64:65],v[42:43] op_sel_hi:[1,1,0] neg_lo:[0,0,1] neg_hi:[0,0,1]
v_pk_fma_f32 v[156:157],v[246:247],s[64:65],v[44:45] op_sel_hi:[1,1,0] neg_lo:[1,0,0] neg_hi:[1,0,0]
v_pk_fma_f32 v[140:141],v[248:249],s[64:65],v[42:43] op_sel_hi:[1,1,0] neg_lo:[0,0,1] neg_hi:[0,0,1]
v_pk_fma_f32 v[158:159],v[248:249],s[64:65],v[44:45] op_sel_hi:[1,1,0] neg_lo:[1,0,0] neg_hi:[1,0,0]
v_pk_fma_f32 v[142:143],v[250:251],s[64:65],v[42:43] op_sel_hi:[1,1,0] neg_lo:[0,0,1] neg_hi:[0,0,1]
v_pk_fma_f32 v[160:161],v[250:251],s[64:65],v[44:45] op_sel_hi:[1,1,0] neg_lo:[1,0,0] neg_hi:[1,0,0]
v_pk_fma_f32 v[144:145],v[252:253],s[64:65],v[42:43] op_sel_hi:[1,1,0] neg_lo:[0,0,1] neg_hi:[0,0,1]
v_pk_fma_f32 v[162:163],v[252:253],s[64:65],v[44:45] op_sel_hi:[1,1,0] neg_lo:[1,0,0] neg_hi:[1,0,0]
E v128,v128
E v146,v146
E v129,v129
E v147,v147
E v130,v130
E v148,v148
E v131,v131
E v149,v149
E v132,v132
E v150,v150
E v133,v133
E v151,v151
E v134,v134
E v152,v152
E v135,v135
E v153,v153
E v136,v136
E v154,v154
E v137,v137
E v155,v155
E v138,v138
E v156,v156
E v139,v139
E v157,v157
E v140,v140
E v158,v158
E v141,v141
E v159,v159
E v142,v142
E v160,v160
E v143,v143
E v161,v161
E v144,v144
E v162,v162
E v145,v145
E v163,v163
v_pk_add_f32 v[46:47],v[128:129],v[130:131]
v_pk_add_f32 v[48:49],v[146:147],v[148:149]
v_pk_add_f32 v[46:47],v[46:47],v[132:133]
v_pk_add_f32 v[48:49],v[48:49],v[150:151]
v_pk_add_f32 v[46:47],v[46:47],v[134:135]
v_pk_add_f32 v[48:49],v[48:49],v[152:153]
v_pk_add_f32 v[46:47],v[46:47],v[136:137]
v_pk_add_f32 v[48:49],v[48:49],v[154:155]
v_pk_add_f32 v[46:47],v[46:47],v[138:139]
v_pk_add_f32 v[48:49],v[48:49],v[156:157]
v_pk_add_f32 v[46:47],v[46:47],v[140:141]
v_pk_add_f32 v[48:49],v[48:49],v[158:159]
v_pk_add_f32 v[46:47],v[46:47],v[142:143]
v_pk_add_f32 v[48:49],v[48:49],v[160:161]
v_pk_add_f32 v[46:47],v[46:47],v[144:145]
v_pk_add_f32 v[48:49],v[48:49],v[162:163]
v_add_f32 v46,v46,v47
v_add_f32 v48,v48,v49
v_rcp_f32 v46,v46
v_rcp_f32 v48,v48
v_pk_mul_f32 v[128:129],v[128:129],v[46:47] op_sel_hi:[1,0]
v_pk_mul_f32 v[146:147],v[146:147],v[48:49] op_sel_hi:[1,0]
v_pk_mul_f32 v[130:131],v[130:131],v[46:47] op_sel_hi:[1,0]
v_pk_mul_f32 v[148:149],v[148:149],v[48:49] op_sel_hi:[1,0]
v_pk_mul_f32 v[132:133],v[132:133],v[46:47] op_sel_hi:[1,0]
v_pk_mul_f32 v[150:151],v[150:151],v[48:49] op_sel_hi:[1,0]
v_pk_mul_f32 v[134:135],v[134:135],v[46:47] op_sel_hi:[1,0]
v_pk_mul_f32 v[152:153],v[152:153],v[48:49] op_sel_hi:[1,0]
v_pk_mul_f32 v[136:137],v[136:137],v[46:47] op_sel_hi:[1,0]
v_pk_mul_f32 v[154:155],v[154:155],v[48:49] op_sel_hi:[1,0]
v_pk_mul_f32 v[138:139],v[138:139],v[46:47] op_sel_hi:[1,0]
v_pk_mul_f32 v[156:157],v[156:157],v[48:49] op_sel_hi:[1,0]
v_pk_mul_f32 v[140:141],v[140:141],v[46:47] op_sel_hi:[1,0]
v_pk_mul_f32 v[158:159],v[158:159],v[48:49] op_sel_hi:[1,0]
v_pk_mul_f32 v[142:143],v[142:143],v[46:47] op_sel_hi:[1,0]
v_pk_mul_f32 v[160:161],v[160:161],v[48:49] op_sel_hi:[1,0]
v_pk_mul_f32 v[144:145],v[144:145],v[46:47] op_sel_hi:[1,0]
v_pk_mul_f32 v[162:163],v[162:163],v[48:49] op_sel_hi:[1,0]
C v192,v128,v129
C v201,v146,v147
C v193,v130,v131
C v202,v148,v149
C v194,v132,v133
C v203,v150,v151
C v195,v134,v135
C v204,v152,v153
C v196,v136,v137
C v205,v154,v155
C v197,v138,v139
C v206,v156,v157
C v198,v140,v141
C v207,v158,v159
C v199,v142,v143
C v208,v160,v161
C v200,v144,v145
C v209,v162,v163
v_mov_b32 v210,0
v_mov_b32 v211,0
s_mov_b64 exec,s[90:91]
ds_write_b128 v59,v[192:195] offset:1280
ds_write_b128 v59,v[196:199] offset:1296
ds_write_b128 v59,v[200:203] offset:1312
ds_write_b128 v59,v[204:207] offset:1328
ds_write_b128 v59,v[208:211] offset:1344
s_mov_b64 exec,-1
buffer_store_dwordx4 v[192:195],v58,s[80:83],s28 offen offset:1280 sc1
buffer_store_dwordx4 v[196:199],v58,s[80:83],s28 offen offset:1536 sc1
buffer_store_dwordx4 v[200:203],v58,s[80:83],s28 offen offset:1792 sc1
buffer_store_dwordx4 v[204:207],v58,s[80:83],s28 offen offset:2048 sc1
buffer_store_dwordx4 v[208:211],v58,s[80:83],s28 offen offset:2304 sc1
WL 0
s_barrier
T a[204:205],v61,0
T a[206:207],v61,384
T a[192:193],v60,0
T a[194:195],v60,320
T a[196:197],v60,32
T a[198:199],v60,352
T a[200:201],v60,64
T a[202:203],v60,384
T a[220:221],v61,3072
T a[222:223],v61,3456
T a[208:209],v60,2560
T a[210:211],v60,2880
T a[212:213],v60,2592
T a[214:215],v60,2912
T a[216:217],v60,2624
T a[218:219],v60,2944
T a[236:237],v61,6144
T a[238:239],v61,6528
T a[224:225],v60,5120
T a[226:227],v60,5440
T a[228:229],v60,5152
T a[230:231],v60,5472
T a[232:233],v60,5184
T a[234:235],v60,5504
T a[252:253],v61,9216
T a[254:255],v61,9600
T a[240:241],v60,7680
T a[242:243],v60,8000
T a[244:245],v60,7712
T a[246:247],v60,8032
T a[248:249],v60,7744
T a[250:251],v60,8064
WL 15
M v[216:219],a[192:195],a[204:207],0
M v[220:223],a[196:199],a[204:207],0
M v[224:227],a[200:203],a[204:207],0
M v[216:219],a[208:211],a[220:223],v[216:219]
M v[220:223],a[212:215],a[220:223],v[220:223]
M v[224:227],a[216:219],a[220:223],v[224:227]
WL 12
M v[216:219],a[224:227],a[236:239],v[216:219]
WL 10
M v[220:223],a[228:231],a[236:239],v[220:223]
WL 8
M v[224:227],a[232:235],a[236:239],v[224:227]
WL 4
M v[216:219],a[240:243],a[252:255],v[216:219]
WL 2
M v[220:223],a[244:247],a[252:255],v[220:223]
WL 0
M v[224:227],a[248:251],a[252:255],v[224:227]
s_barrier
s_and_b32 s74,s76,65535
s_or_b32 m0,s74,65536
s_nop 0
GS v0,s[40:43],s76,0
GS v0,s[40:43],s76,1024
GS v0,s[40:43],s76,2048
GS v0,s[40:43],s76,3072
s_add_u32 s76,s76,16384
buffer_store_dwordx4 v[216:219],v62,s[84:87],s29 offen offset:0 sc1
s_add_u32 s29,s29,s30
buffer_store_dwordx4 v[220:223],v62,s[84:87],s29 offen offset:0 sc1
s_add_u32 s29,s29,s30
buffer_store_dwordx4 v[224:227],v63,s[84:87],s29 offen offset:0 sc1
s_and_b32 s21,%2,7
s_mul_i32 s21,s21,30
s_lshr_b32 s22,%2,3
s_add_u32 s21,s21,s22
s_cmp_ge_u32 s22,30
s_cselect_b32 s21,255,s21
s_mov_b32 s73,s21
s_min_u32 s21,s21,233
s_mul_i32 s70,s21,57
s_lshr_b32 s70,s70,9
s_mul_i32 s22,s70,9
s_sub_u32 s71,s21,s22
s_lshl_b32 s24,s71,7
s_mul_i32 s64,s20,1179648
s_add_u32 s64,s64,s24
s_mul_i32 s68,s20,24576
s_load_dwordx2 s[88:89],%0,40
s_mov_b32 s90,4718592
s_mov_b32 s91,131072
s_load_dwordx2 s[28:29],%0,72
s_mov_b32 s30,663552
s_mov_b32 s31,131072
s_lshr_b32 s24,%2,3
s_lshl_b32 s24,s24,2
s_add_u32 s24,s24,s20
s_lshl_b32 s24,s24,13
s_mul_i32 s23,s70,43
s_lshr_b32 s23,s23,7
s_mul_i32 s23,s23,3
s_sub_u32 s23,s70,s23
s_mul_i32 s23,s23,36864
s_add_u32 s23,s23,s64
s_load_dwordx2 s[48:49],%0,88
v_lshrrev_b32 v7,4,v0
v_and_b32 v5,15,v7
v_lshrrev_b32 v6,4,v7
v_lshlrev_b32 v6,2,v6
s_sub_u32 s69,15,s27
s_lshl_b32 s69,s69,2
s_add_u32 s69,s69,s20
s_lshl_b32 s79,s27,2
s_add_u32 s79,s79,s20
s_add_u32 s59,s69,1
s_lshl_b32 s59,s59,13
s_add_u32 s58,s79,1
s_lshl_b32 s58,s58,13
s_lshl_b32 s67,s69,18
s_lshl_b32 s66,s79,18
s_lshl_b32 s74,s35,12
s_add_u32 s67,s67,s74
s_add_u32 s66,s66,s74
v_add_u32 v4,65536,v0
v_add_u32 v7,0,v6
v_cmp_gt_u32 s[44:45],v7,v5
v_add_u32 v7,1,v6
v_cmp_gt_u32 s[46:47],v7,v5
v_add_u32 v7,2,v6
v_cmp_gt_u32 s[54:55],v7,v5
v_add_u32 v7,3,v6
v_cmp_gt_u32 s[56:57],v7,v5
v_mov_b32 v16,1065369472
v_mov_b32 v17,1065369472
v_mov_b32 v18,1065369472
v_mov_b32 v19,1065369472
s_mov_b32 s61,1568669696
s_mov_b32 s62,3256877056
s_mov_b32 s65,4286578688
s_mov_b32 s52,8192
s_mov_b32 s53,0
v_add_u32 v2,8192,v0
s_lshl_b32 s60,s27,1
WL 0
s_add_u32 s48,s48,33554432
s_addc_u32 s49,s49,0
s_and_b32 s49,s49,65535
s_mov_b32 s50,16777216
s_mov_b32 s51,131072
s_and_b32 s89,s89,65535
s_and_b32 s29,s29,65535
v_bfe_u32 v239,v0,4,5
v_lshrrev_b32 v230,9,v0
v_mul_u32_u24 v230,96,v230
v_add_u32 v239,v239,v230
v_mul_u32_u24 v239,1152,v239
s_add_u32 s8,s88,s23
s_addc_u32 s9,s89,0
s_mov_b32 s10,0
s_mov_b32 s11,131072
s_mov_b32 s25,4292976640
v_and_b32 v7,240,v0
v_add_u32 v230,1024,v7
v_cmp_gt_u32 vcc,256,v0
s_nop 1
v_cndmask_b32 v7,v7,v230,vcc
v_bfe_u32 v230,v0,4,4
s_lshr_b32 s21,s35,4
s_and_b32 s22,s35,14
s_lshl_b32 s22,s22,2
s_add_u32 s21,s21,s22
v_lshrrev_b32 v231,2,v230
v_lshlrev_b32 v231,1,v231
v_and_b32 v230,3,v230
v_lshl_add_u32 v231,v230,6,v231
v_add_u32 v231,s21,v231
v_lshlrev_b32 v5,6,v231
s_add_u32 s84,s94,16384
s_addc_u32 s85,s95,0
s_and_b32 s85,s85,65535
s_mov_b32 s86,16384
s_mov_b32 s63,0
s_mov_b32 s77,0
s_mov_b32 s26,1073741824
s_mov_b32 s78,0
v_mov_b32 v244,0
v_mov_b32 v245,0
v_mov_b32 v246,0
v_mov_b32 v247,0
WV 21
s_barrier
s_nop 1
D a[128:131],v0,0
D a[132:135],v0,1024
D a[136:139],v0,2048
D a[140:143],v0,3072
D a[144:147],v0,4096
D a[148:151],v0,5120
D a[152:155],v0,6144
D a[156:159],v0,7168
WL 4
M v[68:71],a[128:131],a[64:67],0
AW a0,0
AW a1,0
AW a2,0
AW a3,0
M v[68:71],a[132:135],a[68:71],v[68:71]
AW a4,0
AW a5,0
AW a6,0
AW a7,0
M v[68:71],a[136:139],a[72:75],v[68:71]
AW a8,0
AW a9,0
AW a10,0
AW a11,0
)"
R"(M v[68:71],a[140:143],a[76:79],v[68:71]
AW a12,0
AW a13,0
AW a14,0
AW a15,0
M v[72:75],a[128:131],a[80:83],0
AW a16,0
AW a17,0
AW a18,0
AW a19,0
M v[72:75],a[132:135],a[84:87],v[72:75]
AW a20,0
AW a21,0
AW a22,0
AW a23,0
M v[72:75],a[136:139],a[88:91],v[72:75]
AW a24,0
AW a25,0
AW a26,0
AW a27,0
M v[72:75],a[140:143],a[92:95],v[72:75]
AW a28,0
AW a29,0
AW a30,0
AW a31,0
M v[76:79],a[128:131],a[96:99],0
AW a32,0
AW a33,0
AW a34,0
AW a35,0
M v[76:79],a[132:135],a[100:103],v[76:79]
AW a36,0
AW a37,0
AW a38,0
AW a39,0
M v[76:79],a[136:139],a[104:107],v[76:79]
AW a40,0
AW a41,0
AW a42,0
AW a43,0
M v[76:79],a[140:143],a[108:111],v[76:79]
AW a44,0
AW a45,0
AW a46,0
AW a47,0
M v[80:83],a[128:131],a[112:115],0
AW a48,0
AW a49,0
AW a50,0
AW a51,0
M v[80:83],a[132:135],a[116:119],v[80:83]
AW a52,0
AW a53,0
AW a54,0
AW a55,0
M v[80:83],a[136:139],a[120:123],v[80:83]
AW a56,0
AW a57,0
AW a58,0
AW a59,0
M v[80:83],a[140:143],a[124:127],v[80:83]
AW a60,0
AW a61,0
AW a62,0
AW a63,0
WL 0
M v[84:87],a[144:147],a[64:67],0
v_mov_b32 v164,0
v_mov_b32 v165,0
v_mov_b32 v166,0
v_mov_b32 v167,0
M v[84:87],a[148:151],a[68:71],v[84:87]
v_mov_b32 v168,0
v_mov_b32 v169,0
v_mov_b32 v170,0
v_mov_b32 v171,0
M v[84:87],a[152:155],a[72:75],v[84:87]
v_mov_b32 v172,0
v_mov_b32 v173,0
v_mov_b32 v174,0
v_mov_b32 v175,0
M v[84:87],a[156:159],a[76:79],v[84:87]
v_mov_b32 v176,0
v_mov_b32 v177,0
v_mov_b32 v178,0
v_mov_b32 v179,0
D a[128:131],v2,0
D a[132:135],v2,1024
D a[136:139],v2,2048
D a[140:143],v2,3072
M v[88:91],a[144:147],a[80:83],0
v_mov_b32 v180,0
v_mov_b32 v181,0
v_mov_b32 v182,0
v_mov_b32 v183,0
M v[88:91],a[148:151],a[84:87],v[88:91]
v_mov_b32 v184,0
v_mov_b32 v185,0
v_mov_b32 v186,0
v_mov_b32 v187,0
M v[88:91],a[152:155],a[88:91],v[88:91]
v_mov_b32 v188,0
v_mov_b32 v189,0
v_mov_b32 v190,0
v_mov_b32 v191,0
M v[88:91],a[156:159],a[92:95],v[88:91]
v_mov_b32 v192,0
v_mov_b32 v193,0
v_mov_b32 v194,0
v_mov_b32 v195,0
M v[92:95],a[144:147],a[96:99],0
v_mov_b32 v196,0
v_mov_b32 v197,0
v_mov_b32 v198,0
v_mov_b32 v199,0
M v[92:95],a[148:151],a[100:103],v[92:95]
v_mov_b32 v200,0
v_mov_b32 v201,0
v_mov_b32 v202,0
v_mov_b32 v203,0
M v[92:95],a[152:155],a[104:107],v[92:95]
v_mov_b32 v204,0
v_mov_b32 v205,0
v_mov_b32 v206,0
v_mov_b32 v207,0
M v[92:95],a[156:159],a[108:111],v[92:95]
v_mov_b32 v208,0
v_mov_b32 v209,0
v_mov_b32 v210,0
v_mov_b32 v211,0
M v[96:99],a[144:147],a[112:115],0
v_mov_b32 v212,0
v_mov_b32 v213,0
v_mov_b32 v214,0
v_mov_b32 v215,0
M v[96:99],a[148:151],a[116:119],v[96:99]
v_mov_b32 v216,0
v_mov_b32 v217,0
v_mov_b32 v218,0
v_mov_b32 v219,0
M v[96:99],a[152:155],a[120:123],v[96:99]
v_mov_b32 v220,0
v_mov_b32 v221,0
v_mov_b32 v222,0
v_mov_b32 v223,0
M v[96:99],a[156:159],a[124:127],v[96:99]
v_mov_b32 v224,0
v_mov_b32 v225,0
v_mov_b32 v226,0
v_mov_b32 v227,0
s_cmp_ge_u32 s52,s58
s_cbranch_scc1 f_mask_P
f_rm_P:
s_nop 7
v_max3_f32 v229,v68,v69,v70
v_max3_f32 v229,v229,v71,v84
v_max3_f32 v229,v229,v85,v86
v_max_f32 v229,v229,v87
v_max3_f32 v230,v72,v73,v74
v_max3_f32 v230,v230,v75,v88
v_max3_f32 v230,v230,v89,v90
v_max_f32 v230,v230,v91
v_max3_f32 v231,v76,v77,v78
v_max3_f32 v231,v231,v79,v92
v_max3_f32 v231,v231,v93,v94
v_max_f32 v231,v231,v95
v_max3_f32 v232,v80,v81,v82
v_max3_f32 v232,v232,v83,v96
v_max3_f32 v232,v232,v97,v98
v_max_f32 v232,v232,v99
v_mov_b32 v233,v229
s_nop 1
v_permlane32_swap_b32 v229,v233
v_max_f32 v229,v229,v233
v_mov_b32 v233,v229
s_nop 1
v_permlane16_swap_b32 v229,v233
v_max_f32 v229,v229,v233
v_mov_b32 v234,v230
s_nop 1
v_permlane32_swap_b32 v230,v234
v_max_f32 v230,v230,v234
v_mov_b32 v234,v230
s_nop 1
v_permlane16_swap_b32 v230,v234
v_max_f32 v230,v230,v234
v_mov_b32 v235,v231
s_nop 1
v_permlane32_swap_b32 v231,v235
v_max_f32 v231,v231,v235
v_mov_b32 v235,v231
s_nop 1
v_permlane16_swap_b32 v231,v235
v_max_f32 v231,v231,v235
v_mov_b32 v236,v232
s_nop 1
v_permlane32_swap_b32 v232,v236
v_max_f32 v232,v232,v236
v_mov_b32 v236,v232
s_nop 1
v_permlane16_swap_b32 v232,v236
v_max_f32 v232,v232,v236
v_add_f32 v229,1109393408,v229
v_sub_f32 v20,0,v229
v_sub_f32 v21,0,v229
v_sub_f32 v22,0,v229
v_sub_f32 v23,0,v229
v_sub_f32 v68,v68,v229
v_sub_f32 v69,v69,v229
v_sub_f32 v70,v70,v229
v_sub_f32 v71,v71,v229
v_sub_f32 v84,v84,v229
v_sub_f32 v85,v85,v229
v_sub_f32 v86,v86,v229
v_sub_f32 v87,v87,v229
v_add_f32 v230,1109393408,v230
v_sub_f32 v24,0,v230
v_sub_f32 v25,0,v230
v_sub_f32 v26,0,v230
v_sub_f32 v27,0,v230
v_sub_f32 v72,v72,v230
v_sub_f32 v73,v73,v230
v_sub_f32 v74,v74,v230
v_sub_f32 v75,v75,v230
v_sub_f32 v88,v88,v230
v_sub_f32 v89,v89,v230
v_sub_f32 v90,v90,v230
v_sub_f32 v91,v91,v230
v_add_f32 v231,1109393408,v231
v_sub_f32 v28,0,v231
v_sub_f32 v29,0,v231
v_sub_f32 v30,0,v231
v_sub_f32 v31,0,v231
v_sub_f32 v76,v76,v231
v_sub_f32 v77,v77,v231
v_sub_f32 v78,v78,v231
v_sub_f32 v79,v79,v231
v_sub_f32 v92,v92,v231
v_sub_f32 v93,v93,v231
v_sub_f32 v94,v94,v231
v_sub_f32 v95,v95,v231
v_add_f32 v232,1109393408,v232
v_sub_f32 v32,0,v232
v_sub_f32 v33,0,v232
v_sub_f32 v34,0,v232
v_sub_f32 v35,0,v232
v_sub_f32 v80,v80,v232
v_sub_f32 v81,v81,v232
v_sub_f32 v82,v82,v232
v_sub_f32 v83,v83,v232
v_sub_f32 v96,v96,v232
v_sub_f32 v97,v97,v232
v_sub_f32 v98,v98,v232
v_sub_f32 v99,v99,v232
s_waitcnt vmcnt(21) lgkmcnt(0)
s_barrier
M v[100:103],a[128:131],a[64:67],v[20:23]
s_add_u32 s52,s52,8192
E v8,v68
M v[100:103],a[132:135],a[68:71],v[100:103]
s_and_b32 s69,s52,65535
E v9,v69
M v[100:103],a[136:139],a[72:75],v[100:103]
s_and_b32 s79,s53,65535
E v10,v70
D a[144:147],v2,4096
C v132,v8,v9
v_or_b32 v1,s69,v0
M v[100:103],a[140:143],a[76:79],v[100:103]
s_and_b32 m0,s75,65535
E v11,v71
M v[104:107],a[128:131],a[80:83],v[24:27]
GS v0,s[36:39],s75,0
E v12,v72
M v[104:107],a[132:135],a[84:87],v[104:107]
D a[148:151],v2,5120
C v133,v10,v11
v_or_b32 v3,s79,v4
M v[104:107],a[136:139],a[88:91],v[104:107]
E v13,v73
E v14,v74
M v[104:107],a[140:143],a[92:95],v[104:107]
D a[152:155],v2,6144
C v136,v12,v13
M v[108:111],a[128:131],a[96:99],v[28:31]
WL 3
E v15,v75
E v8,v76
M v[108:111],a[132:135],a[100:103],v[108:111]
D a[156:159],v2,7168
C v137,v14,v15
M v[108:111],a[136:139],a[104:107],v[108:111]
GS v0,s[36:39],s75,1024
E v9,v77
M v[108:111],a[140:143],a[108:111],v[108:111]
E v10,v78
M v[112:115],a[128:131],a[112:115],v[32:35]
E v11,v79
E v12,v80
M v[112:115],a[132:135],a[116:119],v[112:115]
WL 2
D a[192:195],v3,0
C v140,v8,v9
C v141,v10,v11
M v[112:115],a[136:139],a[120:123],v[112:115]
D a[196:199],v3,1024
D a[200:203],v3,2048
M v[112:115],a[140:143],a[124:127],v[112:115]
WL 4
D a[204:207],v3,3072
M v[116:119],a[144:147],a[64:67],v[20:23]
D a[208:211],v3,4096
D a[212:215],v3,5120
M v[116:119],a[148:151],a[68:71],v[116:119]
WL 6
D a[216:219],v3,6144
M v[116:119],a[152:155],a[72:75],v[116:119]
D a[220:223],v3,7168
E v13,v81
M v[116:119],a[156:159],a[76:79],v[116:119]
E v14,v82
M v[120:123],a[144:147],a[80:83],v[24:27]
E v15,v83
GS v0,s[36:39],s75,2048
M v[120:123],a[148:151],a[84:87],v[120:123]
WL 7
E v8,v84
M v[120:123],a[152:155],a[88:91],v[120:123]
E v9,v85
E v10,v86
M v[120:123],a[156:159],a[92:95],v[120:123]
E v11,v87
M v[124:127],a[144:147],a[96:99],v[28:31]
GS v0,s[36:39],s75,3072
D a[128:131],v1,0
C v144,v12,v13
C v145,v14,v15
M v[124:127],a[148:151],a[100:103],v[124:127]
s_add_u32 s75,s75,16384
E v12,v88
M v[124:127],a[152:155],a[104:107],v[124:127]
E v13,v89
E v14,v90
M v[124:127],a[156:159],a[108:111],v[124:127]
E v15,v91
M v[128:131],a[144:147],a[112:115],v[32:35]
D a[132:135],v1,1024
C v134,v8,v9
E v8,v92
M v[128:131],a[148:151],a[116:119],v[128:131]
E v9,v93
M v[128:131],a[152:155],a[120:123],v[128:131]
WL 2
D a[136:139],v1,2048
C v135,v10,v11
E v10,v94
M v[128:131],a[156:159],a[124:127],v[128:131]
E v11,v95
M v[36:39],v[16:19],v[132:135],0
D a[140:143],v1,3072
C v138,v12,v13
C v139,v14,v15
C v142,v8,v9
C v143,v10,v11
M v[40:43],v[16:19],v[136:139],0
E v12,v96
M v[44:47],v[16:19],v[140:143],0
E v13,v97
E v14,v98
C v146,v12,v13
WV 4
s_barrier
E v15,v99
s_nop 0
C v147,v14,v15
s_nop 1
M v[48:51],v[16:19],v[144:147],0
WL 0
s_lshl_b32 s21,%2,2
s_add_u32 s21,s21,s20
s_mul_i32 s22,s21,4608
s_mov_b32 m0,147456
s_nop 0
G v0,s[88:91],s22,0
G v0,s[88:91],s22,1024
G v0,s[88:91],s22,2048
G v0,s[88:91],s22,3072
s_add_u32 s22,s22,3584
G v0,s[88:91],s22,0
s_mul_i32 s22,s21,648
G v0,s[28:31],s22,0
s_cmp_lg_u32 s20,0
s_cbranch_scc1 f_pb2
s_lshl_b32 s92,%2,6
s_add_u32 s92,s92,16384
v_mov_b32 v242,s92
v_mov_b32 v240,s96
v_mov_b32 v241,s97
s_mov_b64 exec,1
global_store_dwordx2 v242,v[240:241],s[94:95] sc1
)"
R"(s_mov_b64 exec,-1
f_pb2:
.p2align 6
f_LA:
M a[0:3],a[192:195],v[132:135],a[0:3]
s_cmp_eq_u32 s63,3
s_cbranch_scc1 f_kpA
f_krA:
s_cmp_ge_u32 s52,s58
s_cbranch_scc1 f_mask_Ao
f_rm_Ao:
M a[32:35],a[192:195],v[136:139],a[32:35]
s_add_u32 s52,s52,8192
E v8,v100
M v[164:167],a[192:195],v[140:143],v[164:167]
s_and_b32 s69,s52,65535
E v9,v101
M v[196:199],a[192:195],v[144:147],v[196:199]
s_add_u32 s53,s53,8192
E v10,v102
M v[68:71],a[128:131],a[64:67],v[20:23]
s_and_b32 s79,s53,65535
E v11,v103
M v[68:71],a[132:135],a[68:71],v[68:71]
s_and_b32 s74,s76,65535
E v12,v104
M v[68:71],a[136:139],a[72:75],v[68:71]
s_or_b32 m0,s74,65536
E v13,v105
M v[68:71],a[140:143],a[76:79],v[68:71]
GS v0,s[40:43],s76,0
M a[4:7],a[196:199],v[132:135],a[4:7]
WL 0
E v14,v106
M a[36:39],a[196:199],v[136:139],a[36:39]
E v15,v107
M v[168:171],a[196:199],v[140:143],v[168:171]
D a[144:147],v1,4096
v_or_b32 v2,s69,v0
M v[200:203],a[196:199],v[144:147],v[200:203]
D a[148:151],v1,5120
v_or_b32 v3,s79,v4
M v[72:75],a[128:131],a[80:83],v[24:27]
D a[152:155],v1,6144
C v148,v8,v9
M v[72:75],a[132:135],a[84:87],v[72:75]
E v8,v108
M v[72:75],a[136:139],a[88:91],v[72:75]
D a[156:159],v1,7168
C v149,v10,v11
M v[72:75],a[140:143],a[92:95],v[72:75]
E v9,v109
M a[8:11],a[200:203],v[132:135],a[8:11]
E v10,v110
M a[40:43],a[200:203],v[136:139],a[40:43]
D a[192:195],v3,0
C v152,v12,v13
M v[172:175],a[200:203],v[140:143],v[172:175]
GS v0,s[40:43],s76,1024
M v[204:207],a[200:203],v[144:147],v[204:207]
E v11,v111
M v[76:79],a[128:131],a[96:99],v[28:31]
E v12,v112
M v[76:79],a[132:135],a[100:103],v[76:79]
WL 2
E v13,v113
M v[76:79],a[136:139],a[104:107],v[76:79]
D a[196:199],v3,1024
C v153,v14,v15
M v[76:79],a[140:143],a[108:111],v[76:79]
E v14,v114
M a[12:15],a[204:207],v[132:135],a[12:15]
E v15,v115
M a[44:47],a[204:207],v[136:139],a[44:47]
GS v0,s[40:43],s76,2048
M v[176:179],a[204:207],v[140:143],v[176:179]
D a[200:203],v3,2048
C v156,v8,v9
M v[208:211],a[204:207],v[144:147],v[208:211]
E v8,v116
M v[80:83],a[128:131],a[112:115],v[32:35]
E v9,v117
M v[80:83],a[132:135],a[116:119],v[80:83]
D a[204:207],v3,3072
C v157,v10,v11
M v[80:83],a[136:139],a[120:123],v[80:83]
E v10,v118
M v[80:83],a[140:143],a[124:127],v[80:83]
E v11,v119
M a[16:19],a[208:211],v[132:135],a[16:19]
D a[128:131],v2,0
C v160,v12,v13
M a[48:51],a[208:211],v[136:139],a[48:51]
E v12,v120
M v[180:183],a[208:211],v[140:143],v[180:183]
WL 2
E v13,v121
M v[212:215],a[208:211],v[144:147],v[212:215]
D a[132:135],v2,1024
C v161,v14,v15
M v[84:87],a[144:147],a[64:67],v[20:23]
E v14,v122
M v[84:87],a[148:151],a[68:71],v[84:87]
E v15,v123
M v[84:87],a[152:155],a[72:75],v[84:87]
D a[136:139],v2,2048
C v150,v8,v9
M v[84:87],a[156:159],a[76:79],v[84:87]
E v8,v124
M a[20:23],a[212:215],v[132:135],a[20:23]
E v9,v125
M a[52:55],a[212:215],v[136:139],a[52:55]
GS v0,s[40:43],s76,3072
M v[184:187],a[212:215],v[140:143],v[184:187]
s_add_u32 s76,s76,16384
D a[140:143],v2,3072
C v151,v10,v11
M v[216:219],a[212:215],v[144:147],v[216:219]
E v10,v126
M v[52:55],v[16:19],v[148:151],v[36:39]
E v11,v127
M v[88:91],a[144:147],a[80:83],v[24:27]
D a[208:211],v3,4096
C v154,v12,v13
M v[88:91],a[148:151],a[84:87],v[88:91]
E v12,v128
M v[88:91],a[152:155],a[88:91],v[88:91]
E v13,v129
M v[88:91],a[156:159],a[92:95],v[88:91]
WL 2
D a[212:215],v3,5120
C v155,v14,v15
M a[24:27],a[216:219],v[132:135],a[24:27]
E v14,v130
M v[56:59],v[16:19],v[152:155],v[40:43]
E v15,v131
M a[56:59],a[216:219],v[136:139],a[56:59]
C v158,v8,v9
M v[188:191],a[216:219],v[140:143],v[188:191]
C v159,v10,v11
M v[220:223],a[216:219],v[144:147],v[220:223]
D a[216:219],v3,6144
C v162,v12,v13
M v[60:63],v[16:19],v[156:159],v[44:47]
C v163,v14,v15
M v[92:95],a[144:147],a[96:99],v[28:31]
v_add_f32 v248,v248,v252
M v[64:67],v[16:19],v[160:163],v[48:51]
v_add_f32 v249,v249,v253
M v[92:95],a[148:151],a[100:103],v[92:95]
v_add_f32 v250,v250,v254
M v[92:95],a[152:155],a[104:107],v[92:95]
v_add_f32 v251,v251,v255
M v[92:95],a[156:159],a[108:111],v[92:95]
M a[28:31],a[220:223],v[132:135],a[28:31]
M a[60:63],a[220:223],v[136:139],a[60:63]
v_max3_f32 v228,v52,v56,v60
M v[192:195],a[220:223],v[140:143],v[192:195]
WV 4
s_barrier
M v[224:227],a[220:223],v[144:147],v[224:227]
buffer_load_dwordx4 v[252:255],v5,s[84:87],s77 offen offset:0 sc1
M v[96:99],a[144:147],a[112:115],v[32:35]
WL 0
D a[220:223],v3,7168
v_max_f32 v228,v228,v64
M v[96:99],a[148:151],a[116:119],v[96:99]
s_add_u32 s77,s77,s78
v_cmp_lt_f32 vcc,s61,v228
M v[96:99],a[152:155],a[120:123],v[96:99]
s_cbranch_vccnz f_slow_Ao
f_rs_Ao:
M v[96:99],a[156:159],a[124:127],v[96:99]
WL 1
s_add_u32 s63,s63,1
M a[0:3],a[192:195],v[148:151],a[0:3]
s_cmp_ge_u32 s52,s58
s_cbranch_scc1 f_mask_Ae
f_rm_Ae:
M a[32:35],a[192:195],v[152:155],a[32:35]
s_add_u32 s52,s52,8192
E v8,v68
M v[164:167],a[192:195],v[156:159],v[164:167]
s_and_b32 s69,s52,65535
E v9,v69
M v[196:199],a[192:195],v[160:163],v[196:199]
s_add_u32 s53,s53,8192
E v10,v70
M v[100:103],a[128:131],a[64:67],v[20:23]
s_and_b32 s79,s53,65535
E v11,v71
M v[100:103],a[132:135],a[68:71],v[100:103]
s_and_b32 m0,s75,65535
E v12,v72
M v[100:103],a[136:139],a[72:75],v[100:103]
GS v0,s[36:39],s75,0
M v[100:103],a[140:143],a[76:79],v[100:103]
E v13,v73
M a[4:7],a[196:199],v[148:151],a[4:7]
WL 0
E v14,v74
M a[36:39],a[196:199],v[152:155],a[36:39]
E v15,v75
M v[168:171],a[196:199],v[156:159],v[168:171]
D a[144:147],v2,4096
v_or_b32 v1,s69,v0
M v[200:203],a[196:199],v[160:163],v[200:203]
D a[148:151],v2,5120
v_or_b32 v3,s79,v4
M v[104:107],a[128:131],a[80:83],v[24:27]
D a[152:155],v2,6144
C v132,v8,v9
M v[104:107],a[132:135],a[84:87],v[104:107]
E v8,v76
M v[104:107],a[136:139],a[88:91],v[104:107]
D a[156:159],v2,7168
C v133,v10,v11
M v[104:107],a[140:143],a[92:95],v[104:107]
E v9,v77
M a[8:11],a[200:203],v[148:151],a[8:11]
E v10,v78
M a[40:43],a[200:203],v[152:155],a[40:43]
D a[192:195],v3,0
C v136,v12,v13
M v[172:175],a[200:203],v[156:159],v[172:175]
GS v0,s[36:39],s75,1024
M v[204:207],a[200:203],v[160:163],v[204:207]
E v11,v79
M v[108:111],a[128:131],a[96:99],v[28:31]
E v12,v80
M v[108:111],a[132:135],a[100:103],v[108:111]
WL 2
E v13,v81
M v[108:111],a[136:139],a[104:107],v[108:111]
D a[196:199],v3,1024
C v137,v14,v15
M v[108:111],a[140:143],a[108:111],v[108:111]
E v14,v82
M a[12:15],a[204:207],v[148:151],a[12:15]
E v15,v83
M a[44:47],a[204:207],v[152:155],a[44:47]
GS v0,s[36:39],s75,2048
M v[176:179],a[204:207],v[156:159],v[176:179]
D a[200:203],v3,2048
C v140,v8,v9
M v[208:211],a[204:207],v[160:163],v[208:211]
E v8,v84
M v[112:115],a[128:131],a[112:115],v[32:35]
E v9,v85
M v[112:115],a[132:135],a[116:119],v[112:115]
D a[204:207],v3,3072
C v141,v10,v11
M v[112:115],a[136:139],a[120:123],v[112:115]
E v10,v86
M v[112:115],a[140:143],a[124:127],v[112:115]
E v11,v87
M a[16:19],a[208:211],v[148:151],a[16:19]
D a[128:131],v1,0
C v144,v12,v13
M a[48:51],a[208:211],v[152:155],a[48:51]
E v12,v88
M v[180:183],a[208:211],v[156:159],v[180:183]
WL 2
E v13,v89
M v[212:215],a[208:211],v[160:163],v[212:215]
D a[132:135],v1,1024
C v145,v14,v15
M v[116:119],a[144:147],a[64:67],v[20:23]
E v14,v90
M v[116:119],a[148:151],a[68:71],v[116:119]
E v15,v91
M v[116:119],a[152:155],a[72:75],v[116:119]
D a[136:139],v1,2048
C v134,v8,v9
M v[116:119],a[156:159],a[76:79],v[116:119]
E v8,v92
M a[20:23],a[212:215],v[148:151],a[20:23]
E v9,v93
M a[52:55],a[212:215],v[152:155],a[52:55]
GS v0,s[36:39],s75,3072
M v[184:187],a[212:215],v[156:159],v[184:187]
s_add_u32 s75,s75,16384
D a[140:143],v1,3072
C v135,v10,v11
M v[216:219],a[212:215],v[160:163],v[216:219]
E v10,v94
M v[36:39],v[16:19],v[132:135],v[52:55]
E v11,v95
M v[120:123],a[144:147],a[80:83],v[24:27]
D a[208:211],v3,4096
C v138,v12,v13
M v[120:123],a[148:151],a[84:87],v[120:123]
E v12,v96
M v[120:123],a[152:155],a[88:91],v[120:123]
E v13,v97
M v[120:123],a[156:159],a[92:95],v[120:123]
WL 2
D a[212:215],v3,5120
C v139,v14,v15
M a[24:27],a[216:219],v[148:151],a[24:27]
E v14,v98
M v[40:43],v[16:19],v[136:139],v[56:59]
E v15,v99
M a[56:59],a[216:219],v[152:155],a[56:59]
C v142,v8,v9
M v[188:191],a[216:219],v[156:159],v[188:191]
C v143,v10,v11
M v[220:223],a[216:219],v[160:163],v[220:223]
D a[216:219],v3,6144
C v146,v12,v13
M v[44:47],v[16:19],v[140:143],v[60:63]
C v147,v14,v15
M v[124:127],a[144:147],a[96:99],v[28:31]
v_add_f32 v248,v248,v244
M v[48:51],v[16:19],v[144:147],v[64:67]
v_add_f32 v249,v249,v245
M v[124:127],a[148:151],a[100:103],v[124:127]
v_add_f32 v250,v250,v246
M v[124:127],a[152:155],a[104:107],v[124:127]
v_add_f32 v251,v251,v247
M v[124:127],a[156:159],a[108:111],v[124:127]
M a[28:31],a[220:223],v[148:151],a[28:31]
M a[60:63],a[220:223],v[152:155],a[60:63]
v_max3_f32 v228,v36,v40,v44
M v[192:195],a[220:223],v[156:159],v[192:195]
WV 4
s_barrier
M v[224:227],a[220:223],v[160:163],v[224:227]
buffer_load_dwordx4 v[244:247],v5,s[84:87],s26 offen offset:0 sc1
M v[128:131],a[144:147],a[112:115],v[32:35]
WL 0
D a[220:223],v3,7168
v_max_f32 v228,v228,v48
M v[128:131],a[148:151],a[116:119],v[128:131]
s_add_u32 s26,s26,s78
v_cmp_lt_f32 vcc,s61,v228
M v[128:131],a[152:155],a[120:123],v[128:131]
s_cbranch_vccnz f_slow_Ae
f_rs_Ae:
M v[128:131],a[156:159],a[124:127],v[128:131]
s_sub_u32 s60,s60,1
s_cbranch_scc0 f_LA
M a[0:3],a[192:195],v[132:135],a[0:3]
s_cmp_ge_u32 s52,s58
s_cbranch_scc1 f_mask_B
f_rm_B:
M a[32:35],a[192:195],v[136:139],a[32:35]
s_add_u32 s52,s52,16384
E v8,v100
M v[164:167],a[192:195],v[140:143],v[164:167]
s_and_b32 s69,s52,65535
E v9,v101
M v[196:199],a[192:195],v[144:147],v[196:199]
s_add_u32 s53,s53,8192
E v10,v102
M v[68:71],a[128:131],a[64:67],v[20:23]
s_and_b32 s79,s53,65535
E v11,v103
M v[68:71],a[132:135],a[68:71],v[68:71]
s_and_b32 m0,s75,65535
D a[144:147],v1,4096
C v148,v8,v9
M v[68:71],a[136:139],a[72:75],v[68:71]
GS v0,s[36:39],s75,0
M v[68:71],a[140:143],a[76:79],v[68:71]
E v12,v104
M a[4:7],a[196:199],v[132:135],a[4:7]
WL 1
E v13,v105
M a[36:39],a[196:199],v[136:139],a[36:39]
D a[148:151],v1,5120
v_or_b32 v2,s69,v0
M v[168:171],a[196:199],v[140:143],v[168:171]
E v14,v106
M v[200:203],a[196:199],v[144:147],v[200:203]
D a[152:155],v1,6144
C v149,v10,v11
M v[72:75],a[128:131],a[80:83],v[24:27]
E v15,v107
M v[72:75],a[132:135],a[84:87],v[72:75]
D a[156:159],v1,7168
v_or_b32 v3,s79,v4
M v[72:75],a[136:139],a[88:91],v[72:75]
E v8,v108
M v[72:75],a[140:143],a[92:95],v[72:75]
D a[160:163],v1,8192
C v152,v12,v13
M a[8:11],a[200:203],v[132:135],a[8:11]
D a[164:167],v1,9216
C v153,v14,v15
M a[40:43],a[200:203],v[136:139],a[40:43]
WL 4
D a[168:171],v1,10240
M v[172:175],a[200:203],v[140:143],v[172:175]
D a[172:175],v1,11264
M v[204:207],a[200:203],v[144:147],v[204:207]
WL 5
D a[176:179],v1,12288
E v9,v109
M v[76:79],a[144:147],a[64:67],v[20:23]
)"
R"(D a[180:183],v1,13312
C v156,v8,v9
M v[76:79],a[148:151],a[68:71],v[76:79]
WL 6
D a[184:187],v1,14336
M v[76:79],a[152:155],a[72:75],v[76:79]
WL 7
D a[188:191],v1,15360
M v[76:79],a[156:159],a[76:79],v[76:79]
E v10,v110
M a[12:15],a[204:207],v[132:135],a[12:15]
D a[192:195],v3,0
M a[44:47],a[204:207],v[136:139],a[44:47]
GS v0,s[36:39],s75,1024
M v[176:179],a[204:207],v[140:143],v[176:179]
E v11,v111
M v[208:211],a[204:207],v[144:147],v[208:211]
E v12,v112
M v[80:83],a[144:147],a[80:83],v[24:27]
E v13,v113
M v[80:83],a[148:151],a[84:87],v[80:83]
E v14,v114
M v[80:83],a[152:155],a[88:91],v[80:83]
E v15,v115
M v[80:83],a[156:159],a[92:95],v[80:83]
D a[196:199],v3,1024
C v157,v10,v11
M a[16:19],a[208:211],v[132:135],a[16:19]
D a[128:131],v2,0
C v160,v12,v13
M a[48:51],a[208:211],v[136:139],a[48:51]
WL 2
E v8,v116
M v[180:183],a[208:211],v[140:143],v[180:183]
WL 2
D a[132:135],v2,1024
C v161,v14,v15
M v[212:215],a[208:211],v[144:147],v[212:215]
E v9,v117
M v[84:87],a[160:163],a[64:67],v[20:23]
D a[136:139],v2,2048
C v150,v8,v9
M v[84:87],a[164:167],a[68:71],v[84:87]
D a[140:143],v2,3072
M v[84:87],a[168:171],a[72:75],v[84:87]
D a[200:203],v3,2048
E v10,v118
M v[84:87],a[172:175],a[76:79],v[84:87]
E v11,v119
M a[20:23],a[212:215],v[132:135],a[20:23]
GS v0,s[36:39],s75,2048
M a[52:55],a[212:215],v[136:139],a[52:55]
E v12,v120
M v[184:187],a[212:215],v[140:143],v[184:187]
E v13,v121
M v[216:219],a[212:215],v[144:147],v[216:219]
E v14,v122
M v[88:91],a[160:163],a[80:83],v[24:27]
E v15,v123
M v[88:91],a[164:167],a[84:87],v[88:91]
E v8,v124
M v[88:91],a[168:171],a[88:91],v[88:91]
E v9,v125
M v[88:91],a[172:175],a[92:95],v[88:91]
WL 0
D a[204:207],v3,3072
C v151,v10,v11
M a[24:27],a[216:219],v[132:135],a[24:27]
E v10,v126
M v[52:55],v[16:19],v[148:151],v[36:39]
D a[144:147],v2,4096
C v154,v12,v13
M a[56:59],a[216:219],v[136:139],a[56:59]
E v11,v127
M v[188:191],a[216:219],v[140:143],v[188:191]
D a[148:151],v2,5120
C v155,v14,v15
M v[220:223],a[216:219],v[144:147],v[220:223]
D a[152:155],v2,6144
C v158,v8,v9
M v[56:59],v[16:19],v[152:155],v[40:43]
E v12,v128
M v[92:95],a[176:179],a[64:67],v[20:23]
GS v0,s[36:39],s75,3072
M v[92:95],a[180:183],a[68:71],v[92:95]
s_add_u32 s75,s75,16384
D a[156:159],v2,7168
C v159,v10,v11
M v[92:95],a[184:187],a[72:75],v[92:95]
E v13,v129
M v[60:63],v[16:19],v[156:159],v[44:47]
D a[208:211],v3,4096
E v14,v130
M v[92:95],a[188:191],a[76:79],v[92:95]
E v15,v131
M a[28:31],a[220:223],v[132:135],a[28:31]
D a[212:215],v3,5120
C v162,v12,v13
M a[60:63],a[220:223],v[136:139],a[60:63]
D a[160:163],v2,8192
C v163,v14,v15
M v[192:195],a[220:223],v[140:143],v[192:195]
D a[164:167],v2,9216
M v[64:67],v[16:19],v[160:163],v[48:51]
D a[168:171],v2,10240
v_max3_f32 v228,v52,v56,v60
M v[224:227],a[220:223],v[144:147],v[224:227]
WV 4
s_barrier
M v[96:99],a[176:179],a[80:83],v[24:27]
D a[172:175],v2,11264
M v[96:99],a[180:183],a[84:87],v[96:99]
D a[216:219],v3,6144
v_max_f32 v228,v228,v64
M v[96:99],a[184:187],a[88:91],v[96:99]
WL 6
D a[220:223],v3,7168
v_cmp_lt_f32 vcc,s61,v228
M v[96:99],a[188:191],a[92:95],v[96:99]
s_cbranch_vccnz f_slow_B
f_rs_B:
M a[0:3],a[192:195],v[148:151],a[0:3]
s_cmp_ge_u32 s52,s59
s_cbranch_scc1 f_mask_C
f_rm_C:
M a[32:35],a[192:195],v[152:155],a[32:35]
s_add_u32 s52,s52,16384
E v8,v68
M v[164:167],a[192:195],v[156:159],v[164:167]
s_and_b32 s69,s52,65535
E v9,v69
M v[196:199],a[192:195],v[160:163],v[196:199]
s_add_u32 s53,s53,8192
E v10,v70
M v[100:103],a[128:131],a[64:67],v[20:23]
s_and_b32 s79,s53,65535
E v11,v71
M v[100:103],a[132:135],a[68:71],v[100:103]
s_and_b32 m0,s75,65535
E v12,v72
M v[100:103],a[136:139],a[72:75],v[100:103]
GS v0,s[36:39],s75,0
M v[100:103],a[140:143],a[76:79],v[100:103]
E v13,v73
M a[4:7],a[196:199],v[148:151],a[4:7]
WL 0
GS v0,s[36:39],s75,1024
M a[36:39],a[196:199],v[152:155],a[36:39]
D a[176:179],v2,12288
E v14,v74
M v[168:171],a[196:199],v[156:159],v[168:171]
E v15,v75
M v[200:203],a[196:199],v[160:163],v[200:203]
D a[180:183],v2,13312
C v132,v8,v9
M v[104:107],a[128:131],a[80:83],v[24:27]
E v8,v76
M v[104:107],a[132:135],a[84:87],v[104:107]
D a[184:187],v2,14336
v_or_b32 v1,s69,v0
M v[104:107],a[136:139],a[88:91],v[104:107]
D a[188:191],v2,15360
C v133,v10,v11
M v[104:107],a[140:143],a[92:95],v[104:107]
E v9,v77
M a[8:11],a[200:203],v[148:151],a[8:11]
E v10,v78
M a[40:43],a[200:203],v[152:155],a[40:43]
GS v0,s[36:39],s75,2048
M v[172:175],a[200:203],v[156:159],v[172:175]
D a[128:131],v1,0
E v11,v79
M v[204:207],a[200:203],v[160:163],v[204:207]
GS v0,s[36:39],s75,3072
M v[108:111],a[144:147],a[64:67],v[20:23]
s_add_u32 s75,s75,16384
D a[132:135],v1,1024
v_or_b32 v3,s79,v4
M v[108:111],a[148:151],a[68:71],v[108:111]
WL 3
D a[224:227],v3,8192
C v136,v12,v13
M v[108:111],a[152:155],a[72:75],v[108:111]
s_and_b32 s74,s76,65535
D a[228:231],v3,9216
C v137,v14,v15
M v[108:111],a[156:159],a[76:79],v[108:111]
s_or_b32 m0,s74,65536
D a[232:235],v3,10240
C v134,v8,v9
M a[12:15],a[204:207],v[148:151],a[12:15]
D a[236:239],v3,11264
C v135,v10,v11
M a[44:47],a[204:207],v[152:155],a[44:47]
D a[240:243],v3,12288
M v[36:39],v[16:19],v[132:135],v[52:55]
D a[244:247],v3,13312
M v[176:179],a[204:207],v[156:159],v[176:179]
D a[248:251],v3,14336
E v12,v80
M v[208:211],a[204:207],v[160:163],v[208:211]
D a[252:255],v3,15360
M v[112:115],a[144:147],a[80:83],v[24:27]
D a[192:195],v3,0
M v[112:115],a[148:151],a[84:87],v[112:115]
E v13,v81
M v[112:115],a[152:155],a[88:91],v[112:115]
E v14,v82
M v[112:115],a[156:159],a[92:95],v[112:115]
E v15,v83
M a[16:19],a[208:211],v[148:151],a[16:19]
D a[196:199],v3,1024
C v138,v12,v13
M a[48:51],a[208:211],v[152:155],a[48:51]
E v8,v84
M v[180:183],a[208:211],v[156:159],v[180:183]
WL 3
E v9,v85
M v[212:215],a[208:211],v[160:163],v[212:215]
D a[136:139],v1,2048
E v10,v86
M v[116:119],a[160:163],a[64:67],v[20:23]
D a[140:143],v1,3072
C v139,v14,v15
M v[116:119],a[164:167],a[68:71],v[116:119]
E v11,v87
M v[40:43],v[16:19],v[136:139],v[56:59]
GS v0,s[40:43],s76,0
M v[116:119],a[168:171],a[72:75],v[116:119]
E v12,v88
M v[116:119],a[172:175],a[76:79],v[116:119]
D a[200:203],v3,2048
C v140,v8,v9
M a[20:23],a[212:215],v[148:151],a[20:23]
E v13,v89
M a[52:55],a[212:215],v[152:155],a[52:55]
E v14,v90
M v[184:187],a[212:215],v[156:159],v[184:187]
E v15,v91
M v[216:219],a[212:215],v[160:163],v[216:219]
D a[204:207],v3,3072
E v8,v92
M v[120:123],a[160:163],a[80:83],v[24:27]
GS v0,s[40:43],s76,1024
M v[120:123],a[164:167],a[84:87],v[120:123]
E v9,v93
M v[120:123],a[168:171],a[88:91],v[120:123]
D a[144:147],v1,4096
C v141,v10,v11
M v[120:123],a[172:175],a[92:95],v[120:123]
WL 2
E v10,v94
M a[24:27],a[216:219],v[148:151],a[24:27]
E v11,v95
M a[56:59],a[216:219],v[152:155],a[56:59]
D a[148:151],v1,5120
C v144,v12,v13
M v[188:191],a[216:219],v[156:159],v[188:191]
D a[152:155],v1,6144
C v145,v14,v15
M v[220:223],a[216:219],v[160:163],v[220:223]
D a[156:159],v1,7168
E v12,v96
M v[124:127],a[176:179],a[64:67],v[20:23]
GS v0,s[40:43],s76,2048
M v[124:127],a[180:183],a[68:71],v[124:127]
E v13,v97
M v[124:127],a[184:187],a[72:75],v[124:127]
E v14,v98
M v[124:127],a[188:191],a[76:79],v[124:127]
E v15,v99
M a[28:31],a[220:223],v[148:151],a[28:31]
D a[208:211],v3,4096
C v142,v8,v9
M a[60:63],a[220:223],v[152:155],a[60:63]
GS v0,s[40:43],s76,3072
M v[192:195],a[220:223],v[156:159],v[192:195]
s_add_u32 s76,s76,16384
D a[212:215],v3,5120
C v143,v10,v11
M v[224:227],a[220:223],v[160:163],v[224:227]
D a[160:163],v1,8192
C v146,v12,v13
M v[36:39],v[16:19],v[140:143],v[36:39]
D a[164:167],v1,9216
C v147,v14,v15
M v[128:131],a[176:179],a[80:83],v[24:27]
D a[168:171],v1,10240
M v[40:43],v[16:19],v[144:147],v[40:43]
WV 8
s_barrier
M v[128:131],a[180:183],a[84:87],v[128:131]
D a[172:175],v1,11264
M v[128:131],a[184:187],a[88:91],v[128:131]
WL 5
D a[216:219],v3,6144
M v[128:131],a[188:191],a[92:95],v[128:131]
D a[220:223],v3,7168
v_max_f32 v228,v36,v40
v_cmp_lt_f32 vcc,s61,v228
s_cbranch_vccnz f_slow_C
f_rs_C:
s_nop 9
v_rcp_f32 v228,v60
v_rcp_f32 v229,v64
v_mul_f32 v230,v164,v228
v_mul_f32 v231,v165,v228
v_mul_f32 v232,v166,v228
v_mul_f32 v233,v167,v228
v_mul_f32 v234,v168,v228
v_mul_f32 v235,v169,v228
v_mul_f32 v236,v170,v228
v_mul_f32 v237,v171,v228
C v148,v230,v231
C v149,v232,v233
C v150,v234,v235
C v151,v236,v237
buffer_store_dwordx4 v[148:151],v0,s[48:51],s66 offen offset:0 sc1
v_mul_f32 v230,v172,v228
v_mul_f32 v231,v173,v228
v_mul_f32 v232,v174,v228
v_mul_f32 v233,v175,v228
v_mul_f32 v234,v176,v228
v_mul_f32 v235,v177,v228
v_mul_f32 v236,v178,v228
v_mul_f32 v237,v179,v228
C v152,v230,v231
C v153,v232,v233
C v154,v234,v235
C v155,v236,v237
buffer_store_dwordx4 v[152:155],v0,s[48:51],s66 offen offset:1024 sc1
v_mul_f32 v230,v180,v228
v_mul_f32 v231,v181,v228
v_mul_f32 v232,v182,v228
v_mul_f32 v233,v183,v228
v_mul_f32 v234,v184,v228
v_mul_f32 v235,v185,v228
v_mul_f32 v236,v186,v228
v_mul_f32 v237,v187,v228
C v156,v230,v231
C v157,v232,v233
C v158,v234,v235
C v159,v236,v237
buffer_store_dwordx4 v[156:159],v0,s[48:51],s66 offen offset:2048 sc1
v_mul_f32 v230,v188,v228
v_mul_f32 v231,v189,v228
v_mul_f32 v232,v190,v228
v_mul_f32 v233,v191,v228
v_mul_f32 v234,v192,v228
v_mul_f32 v235,v193,v228
v_mul_f32 v236,v194,v228
v_mul_f32 v237,v195,v228
C v160,v230,v231
C v161,v232,v233
C v162,v234,v235
C v163,v236,v237
buffer_store_dwordx4 v[160:163],v0,s[48:51],s66 offen offset:3072 sc1
s_add_u32 s66,s66,131072
v_mul_f32 v230,v196,v229
v_mul_f32 v231,v197,v229
v_mul_f32 v232,v198,v229
v_mul_f32 v233,v199,v229
v_mul_f32 v234,v200,v229
v_mul_f32 v235,v201,v229
v_mul_f32 v236,v202,v229
v_mul_f32 v237,v203,v229
C v148,v230,v231
C v149,v232,v233
C v150,v234,v235
C v151,v236,v237
buffer_store_dwordx4 v[148:151],v0,s[48:51],s66 offen offset:0 sc1
v_mul_f32 v230,v204,v229
v_mul_f32 v231,v205,v229
v_mul_f32 v232,v206,v229
v_mul_f32 v233,v207,v229
v_mul_f32 v234,v208,v229
v_mul_f32 v235,v209,v229
v_mul_f32 v236,v210,v229
v_mul_f32 v237,v211,v229
C v152,v230,v231
C v153,v232,v233
C v154,v234,v235
C v155,v236,v237
buffer_store_dwordx4 v[152:155],v0,s[48:51],s66 offen offset:1024 sc1
v_mul_f32 v230,v212,v229
v_mul_f32 v231,v213,v229
v_mul_f32 v232,v214,v229
v_mul_f32 v233,v215,v229
v_mul_f32 v234,v216,v229
v_mul_f32 v235,v217,v229
v_mul_f32 v236,v218,v229
v_mul_f32 v237,v219,v229
C v156,v230,v231
C v157,v232,v233
C v158,v234,v235
C v159,v236,v237
buffer_store_dwordx4 v[156:159],v0,s[48:51],s66 offen offset:2048 sc1
v_mul_f32 v230,v220,v229
v_mul_f32 v231,v221,v229
v_mul_f32 v232,v222,v229
)"
R"(v_mul_f32 v233,v223,v229
v_mul_f32 v234,v224,v229
v_mul_f32 v235,v225,v229
v_mul_f32 v236,v226,v229
v_mul_f32 v237,v227,v229
C v160,v230,v231
C v161,v232,v233
C v162,v234,v235
C v163,v236,v237
buffer_store_dwordx4 v[160:163],v0,s[48:51],s66 offen offset:3072 sc1
v_lshlrev_b32 v166,3,v0
buffer_load_dword v165,v166,s[28:31],s24 offen offset:0
s_lshl_b32 s60,s27,1
s_sub_u32 s60,12,s60
s_cbranch_scc1 f_E7
.p2align 6
f_LD:
M a[0:3],a[192:195],v[132:135],a[0:3]
s_cmp_eq_u32 s63,3
s_cbranch_scc1 f_kpD
f_krD:
M a[0:3],a[224:227],v[140:143],a[0:3]
s_add_u32 s52,s52,16384
E v8,v100
M a[32:35],a[192:195],v[136:139],a[32:35]
s_and_b32 s69,s52,65535
E v9,v101
M a[32:35],a[224:227],v[144:147],a[32:35]
s_add_u32 s53,s53,16384
E v10,v102
M v[68:71],a[128:131],a[64:67],v[20:23]
s_and_b32 s79,s53,65535
E v11,v103
M v[68:71],a[132:135],a[68:71],v[68:71]
s_and_b32 m0,s75,65535
E v12,v104
M v[68:71],a[136:139],a[72:75],v[68:71]
GS v0,s[36:39],s75,0
M v[68:71],a[140:143],a[76:79],v[68:71]
E v13,v105
M a[4:7],a[196:199],v[132:135],a[4:7]
WL 0
GS v0,s[36:39],s75,1024
M a[4:7],a[228:231],v[140:143],a[4:7]
D a[176:179],v1,12288
E v14,v106
M a[36:39],a[196:199],v[136:139],a[36:39]
E v15,v107
M a[36:39],a[228:231],v[144:147],a[36:39]
D a[180:183],v1,13312
v_or_b32 v2,s69,v0
M v[72:75],a[128:131],a[80:83],v[24:27]
D a[184:187],v1,14336
v_or_b32 v3,s79,v4
M v[72:75],a[132:135],a[84:87],v[72:75]
D a[188:191],v1,15360
C v148,v8,v9
M v[72:75],a[136:139],a[88:91],v[72:75]
E v8,v108
M v[72:75],a[140:143],a[92:95],v[72:75]
E v9,v109
M a[8:11],a[200:203],v[132:135],a[8:11]
D a[192:195],v3,0
C v149,v10,v11
M a[8:11],a[232:235],v[140:143],a[8:11]
E v10,v110
M a[40:43],a[200:203],v[136:139],a[40:43]
D a[224:227],v3,8192
E v11,v111
M a[40:43],a[232:235],v[144:147],a[40:43]
GS v0,s[36:39],s75,2048
M v[76:79],a[144:147],a[64:67],v[20:23]
D a[196:199],v3,1024
C v152,v12,v13
M v[76:79],a[148:151],a[68:71],v[76:79]
WL 3
E v12,v112
M v[76:79],a[152:155],a[72:75],v[76:79]
E v13,v113
M v[76:79],a[156:159],a[76:79],v[76:79]
GS v0,s[36:39],s75,3072
M a[12:15],a[204:207],v[132:135],a[12:15]
s_add_u32 s75,s75,16384
D a[228:231],v3,9216
C v153,v14,v15
M a[12:15],a[236:239],v[140:143],a[12:15]
s_and_b32 s74,s76,65535
E v14,v114
M a[44:47],a[204:207],v[136:139],a[44:47]
s_or_b32 m0,s74,65536
E v15,v115
M a[44:47],a[236:239],v[144:147],a[44:47]
D a[128:131],v2,0
C v150,v8,v9
M v[80:83],a[144:147],a[80:83],v[24:27]
D a[132:135],v2,1024
E v8,v116
M v[80:83],a[148:151],a[84:87],v[80:83]
E v9,v117
M v[80:83],a[152:155],a[88:91],v[80:83]
D a[136:139],v2,2048
C v151,v10,v11
M v[80:83],a[156:159],a[92:95],v[80:83]
E v10,v118
M v[52:55],v[16:19],v[148:151],v[36:39]
D a[140:143],v2,3072
C v154,v12,v13
M a[16:19],a[208:211],v[132:135],a[16:19]
E v11,v119
M a[16:19],a[240:243],v[140:143],a[16:19]
GS v0,s[40:43],s76,0
M a[48:51],a[208:211],v[136:139],a[48:51]
WL 3
E v12,v120
M a[48:51],a[240:243],v[144:147],a[48:51]
D a[200:203],v3,2048
E v13,v121
M v[84:87],a[160:163],a[64:67],v[20:23]
D a[232:235],v3,10240
C v155,v14,v15
M v[84:87],a[164:167],a[68:71],v[84:87]
E v14,v122
M v[56:59],v[16:19],v[152:155],v[40:43]
E v15,v123
M v[84:87],a[168:171],a[72:75],v[84:87]
GS v0,s[40:43],s76,1024
M v[84:87],a[172:175],a[76:79],v[84:87]
D a[204:207],v3,3072
C v156,v8,v9
M a[20:23],a[212:215],v[132:135],a[20:23]
E v8,v124
M a[20:23],a[244:247],v[140:143],a[20:23]
E v9,v125
M a[52:55],a[212:215],v[136:139],a[52:55]
D a[236:239],v3,11264
C v157,v10,v11
M a[52:55],a[244:247],v[144:147],a[52:55]
D a[144:147],v2,4096
E v10,v126
M v[88:91],a[160:163],a[80:83],v[24:27]
E v11,v127
M v[88:91],a[164:167],a[84:87],v[88:91]
D a[148:151],v2,5120
C v160,v12,v13
M v[88:91],a[168:171],a[88:91],v[88:91]
E v12,v128
M v[88:91],a[172:175],a[92:95],v[88:91]
WL 3
D a[152:155],v2,6144
C v161,v14,v15
M a[24:27],a[216:219],v[132:135],a[24:27]
GS v0,s[40:43],s76,2048
M a[24:27],a[248:251],v[140:143],a[24:27]
D a[156:159],v2,7168
C v158,v8,v9
M a[56:59],a[216:219],v[136:139],a[56:59]
E v13,v129
M a[56:59],a[248:251],v[144:147],a[56:59]
E v14,v130
M v[92:95],a[176:179],a[64:67],v[20:23]
D a[208:211],v3,4096
E v15,v131
M v[92:95],a[180:183],a[68:71],v[92:95]
D a[240:243],v3,12288
C v159,v10,v11
M v[92:95],a[184:187],a[72:75],v[92:95]
GS v0,s[40:43],s76,3072
M v[52:55],v[16:19],v[156:159],v[52:55]
s_add_u32 s76,s76,16384
D a[212:215],v3,5120
C v162,v12,v13
M v[92:95],a[188:191],a[76:79],v[92:95]
D a[244:247],v3,13312
C v163,v14,v15
M a[28:31],a[220:223],v[132:135],a[28:31]
D a[160:163],v2,8192
v_add_f32 v248,v248,v252
M v[56:59],v[16:19],v[160:163],v[56:59]
D a[164:167],v2,9216
v_add_f32 v249,v249,v253
M a[28:31],a[252:255],v[140:143],a[28:31]
D a[168:171],v2,10240
v_add_f32 v250,v250,v254
M a[60:63],a[220:223],v[136:139],a[60:63]
v_add_f32 v251,v251,v255
WV 8
s_barrier
M a[60:63],a[252:255],v[144:147],a[60:63]
buffer_load_dwordx4 v[252:255],v5,s[84:87],s77 offen offset:0 sc1
M v[96:99],a[176:179],a[80:83],v[24:27]
WL 5
D a[172:175],v2,11264
v_max_f32 v228,v52,v56
M v[96:99],a[180:183],a[84:87],v[96:99]
s_add_u32 s77,s77,s78
D a[216:219],v3,6144
v_cmp_lt_f32 vcc,s61,v228
M v[96:99],a[184:187],a[88:91],v[96:99]
s_cbranch_vccnz f_slow_Do
f_rs_Do:
M v[96:99],a[188:191],a[92:95],v[96:99]
WL 4
D a[248:251],v3,14336
D a[220:223],v3,7168
D a[252:255],v3,15360
s_add_u32 s63,s63,1
M a[0:3],a[192:195],v[148:151],a[0:3]
s_add_u32 s52,s52,16384
E v8,v68
M a[0:3],a[224:227],v[156:159],a[0:3]
s_and_b32 s69,s52,65535
E v9,v69
M a[32:35],a[192:195],v[152:155],a[32:35]
s_add_u32 s53,s53,16384
E v10,v70
M a[32:35],a[224:227],v[160:163],a[32:35]
s_and_b32 s79,s53,65535
E v11,v71
M v[100:103],a[128:131],a[64:67],v[20:23]
s_and_b32 m0,s75,65535
E v12,v72
M v[100:103],a[132:135],a[68:71],v[100:103]
GS v0,s[36:39],s75,0
M v[100:103],a[136:139],a[72:75],v[100:103]
E v13,v73
M v[100:103],a[140:143],a[76:79],v[100:103]
GS v0,s[36:39],s75,1024
M a[4:7],a[196:199],v[148:151],a[4:7]
WL 0
E v14,v74
M a[4:7],a[228:231],v[156:159],a[4:7]
E v15,v75
M a[36:39],a[196:199],v[152:155],a[36:39]
D a[176:179],v2,12288
v_or_b32 v1,s69,v0
M a[36:39],a[228:231],v[160:163],a[36:39]
D a[180:183],v2,13312
v_or_b32 v3,s79,v4
M v[104:107],a[128:131],a[80:83],v[24:27]
D a[184:187],v2,14336
C v132,v8,v9
M v[104:107],a[132:135],a[84:87],v[104:107]
D a[188:191],v2,15360
E v8,v76
M v[104:107],a[136:139],a[88:91],v[104:107]
E v9,v77
M v[104:107],a[140:143],a[92:95],v[104:107]
D a[192:195],v3,0
C v133,v10,v11
M a[8:11],a[200:203],v[148:151],a[8:11]
E v10,v78
M a[8:11],a[232:235],v[156:159],a[8:11]
D a[224:227],v3,8192
C v136,v12,v13
M a[40:43],a[200:203],v[152:155],a[40:43]
GS v0,s[36:39],s75,2048
M a[40:43],a[232:235],v[160:163],a[40:43]
E v11,v79
M v[108:111],a[144:147],a[64:67],v[20:23]
D a[196:199],v3,1024
E v12,v80
M v[108:111],a[148:151],a[68:71],v[108:111]
WL 3
E v13,v81
M v[108:111],a[152:155],a[72:75],v[108:111]
GS v0,s[36:39],s75,3072
M v[108:111],a[156:159],a[76:79],v[108:111]
s_add_u32 s75,s75,16384
D a[228:231],v3,9216
C v137,v14,v15
M a[12:15],a[204:207],v[148:151],a[12:15]
s_and_b32 s74,s76,65535
E v14,v82
M a[12:15],a[236:239],v[156:159],a[12:15]
s_or_b32 m0,s74,65536
E v15,v83
M a[44:47],a[204:207],v[152:155],a[44:47]
D a[128:131],v1,0
C v134,v8,v9
M a[44:47],a[236:239],v[160:163],a[44:47]
E v8,v84
M v[112:115],a[144:147],a[80:83],v[24:27]
D a[132:135],v1,1024
C v135,v10,v11
M v[112:115],a[148:151],a[84:87],v[112:115]
E v9,v85
M v[36:39],v[16:19],v[132:135],v[52:55]
D a[136:139],v1,2048
E v10,v86
M v[112:115],a[152:155],a[88:91],v[112:115]
D a[140:143],v1,3072
C v138,v12,v13
M v[112:115],a[156:159],a[92:95],v[112:115]
E v11,v87
M a[16:19],a[208:211],v[148:151],a[16:19]
GS v0,s[40:43],s76,0
M a[16:19],a[240:243],v[156:159],a[16:19]
E v12,v88
M a[48:51],a[208:211],v[152:155],a[48:51]
WL 3
D a[200:203],v3,2048
C v139,v14,v15
M a[48:51],a[240:243],v[160:163],a[48:51]
D a[232:235],v3,10240
C v140,v8,v9
M v[40:43],v[16:19],v[136:139],v[56:59]
E v13,v89
M v[116:119],a[160:163],a[64:67],v[20:23]
E v14,v90
M v[116:119],a[164:167],a[68:71],v[116:119]
E v15,v91
M v[116:119],a[168:171],a[72:75],v[116:119]
GS v0,s[40:43],s76,1024
M v[116:119],a[172:175],a[76:79],v[116:119]
D a[204:207],v3,3072
E v8,v92
M a[20:23],a[212:215],v[148:151],a[20:23]
E v9,v93
M a[20:23],a[244:247],v[156:159],a[20:23]
D a[236:239],v3,11264
C v141,v10,v11
M a[52:55],a[212:215],v[152:155],a[52:55]
E v10,v94
M a[52:55],a[244:247],v[160:163],a[52:55]
D a[144:147],v1,4096
C v144,v12,v13
M v[120:123],a[160:163],a[80:83],v[24:27]
E v11,v95
M v[120:123],a[164:167],a[84:87],v[120:123]
D a[148:151],v1,5120
C v145,v14,v15
M v[120:123],a[168:171],a[88:91],v[120:123]
E v12,v96
M v[120:123],a[172:175],a[92:95],v[120:123]
WL 3
D a[152:155],v1,6144
C v142,v8,v9
M a[24:27],a[216:219],v[148:151],a[24:27]
GS v0,s[40:43],s76,2048
M a[24:27],a[248:251],v[156:159],a[24:27]
D a[156:159],v1,7168
C v143,v10,v11
M a[56:59],a[216:219],v[152:155],a[56:59]
D a[208:211],v3,4096
E v13,v97
M v[36:39],v[16:19],v[140:143],v[36:39]
E v14,v98
M a[56:59],a[248:251],v[160:163],a[56:59]
E v15,v99
M v[124:127],a[176:179],a[64:67],v[20:23]
D a[240:243],v3,12288
C v146,v12,v13
M v[124:127],a[180:183],a[68:71],v[124:127]
GS v0,s[40:43],s76,3072
M v[124:127],a[184:187],a[72:75],v[124:127]
s_add_u32 s76,s76,16384
D a[212:215],v3,5120
C v147,v14,v15
M v[124:127],a[188:191],a[76:79],v[124:127]
D a[244:247],v3,13312
v_add_f32 v248,v248,v244
M v[40:43],v[16:19],v[144:147],v[40:43]
D a[160:163],v1,8192
v_add_f32 v249,v249,v245
M a[28:31],a[220:223],v[148:151],a[28:31]
D a[164:167],v1,9216
v_add_f32 v250,v250,v246
M a[28:31],a[252:255],v[156:159],a[28:31]
D a[168:171],v1,10240
v_add_f32 v251,v251,v247
M a[60:63],a[220:223],v[152:155],a[60:63]
WV 8
s_barrier
M a[60:63],a[252:255],v[160:163],a[60:63]
buffer_load_dwordx4 v[244:247],v5,s[84:87],s26 offen offset:0 sc1
M v[128:131],a[176:179],a[80:83],v[24:27]
WL 5
D a[172:175],v1,11264
v_max_f32 v228,v36,v40
M v[128:131],a[180:183],a[84:87],v[128:131]
s_add_u32 s26,s26,s78
D a[216:219],v3,6144
v_cmp_lt_f32 vcc,s61,v228
M v[128:131],a[184:187],a[88:91],v[128:131]
s_cbranch_vccnz f_slow_De
f_rs_De:
M v[128:131],a[188:191],a[92:95],v[128:131]
s_sub_u32 s60,s60,1
D a[248:251],v3,14336
D a[220:223],v3,7168
D a[252:255],v3,15360
s_cbranch_scc0 f_LD
M a[0:3],a[192:195],v[132:135],a[0:3]
s_add_u32 s52,s52,16384
E v8,v100
M a[0:3],a[224:227],v[140:143],a[0:3]
s_and_b32 s69,s52,65535
E v9,v101
M a[32:35],a[192:195],v[136:139],a[32:35]
s_add_u32 s53,s53,16384
)"
R"(E v10,v102
M a[32:35],a[224:227],v[144:147],a[32:35]
s_and_b32 s79,s53,65535
E v11,v103
M v[68:71],a[128:131],a[64:67],v[20:23]
s_and_b32 s74,s76,65535
E v12,v104
M v[68:71],a[132:135],a[68:71],v[68:71]
s_or_b32 m0,s74,65536
E v13,v105
M v[68:71],a[136:139],a[72:75],v[68:71]
GS v0,s[40:43],s76,0
M v[68:71],a[140:143],a[76:79],v[68:71]
E v14,v106
M a[4:7],a[196:199],v[132:135],a[4:7]
WL 0
E v15,v107
M a[4:7],a[228:231],v[140:143],a[4:7]
D a[176:179],v1,12288
C v148,v8,v9
M a[36:39],a[196:199],v[136:139],a[36:39]
E v8,v108
M a[36:39],a[228:231],v[144:147],a[36:39]
D a[180:183],v1,13312
v_or_b32 v2,s69,v0
M v[72:75],a[128:131],a[80:83],v[24:27]
D a[184:187],v1,14336
C v149,v10,v11
M v[72:75],a[132:135],a[84:87],v[72:75]
D a[188:191],v1,15360
v_or_b32 v3,s79,v4
M v[72:75],a[136:139],a[88:91],v[72:75]
E v9,v109
M v[72:75],a[140:143],a[92:95],v[72:75]
D a[192:195],v3,0
C v152,v12,v13
M a[8:11],a[200:203],v[132:135],a[8:11]
E v10,v110
M a[8:11],a[232:235],v[140:143],a[8:11]
D a[224:227],v3,8192
C v153,v14,v15
M a[40:43],a[200:203],v[136:139],a[40:43]
GS v0,s[40:43],s76,1024
M a[40:43],a[232:235],v[144:147],a[40:43]
E v11,v111
M v[76:79],a[144:147],a[64:67],v[20:23]
E v12,v112
M v[76:79],a[148:151],a[68:71],v[76:79]
WL 2
E v13,v113
M v[76:79],a[152:155],a[72:75],v[76:79]
E v14,v114
M v[76:79],a[156:159],a[76:79],v[76:79]
D a[196:199],v3,1024
C v150,v8,v9
M a[12:15],a[204:207],v[132:135],a[12:15]
E v15,v115
M a[12:15],a[236:239],v[140:143],a[12:15]
D a[228:231],v3,9216
C v151,v10,v11
M a[44:47],a[204:207],v[136:139],a[44:47]
D a[128:131],v2,0
E v8,v116
M v[52:55],v[16:19],v[148:151],v[36:39]
D a[132:135],v2,1024
C v154,v12,v13
M a[44:47],a[236:239],v[144:147],a[44:47]
E v9,v117
M v[80:83],a[144:147],a[80:83],v[24:27]
D a[136:139],v2,2048
C v155,v14,v15
M v[80:83],a[148:151],a[84:87],v[80:83]
D a[140:143],v2,3072
C v156,v8,v9
M v[56:59],v[16:19],v[152:155],v[40:43]
E v10,v118
M v[80:83],a[152:155],a[88:91],v[80:83]
E v11,v119
M v[80:83],a[156:159],a[92:95],v[80:83]
GS v0,s[40:43],s76,2048
M a[16:19],a[208:211],v[132:135],a[16:19]
E v12,v120
M a[16:19],a[240:243],v[140:143],a[16:19]
D a[200:203],v3,2048
C v157,v10,v11
M a[48:51],a[208:211],v[136:139],a[48:51]
WL 3
D a[232:235],v3,10240
M a[48:51],a[240:243],v[144:147],a[48:51]
E v13,v121
M v[84:87],a[160:163],a[64:67],v[20:23]
E v14,v122
M v[84:87],a[164:167],a[68:71],v[84:87]
E v15,v123
M v[84:87],a[168:171],a[72:75],v[84:87]
E v8,v124
M v[84:87],a[172:175],a[76:79],v[84:87]
E v9,v125
M a[20:23],a[212:215],v[132:135],a[20:23]
D a[204:207],v3,3072
C v160,v12,v13
M a[20:23],a[244:247],v[140:143],a[20:23]
E v10,v126
M a[52:55],a[212:215],v[136:139],a[52:55]
D a[236:239],v3,11264
C v161,v14,v15
M a[52:55],a[244:247],v[144:147],a[52:55]
D a[144:147],v2,4096
C v158,v8,v9
M v[88:91],a[160:163],a[80:83],v[24:27]
E v11,v127
M v[88:91],a[164:167],a[84:87],v[88:91]
D a[148:151],v2,5120
C v159,v10,v11
M v[88:91],a[168:171],a[88:91],v[88:91]
D a[152:155],v2,6144
M v[52:55],v[16:19],v[156:159],v[52:55]
E v12,v128
M v[88:91],a[172:175],a[92:95],v[88:91]
WL 4
GS v0,s[40:43],s76,3072
M a[24:27],a[216:219],v[132:135],a[24:27]
s_add_u32 s76,s76,16384
D a[156:159],v2,7168
M a[24:27],a[248:251],v[140:143],a[24:27]
E v13,v129
M a[56:59],a[216:219],v[136:139],a[56:59]
D a[208:211],v3,4096
E v14,v130
M a[56:59],a[248:251],v[144:147],a[56:59]
E v15,v131
M v[92:95],a[176:179],a[64:67],v[20:23]
D a[240:243],v3,12288
C v162,v12,v13
M v[92:95],a[180:183],a[68:71],v[92:95]
D a[212:215],v3,5120
C v163,v14,v15
M v[92:95],a[184:187],a[72:75],v[92:95]
D a[244:247],v3,13312
M v[56:59],v[16:19],v[160:163],v[56:59]
D a[160:163],v2,8192
M v[92:95],a[188:191],a[76:79],v[92:95]
D a[164:167],v2,9216
M a[28:31],a[220:223],v[132:135],a[28:31]
D a[168:171],v2,10240
M a[28:31],a[252:255],v[140:143],a[28:31]
D a[172:175],v2,11264
M a[60:63],a[220:223],v[136:139],a[60:63]
WV 4
s_barrier
M a[60:63],a[252:255],v[144:147],a[60:63]
D a[216:219],v3,6144
v_max_f32 v228,v52,v56
M v[96:99],a[176:179],a[80:83],v[24:27]
D a[248:251],v3,14336
v_cmp_lt_f32 vcc,s61,v228
M v[96:99],a[180:183],a[84:87],v[96:99]
s_cbranch_vccnz f_slow_Xo
f_rs_Xo:
M v[96:99],a[184:187],a[88:91],v[96:99]
WL 5
D a[220:223],v3,7168
M v[96:99],a[188:191],a[92:95],v[96:99]
D a[252:255],v3,15360
M a[0:3],a[192:195],v[148:151],a[0:3]
s_cmp_ge_u32 s52,s59
s_cbranch_scc1 f_mask_Xe
f_rm_Xe:
M a[0:3],a[224:227],v[156:159],a[0:3]
s_add_u32 s52,s52,16384
E v8,v68
M a[32:35],a[192:195],v[152:155],a[32:35]
s_add_u32 s53,s53,16384
E v9,v69
M a[32:35],a[224:227],v[160:163],a[32:35]
s_and_b32 s79,s53,65535
E v10,v70
M v[100:103],a[128:131],a[64:67],v[20:23]
s_lshl_b32 s21,s35,7
E v11,v71
M v[100:103],a[132:135],a[68:71],v[100:103]
s_lshl_b32 s22,s27,4
E v12,v72
M v[100:103],a[136:139],a[72:75],v[100:103]
s_add_u32 s21,s21,s22
E v13,v73
M v[100:103],a[140:143],a[76:79],v[100:103]
s_lshl_b32 s22,s20,2
E v14,v74
M a[4:7],a[196:199],v[148:151],a[4:7]
s_add_u32 s21,s21,s22
E v15,v75
M a[4:7],a[228:231],v[156:159],a[4:7]
s_mul_i32 s21,s21,1280
buffer_load_dwordx4 a[96:99],v0,s[80:83],s21 offen offset:0 sc1
M a[36:39],a[196:199],v[152:155],a[36:39]
WL 0
buffer_load_dwordx4 a[100:103],v7,s[80:83],s21 offen offset:0 sc1
M a[36:39],a[228:231],v[160:163],a[36:39]
D a[176:179],v2,12288
C v132,v8,v9
M v[104:107],a[128:131],a[80:83],v[24:27]
E v8,v76
M v[104:107],a[132:135],a[84:87],v[104:107]
D a[180:183],v2,13312
C v133,v10,v11
M v[104:107],a[136:139],a[88:91],v[104:107]
D a[184:187],v2,14336
v_or_b32 v3,s79,v4
M v[104:107],a[140:143],a[92:95],v[104:107]
D a[188:191],v2,15360
C v136,v12,v13
M a[8:11],a[200:203],v[148:151],a[8:11]
E v9,v77
M a[8:11],a[232:235],v[156:159],a[8:11]
D a[192:195],v3,0
C v137,v14,v15
M a[40:43],a[200:203],v[152:155],a[40:43]
E v10,v78
M a[40:43],a[232:235],v[160:163],a[40:43]
D a[224:227],v3,8192
C v134,v8,v9
M v[108:111],a[144:147],a[64:67],v[20:23]
buffer_load_dwordx4 a[104:107],v0,s[80:83],s21 offen offset:1280 sc1
M v[108:111],a[148:151],a[68:71],v[108:111]
WL 4
E v11,v79
M v[108:111],a[152:155],a[72:75],v[108:111]
E v12,v80
M v[108:111],a[156:159],a[76:79],v[108:111]
E v13,v81
M a[12:15],a[204:207],v[148:151],a[12:15]
E v14,v82
M a[12:15],a[236:239],v[156:159],a[12:15]
D a[196:199],v3,1024
C v135,v10,v11
M a[44:47],a[204:207],v[152:155],a[44:47]
buffer_load_dwordx4 a[108:111],v7,s[80:83],s21 offen offset:1280 sc1
M v[36:39],v[16:19],v[132:135],v[52:55]
s_add_u32 s21,s21,2560
E v15,v83
M a[44:47],a[236:239],v[160:163],a[44:47]
D a[228:231],v3,9216
C v138,v12,v13
M v[112:115],a[144:147],a[80:83],v[24:27]
E v8,v84
M v[112:115],a[148:151],a[84:87],v[112:115]
E v9,v85
M v[112:115],a[152:155],a[88:91],v[112:115]
E v10,v86
M v[112:115],a[156:159],a[92:95],v[112:115]
E v11,v87
M a[16:19],a[208:211],v[148:151],a[16:19]
buffer_load_dwordx4 a[112:115],v0,s[80:83],s21 offen offset:0 sc1
M a[16:19],a[240:243],v[156:159],a[16:19]
E v12,v88
M a[48:51],a[208:211],v[152:155],a[48:51]
WL 1
D a[200:203],v3,2048
C v139,v14,v15
M a[48:51],a[240:243],v[160:163],a[48:51]
D a[232:235],v3,10240
C v140,v8,v9
M v[40:43],v[16:19],v[136:139],v[56:59]
E v13,v89
M v[116:119],a[160:163],a[64:67],v[20:23]
E v14,v90
M v[116:119],a[164:167],a[68:71],v[116:119]
E v15,v91
M v[116:119],a[168:171],a[72:75],v[116:119]
buffer_load_dwordx4 a[116:119],v7,s[80:83],s21 offen offset:0 sc1
M v[116:119],a[172:175],a[76:79],v[116:119]
E v8,v92
M a[20:23],a[212:215],v[148:151],a[20:23]
E v9,v93
M a[20:23],a[244:247],v[156:159],a[20:23]
D a[204:207],v3,3072
C v141,v10,v11
M a[52:55],a[212:215],v[152:155],a[52:55]
E v10,v94
M a[52:55],a[244:247],v[160:163],a[52:55]
D a[236:239],v3,11264
C v144,v12,v13
M v[120:123],a[160:163],a[80:83],v[24:27]
E v11,v95
M v[120:123],a[164:167],a[84:87],v[120:123]
E v12,v96
M v[120:123],a[168:171],a[88:91],v[120:123]
buffer_load_dwordx4 a[120:123],v0,s[80:83],s21 offen offset:1280 sc1
M v[120:123],a[172:175],a[92:95],v[120:123]
WL 2
E v13,v97
M a[24:27],a[216:219],v[148:151],a[24:27]
D a[208:211],v3,4096
C v145,v14,v15
M a[24:27],a[248:251],v[156:159],a[24:27]
E v14,v98
M a[56:59],a[216:219],v[152:155],a[56:59]
E v15,v99
M a[56:59],a[248:251],v[160:163],a[56:59]
D a[240:243],v3,12288
C v142,v8,v9
M v[124:127],a[176:179],a[64:67],v[20:23]
buffer_load_dwordx4 a[124:127],v7,s[80:83],s21 offen offset:1280 sc1
M v[124:127],a[180:183],a[68:71],v[124:127]
D a[212:215],v3,5120
C v143,v10,v11
M v[124:127],a[184:187],a[72:75],v[124:127]
D a[244:247],v3,13312
C v146,v12,v13
M v[36:39],v[16:19],v[140:143],v[36:39]
D a[216:219],v3,6144
C v147,v14,v15
M v[124:127],a[188:191],a[76:79],v[124:127]
D a[248:251],v3,14336
M v[40:43],v[16:19],v[144:147],v[40:43]
M a[28:31],a[220:223],v[148:151],a[28:31]
M a[28:31],a[252:255],v[156:159],a[28:31]
M a[60:63],a[220:223],v[152:155],a[60:63]
WV 8
s_barrier
M a[60:63],a[252:255],v[160:163],a[60:63]
D a[220:223],v3,7168
M v[128:131],a[176:179],a[80:83],v[24:27]
D a[252:255],v3,15360
v_max_f32 v228,v36,v40
M v[128:131],a[180:183],a[84:87],v[128:131]
v_cmp_lt_f32 vcc,s61,v228
M v[128:131],a[184:187],a[88:91],v[128:131]
WL 2
s_cbranch_vccnz f_slow_Xe
f_rs_Xe:
M v[128:131],a[188:191],a[92:95],v[128:131]
f_E:
M a[0:3],a[192:195],v[132:135],a[0:3]
s_cmp_ge_u32 s52,s59
s_cbranch_scc1 f_mask_E
f_rm_E:
M a[0:3],a[224:227],v[140:143],a[0:3]
s_add_u32 s53,s53,16384
E v8,v100
M a[32:35],a[192:195],v[136:139],a[32:35]
s_and_b32 s79,s53,65535
E v9,v101
E v10,v102
M a[32:35],a[224:227],v[144:147],a[32:35]
E v11,v103
M a[4:7],a[196:199],v[132:135],a[4:7]
E v12,v104
E v13,v105
M a[4:7],a[228:231],v[140:143],a[4:7]
E v14,v106
M a[36:39],a[196:199],v[136:139],a[36:39]
E v15,v107
M a[36:39],a[228:231],v[144:147],a[36:39]
C v148,v8,v9
C v149,v10,v11
M a[8:11],a[200:203],v[132:135],a[8:11]
WL 0
E v8,v108
M a[8:11],a[232:235],v[140:143],a[8:11]
E v9,v109
E v10,v110
M a[40:43],a[200:203],v[136:139],a[40:43]
E v11,v111
M a[40:43],a[232:235],v[144:147],a[40:43]
C v152,v12,v13
v_or_b32 v3,s79,v4
M a[12:15],a[204:207],v[132:135],a[12:15]
E v12,v112
M a[12:15],a[236:239],v[140:143],a[12:15]
E v13,v113
D a[192:195],v3,0
C v153,v14,v15
C v150,v8,v9
M a[44:47],a[204:207],v[136:139],a[44:47]
E v14,v114
M a[44:47],a[236:239],v[144:147],a[44:47]
E v15,v115
E v8,v116
M a[16:19],a[208:211],v[132:135],a[16:19]
D a[224:227],v3,8192
C v151,v10,v11
C v154,v12,v13
M a[16:19],a[240:243],v[140:143],a[16:19]
E v9,v117
E v10,v118
M v[52:55],v[16:19],v[148:151],v[36:39]
E v11,v119
M a[48:51],a[208:211],v[136:139],a[48:51]
D a[196:199],v3,1024
C v155,v14,v15
E v12,v120
M a[48:51],a[240:243],v[144:147],a[48:51]
D a[228:231],v3,9216
C v156,v8,v9
E v13,v121
M v[56:59],v[16:19],v[152:155],v[40:43]
E v14,v122
M a[20:23],a[212:215],v[132:135],a[20:23]
)"
R"(E v15,v123
D a[200:203],v3,2048
C v157,v10,v11
C v160,v12,v13
M a[20:23],a[244:247],v[140:143],a[20:23]
WL 4
E v8,v124
M a[52:55],a[212:215],v[136:139],a[52:55]
D a[232:235],v3,10240
C v161,v14,v15
E v9,v125
M a[52:55],a[244:247],v[144:147],a[52:55]
E v10,v126
M a[24:27],a[216:219],v[132:135],a[24:27]
E v11,v127
D a[204:207],v3,3072
C v158,v8,v9
C v159,v10,v11
M a[24:27],a[248:251],v[140:143],a[24:27]
E v12,v128
D a[236:239],v3,11264
M v[52:55],v[16:19],v[156:159],v[52:55]
E v13,v129
M a[56:59],a[216:219],v[136:139],a[56:59]
E v14,v130
E v15,v131
M a[56:59],a[248:251],v[144:147],a[56:59]
D a[208:211],v3,4096
C v162,v12,v13
M a[28:31],a[220:223],v[132:135],a[28:31]
D a[240:243],v3,12288
C v163,v14,v15
D a[212:215],v3,5120
M a[28:31],a[252:255],v[140:143],a[28:31]
D a[244:247],v3,13312
M v[56:59],v[16:19],v[160:163],v[56:59]
D a[216:219],v3,6144
D a[248:251],v3,14336
M a[60:63],a[220:223],v[136:139],a[60:63]
WL 7
D a[220:223],v3,7168
M a[60:63],a[252:255],v[144:147],a[60:63]
D a[252:255],v3,15360
s_nop 0
v_max_f32 v228,v52,v56
v_cmp_lt_f32 vcc,s61,v228
s_cbranch_vccnz f_slow_E
f_rs_E:
f_F:
s_load_dwordx2 s[36:37],%0,88
s_mov_b32 s38,16777216
s_mov_b32 s39,131072
s_load_dwordx2 s[40:41],%0,40
s_mov_b32 s42,4718592
s_mov_b32 s43,131072
s_load_dwordx2 s[44:45],%0,88
s_mov_b32 s46,2359296
s_mov_b32 s47,131072
s_load_dwordx2 s[52:53],%0,80
s_mov_b32 s54,4718592
s_mov_b32 s55,131072
WL 0
M a[0:3],a[192:195],v[148:151],a[0:3]
s_add_u32 s36,s36,33554432
s_addc_u32 s37,s37,0
s_and_b32 s37,s37,65535
M a[0:3],a[224:227],v[156:159],a[0:3]
s_add_u32 s44,s44,79691776
s_addc_u32 s45,s45,0
s_and_b32 s45,s45,65535
s_and_b32 s41,s41,65535
M a[32:35],a[192:195],v[152:155],a[32:35]
s_and_b32 s53,s53,65535
v_lshrrev_b32 v18,4,v0
M a[32:35],a[224:227],v[160:163],a[32:35]
v_lshrrev_b32 v19,3,v18
M a[4:7],a[196:199],v[148:151],a[4:7]
v_and_b32 v20,7,v18
v_and_b32 v21,6,v19
v_xor_b32 v20,v20,v21
M a[4:7],a[228:231],v[156:159],a[4:7]
v_lshlrev_b32 v20,4,v20
v_mul_u32_u24 v21,1152,v19
v_add_u32 v1,v21,v20
M a[36:39],a[196:199],v[152:155],a[36:39]
v_add_u32 v2,8192,v1
v_add_u32 v3,16384,v1
v_add_u32 v4,24576,v1
M a[36:39],a[228:231],v[160:163],a[36:39]
v_lshrrev_b32 v21,2,v18
v_lshlrev_b32 v22,7,v21
v_bfe_u32 v21,v21,1,2
M a[8:11],a[200:203],v[148:151],a[8:11]
v_lshl_add_u32 v22,v21,5,v22
v_and_b32 v21,3,v18
v_lshl_add_u32 v22,v21,3,v22
M a[8:11],a[232:235],v[156:159],a[8:11]
v_add_u32 v22,s68,v22
M a[40:43],a[200:203],v[152:155],a[40:43]
s_lshl_b32 s22,s20,5
v_xor_b32 v9,s22,v22
v_xor_b32 v10,32,v9
v_xor_b32 v11,64,v9
M a[40:43],a[232:235],v[160:163],a[40:43]
v_xor_b32 v12,96,v9
v_and_b32 v21,15,v18
v_mul_u32_u24 v13,2304,v21
M a[12:15],a[204:207],v[148:151],a[12:15]
v_lshrrev_b32 v21,4,v18
v_lshl_add_u32 v13,v21,3,v13
M a[12:15],a[236:239],v[156:159],a[12:15]
s_mul_i32 s56,s70,655360
s_lshl_b32 s23,s20,15
s_add_u32 s56,s56,s23
s_add_u32 s57,s56,131072
M a[44:47],a[204:207],v[152:155],a[44:47]
s_add_u32 s58,s56,262144
s_add_u32 s59,s56,393216
s_add_u32 s60,s56,524288
s_mul_i32 s72,s70,184320
M a[44:47],a[236:239],v[160:163],a[44:47]
s_lshl_b32 s24,s71,7
s_add_u32 s72,s72,s24
s_add_u32 s72,s72,s22
s_mul_i32 s22,s20,15360
M a[16:19],a[208:211],v[148:151],a[16:19]
s_add_u32 s22,s22,98304
v_add_u32 v14,s22,v0
M a[16:19],a[240:243],v[156:159],a[16:19]
s_xor_b32 s22,s20,1
s_mul_i32 s22,s22,15360
s_add_u32 s22,s22,98304
v_add_u32 v15,s22,v0
M a[48:51],a[208:211],v[152:155],a[48:51]
s_xor_b32 s22,s20,2
s_mul_i32 s22,s22,15360
s_add_u32 s22,s22,103424
v_add_u32 v16,s22,v0
M a[48:51],a[240:243],v[160:163],a[48:51]
s_xor_b32 s22,s20,3
s_mul_i32 s22,s22,15360
s_add_u32 s22,s22,108544
v_add_u32 v17,s22,v0
M a[20:23],a[212:215],v[148:151],a[20:23]
s_mul_i32 s21,s70,80
s_add_u32 s22,s21,79
s_min_u32 s22,s22,2047
v_mov_b32 v18,s21
v_mov_b32 v19,s22
v_cmp_gt_u32 vcc,512,v0
M a[20:23],a[244:247],v[156:159],a[20:23]
M a[52:55],a[212:215],v[152:155],a[52:55]
v_cndmask_b32 v18,v19,v18,vcc
v_bfe_u32 v19,v0,4,5
v_and_b32 v19,7,v19
M a[52:55],a[244:247],v[160:163],a[52:55]
s_lshl_b32 s23,s20,3
v_add_u32 v19,s23,v19
v_lshrrev_b32 v20,2,v19
v_and_b32 v19,3,v19
M a[24:27],a[216:219],v[148:151],a[24:27]
v_lshl_add_u32 v19,v19,6,v20
v_lshrrev_b32 v20,7,v18
v_sub_u32 v21,15,v20
M a[24:27],a[248:251],v[156:159],a[24:27]
v_min_u32 v20,v20,v21
v_lshl_add_u32 v20,v20,3,v19
v_lshlrev_b32 v20,6,v20
M a[56:59],a[216:219],v[152:155],a[56:59]
v_add_u32 v29,32768,v20
M a[56:59],a[248:251],v[160:163],a[56:59]
s_lshl_b32 s21,s20,10
v_add_u32 v68,s21,v0
v_mov_b32 v72,0
v_mov_b32 v73,0
M a[28:31],a[220:223],v[148:151],a[28:31]
v_mov_b32 v74,0
v_mov_b32 v75,0
v_add_u32 v69,155648,v68
M a[28:31],a[252:255],v[156:159],a[28:31]
ds_write_b128 v69,v[72:75] offset:0
v_lshrrev_b32 v70,4,v68
v_lshrrev_b32 v76,4,v70
v_and_b32 v77,15,v70
M a[60:63],a[220:223],v[152:155],a[60:63]
v_subrev_u32 v78,144,v70
v_lshrrev_b32 v79,3,v78
v_and_b32 v78,7,v78
M a[60:63],a[252:255],v[160:163],a[60:63]
v_add_u32 v78,16,v78
v_cmp_gt_u32 vcc,144,v70
s_nop 1
v_cndmask_b32 v76,v79,v76,vcc
v_cndmask_b32 v77,v78,v77,vcc
s_and_b32 s22,s35,1
s_lshl_b32 s22,s22,1
v_subrev_u32 v78,s22,v77
v_cmp_eq_u32 s[90:91],20,v77
v_mov_b32 v79,40
v_cmp_gt_u32 vcc,18,v78
s_nop 1
v_cndmask_b32 v78,v79,v78,vcc
v_cndmask_b32 v78,v78,18,s[90:91]
v_cmp_gt_u32 vcc,216,v70
s_nop 1
v_cndmask_b32 v78,v79,v78,vcc
v_lshlrev_b32 v78,7,v78
v_lshl_add_u32 v78,v76,3,v78
v_add_u32 v78,155648,v78
v_lshrrev_b32 v69,4,v0
v_and_b32 v70,15,v69
v_lshrrev_b32 v71,4,v69
v_lshlrev_b32 v76,7,v70
v_lshl_add_u32 v76,v71,4,v76
v_add_u32 v76,155648,v76
s_lshl_b32 s74,s27,4
s_lshl_b32 s22,s20,2
s_add_u32 s74,s74,s22
s_mul_i32 s74,s74,18432
s_lshr_b32 s22,s35,1
s_lshl_b32 s22,s22,10
s_and_b32 s23,s35,1
s_lshl_b32 s23,s23,3
s_add_u32 s22,s22,s23
v_add_u32 v77,s22,v0
s_lshr_b32 s22,s35,4
s_add_u32 s22,s22,16
s_lshl_b32 s22,s22,10
s_and_b32 s23,s35,12
s_lshl_b32 s23,s23,6
s_add_u32 s22,s22,s23
s_and_b32 s23,s35,3
s_lshl_b32 s23,s23,2
s_add_u32 s22,s22,s23
v_add_u32 v68,s22,v0
v_mov_b32 v79,2130706432
v_cmp_gt_u32 vcc,256,v0
s_nop 1
v_cndmask_b32 v68,v79,v68,vcc
s_nop 9
v_rcp_f32 v228,v52
v_rcp_f32 v229,v56
AR v230,a0
AR v231,a1
AR v232,a2
AR v233,a3
AR v234,a4
AR v235,a5
AR v236,a6
AR v237,a7
v_mul_f32 v230,v230,v228
v_mul_f32 v231,v231,v228
v_mul_f32 v232,v232,v228
v_mul_f32 v233,v233,v228
v_mul_f32 v234,v234,v228
v_mul_f32 v235,v235,v228
v_mul_f32 v236,v236,v228
v_mul_f32 v237,v237,v228
C v132,v230,v231
C v133,v232,v233
C v134,v234,v235
C v135,v236,v237
buffer_store_dwordx4 v[132:135],v0,s[48:51],s67 offen offset:0 sc1
AR v230,a8
AR v231,a9
AR v232,a10
AR v233,a11
AR v234,a12
AR v235,a13
AR v236,a14
AR v237,a15
v_mul_f32 v230,v230,v228
v_mul_f32 v231,v231,v228
v_mul_f32 v232,v232,v228
v_mul_f32 v233,v233,v228
v_mul_f32 v234,v234,v228
v_mul_f32 v235,v235,v228
v_mul_f32 v236,v236,v228
v_mul_f32 v237,v237,v228
C v136,v230,v231
C v137,v232,v233
C v138,v234,v235
C v139,v236,v237
buffer_store_dwordx4 v[136:139],v0,s[48:51],s67 offen offset:1024 sc1
AR v230,a16
AR v231,a17
AR v232,a18
AR v233,a19
AR v234,a20
AR v235,a21
AR v236,a22
AR v237,a23
v_mul_f32 v230,v230,v228
v_mul_f32 v231,v231,v228
v_mul_f32 v232,v232,v228
v_mul_f32 v233,v233,v228
v_mul_f32 v234,v234,v228
v_mul_f32 v235,v235,v228
v_mul_f32 v236,v236,v228
v_mul_f32 v237,v237,v228
C v140,v230,v231
C v141,v232,v233
C v142,v234,v235
C v143,v236,v237
buffer_store_dwordx4 v[140:143],v0,s[48:51],s67 offen offset:2048 sc1
AR v230,a24
AR v231,a25
AR v232,a26
AR v233,a27
AR v234,a28
AR v235,a29
AR v236,a30
AR v237,a31
v_mul_f32 v230,v230,v228
v_mul_f32 v231,v231,v228
v_mul_f32 v232,v232,v228
v_mul_f32 v233,v233,v228
v_mul_f32 v234,v234,v228
v_mul_f32 v235,v235,v228
v_mul_f32 v236,v236,v228
v_mul_f32 v237,v237,v228
C v144,v230,v231
C v145,v232,v233
C v146,v234,v235
C v147,v236,v237
buffer_store_dwordx4 v[144:147],v0,s[48:51],s67 offen offset:3072 sc1
s_add_u32 s67,s67,131072
AR v230,a32
AR v231,a33
AR v232,a34
AR v233,a35
AR v234,a36
AR v235,a37
AR v236,a38
AR v237,a39
v_mul_f32 v230,v230,v229
v_mul_f32 v231,v231,v229
v_mul_f32 v232,v232,v229
v_mul_f32 v233,v233,v229
v_mul_f32 v234,v234,v229
v_mul_f32 v235,v235,v229
v_mul_f32 v236,v236,v229
v_mul_f32 v237,v237,v229
C v132,v230,v231
C v133,v232,v233
C v134,v234,v235
C v135,v236,v237
buffer_store_dwordx4 v[132:135],v0,s[48:51],s67 offen offset:0 sc1
AR v230,a40
AR v231,a41
AR v232,a42
AR v233,a43
AR v234,a44
AR v235,a45
AR v236,a46
AR v237,a47
v_mul_f32 v230,v230,v229
v_mul_f32 v231,v231,v229
v_mul_f32 v232,v232,v229
v_mul_f32 v233,v233,v229
v_mul_f32 v234,v234,v229
v_mul_f32 v235,v235,v229
v_mul_f32 v236,v236,v229
v_mul_f32 v237,v237,v229
C v136,v230,v231
C v137,v232,v233
C v138,v234,v235
C v139,v236,v237
buffer_store_dwordx4 v[136:139],v0,s[48:51],s67 offen offset:1024 sc1
AR v230,a48
AR v231,a49
AR v232,a50
AR v233,a51
AR v234,a52
AR v235,a53
AR v236,a54
AR v237,a55
v_mul_f32 v230,v230,v229
v_mul_f32 v231,v231,v229
v_mul_f32 v232,v232,v229
v_mul_f32 v233,v233,v229
v_mul_f32 v234,v234,v229
v_mul_f32 v235,v235,v229
v_mul_f32 v236,v236,v229
v_mul_f32 v237,v237,v229
C v140,v230,v231
C v141,v232,v233
C v142,v234,v235
C v143,v236,v237
buffer_store_dwordx4 v[140:143],v0,s[48:51],s67 offen offset:2048 sc1
AR v230,a56
AR v231,a57
AR v232,a58
AR v233,a59
AR v234,a60
AR v235,a61
AR v236,a62
AR v237,a63
v_mul_f32 v230,v230,v229
v_mul_f32 v231,v231,v229
)"
R"(v_mul_f32 v232,v232,v229
v_mul_f32 v233,v233,v229
v_mul_f32 v234,v234,v229
v_mul_f32 v235,v235,v229
v_mul_f32 v236,v236,v229
v_mul_f32 v237,v237,v229
C v144,v230,v231
C v145,v232,v233
C v146,v234,v235
C v147,v236,v237
buffer_store_dwordx4 v[144:147],v0,s[48:51],s67 offen offset:3072 sc1
s_branch f_end
f_E7:
M a[0:3],a[192:195],v[132:135],a[0:3]
s_cmp_ge_u32 s52,s59
s_cbranch_scc1 f_mask_E7
f_rm_E7:
M a[0:3],a[224:227],v[140:143],a[0:3]
s_add_u32 s53,s53,16384
E v8,v100
E v9,v101
M a[32:35],a[192:195],v[136:139],a[32:35]
s_and_b32 s79,s53,65535
E v10,v102
M a[32:35],a[224:227],v[144:147],a[32:35]
s_lshl_b32 s21,s35,7
E v11,v103
E v12,v104
M a[4:7],a[196:199],v[132:135],a[4:7]
s_lshl_b32 s22,s27,4
E v13,v105
E v14,v106
M a[4:7],a[228:231],v[140:143],a[4:7]
s_add_u32 s21,s21,s22
E v15,v107
M a[36:39],a[196:199],v[136:139],a[36:39]
s_lshl_b32 s22,s20,2
C v148,v8,v9
C v149,v10,v11
M a[36:39],a[228:231],v[144:147],a[36:39]
s_add_u32 s21,s21,s22
E v8,v108
E v9,v109
M a[8:11],a[200:203],v[132:135],a[8:11]
s_mul_i32 s21,s21,1280
buffer_load_dwordx4 a[96:99],v0,s[80:83],s21 offen offset:0 sc1
M a[8:11],a[232:235],v[140:143],a[8:11]
WL 0
buffer_load_dwordx4 a[100:103],v7,s[80:83],s21 offen offset:0 sc1
buffer_load_dwordx4 a[104:107],v0,s[80:83],s21 offen offset:1280 sc1
M a[40:43],a[200:203],v[136:139],a[40:43]
E v10,v110
E v11,v111
M a[40:43],a[232:235],v[144:147],a[40:43]
buffer_load_dwordx4 a[108:111],v7,s[80:83],s21 offen offset:1280 sc1
M a[12:15],a[204:207],v[132:135],a[12:15]
s_add_u32 s21,s21,2560
buffer_load_dwordx4 a[112:115],v0,s[80:83],s21 offen offset:0 sc1
buffer_load_dwordx4 a[116:119],v7,s[80:83],s21 offen offset:0 sc1
M a[12:15],a[236:239],v[140:143],a[12:15]
buffer_load_dwordx4 a[120:123],v0,s[80:83],s21 offen offset:1280 sc1
buffer_load_dwordx4 a[124:127],v7,s[80:83],s21 offen offset:1280 sc1
M a[44:47],a[204:207],v[136:139],a[44:47]
C v152,v12,v13
v_or_b32 v3,s79,v4
M a[44:47],a[236:239],v[144:147],a[44:47]
E v12,v112
E v13,v113
M a[16:19],a[208:211],v[132:135],a[16:19]
D a[192:195],v3,0
C v153,v14,v15
E v14,v114
M a[16:19],a[240:243],v[140:143],a[16:19]
E v15,v115
D a[224:227],v3,8192
C v150,v8,v9
C v151,v10,v11
M a[48:51],a[208:211],v[136:139],a[48:51]
E v8,v116
M v[52:55],v[16:19],v[148:151],v[36:39]
E v9,v117
E v10,v118
M a[48:51],a[240:243],v[144:147],a[48:51]
E v11,v119
D a[196:199],v3,1024
C v154,v12,v13
C v155,v14,v15
M a[20:23],a[212:215],v[132:135],a[20:23]
E v12,v120
D a[228:231],v3,9216
C v156,v8,v9
C v157,v10,v11
M v[56:59],v[16:19],v[152:155],v[40:43]
E v13,v121
M a[20:23],a[244:247],v[140:143],a[20:23]
WL 4
E v14,v122
E v15,v123
M a[52:55],a[212:215],v[136:139],a[52:55]
D a[200:203],v3,2048
C v160,v12,v13
E v8,v124
M a[52:55],a[244:247],v[144:147],a[52:55]
D a[232:235],v3,10240
C v161,v14,v15
E v9,v125
M a[24:27],a[216:219],v[132:135],a[24:27]
E v10,v126
M a[24:27],a[248:251],v[140:143],a[24:27]
E v11,v127
D a[204:207],v3,3072
C v158,v8,v9
C v159,v10,v11
M a[56:59],a[216:219],v[136:139],a[56:59]
E v12,v128
D a[236:239],v3,11264
M v[52:55],v[16:19],v[156:159],v[52:55]
E v13,v129
E v14,v130
M a[56:59],a[248:251],v[144:147],a[56:59]
E v15,v131
M a[28:31],a[220:223],v[132:135],a[28:31]
D a[208:211],v3,4096
C v162,v12,v13
D a[240:243],v3,12288
C v163,v14,v15
M a[28:31],a[252:255],v[140:143],a[28:31]
D a[212:215],v3,5120
D a[244:247],v3,13312
M v[56:59],v[16:19],v[160:163],v[56:59]
D a[216:219],v3,6144
M a[60:63],a[220:223],v[136:139],a[60:63]
WL 7
D a[248:251],v3,14336
D a[220:223],v3,7168
M a[60:63],a[252:255],v[144:147],a[60:63]
D a[252:255],v3,15360
s_nop 0
v_max_f32 v228,v52,v56
v_cmp_lt_f32 vcc,s61,v228
s_cbranch_vccnz f_slow_E7
f_rs_E7:
s_branch f_F
f_mask_P:
s_nop 7
v_mov_b32 v238,s65
s_cmp_eq_u32 s52,s58
s_cbranch_scc0 f_mask_P_f
v_cndmask_b32 v76,v76,v238,s[44:45]
v_cndmask_b32 v77,v77,v238,s[46:47]
v_cndmask_b32 v78,v78,v238,s[54:55]
v_cndmask_b32 v79,v79,v238,s[56:57]
v_cndmask_b32 v96,v96,v238,s[44:45]
v_cndmask_b32 v97,v97,v238,s[46:47]
v_cndmask_b32 v98,v98,v238,s[54:55]
v_cndmask_b32 v99,v99,v238,s[56:57]
v_mov_b32 v92,s65
v_mov_b32 v93,s65
v_mov_b32 v94,s65
v_mov_b32 v95,s65
s_branch f_rm_P
f_mask_P_f:
v_mov_b32 v76,s65
v_mov_b32 v77,s65
v_mov_b32 v78,s65
v_mov_b32 v79,s65
v_mov_b32 v80,s65
v_mov_b32 v81,s65
v_mov_b32 v82,s65
v_mov_b32 v83,s65
v_mov_b32 v92,s65
v_mov_b32 v93,s65
v_mov_b32 v94,s65
v_mov_b32 v95,s65
v_mov_b32 v96,s65
v_mov_b32 v97,s65
v_mov_b32 v98,s65
v_mov_b32 v99,s65
s_branch f_rm_P
f_slow_Ao:
s_nop 9
v_max3_f32 v229,v100,v101,v102
v_max3_f32 v229,v229,v103,v116
v_max3_f32 v229,v229,v117,v118
v_max3_f32 v229,v229,v119,s62
v_mov_b32 v233,v229
s_nop 1
v_permlane32_swap_b32 v229,v233
v_max_f32 v229,v229,v233
v_mov_b32 v233,v229
s_nop 1
v_permlane16_swap_b32 v229,v233
v_max_f32 v229,v229,v233
v_add_f32 v229,1109393408,v229
v_max3_f32 v230,v104,v105,v106
v_max3_f32 v230,v230,v107,v120
v_max3_f32 v230,v230,v121,v122
v_max3_f32 v230,v230,v123,s62
v_mov_b32 v233,v230
s_nop 1
v_permlane32_swap_b32 v230,v233
v_max_f32 v230,v230,v233
v_mov_b32 v233,v230
s_nop 1
v_permlane16_swap_b32 v230,v233
v_max_f32 v230,v230,v233
v_add_f32 v230,1109393408,v230
v_max3_f32 v231,v108,v109,v110
v_max3_f32 v231,v231,v111,v124
v_max3_f32 v231,v231,v125,v126
v_max3_f32 v231,v231,v127,s62
v_mov_b32 v233,v231
s_nop 1
v_permlane32_swap_b32 v231,v233
v_max_f32 v231,v231,v233
v_mov_b32 v233,v231
s_nop 1
v_permlane16_swap_b32 v231,v233
v_max_f32 v231,v231,v233
v_add_f32 v231,1109393408,v231
v_max3_f32 v232,v112,v113,v114
v_max3_f32 v232,v232,v115,v128
v_max3_f32 v232,v232,v129,v130
v_max3_f32 v232,v232,v131,s62
v_mov_b32 v233,v232
s_nop 1
v_permlane32_swap_b32 v232,v233
v_max_f32 v232,v232,v233
v_mov_b32 v233,v232
s_nop 1
v_permlane16_swap_b32 v232,v233
v_max_f32 v232,v232,v233
v_add_f32 v232,1109393408,v232
E v234,-v229
v_sub_f32 v20,v20,v229
v_sub_f32 v21,v21,v229
v_sub_f32 v22,v22,v229
v_sub_f32 v23,v23,v229
v_sub_f32 v100,v100,v229
v_sub_f32 v101,v101,v229
v_sub_f32 v102,v102,v229
v_sub_f32 v103,v103,v229
v_sub_f32 v116,v116,v229
v_sub_f32 v117,v117,v229
v_sub_f32 v118,v118,v229
v_sub_f32 v119,v119,v229
v_sub_f32 v68,v68,v229
v_sub_f32 v69,v69,v229
v_sub_f32 v70,v70,v229
v_sub_f32 v71,v71,v229
v_sub_f32 v84,v84,v229
v_sub_f32 v85,v85,v229
v_sub_f32 v86,v86,v229
v_sub_f32 v87,v87,v229
v_mul_f32 v36,v36,v234
v_mul_f32 v37,v37,v234
v_mul_f32 v38,v38,v234
v_mul_f32 v39,v39,v234
AR v240,a0
v_mul_f32 v240,v240,v234
AW a0,v240
AR v241,a1
v_mul_f32 v241,v241,v234
AW a1,v241
AR v242,a2
v_mul_f32 v242,v242,v234
AW a2,v242
AR v243,a3
v_mul_f32 v243,v243,v234
AW a3,v243
AR v240,a4
v_mul_f32 v240,v240,v234
AW a4,v240
AR v241,a5
v_mul_f32 v241,v241,v234
AW a5,v241
AR v242,a6
v_mul_f32 v242,v242,v234
AW a6,v242
AR v243,a7
v_mul_f32 v243,v243,v234
AW a7,v243
AR v240,a8
v_mul_f32 v240,v240,v234
AW a8,v240
AR v241,a9
v_mul_f32 v241,v241,v234
AW a9,v241
AR v242,a10
v_mul_f32 v242,v242,v234
AW a10,v242
AR v243,a11
v_mul_f32 v243,v243,v234
AW a11,v243
AR v240,a12
v_mul_f32 v240,v240,v234
AW a12,v240
AR v241,a13
v_mul_f32 v241,v241,v234
AW a13,v241
AR v242,a14
v_mul_f32 v242,v242,v234
AW a14,v242
AR v243,a15
v_mul_f32 v243,v243,v234
AW a15,v243
AR v240,a16
v_mul_f32 v240,v240,v234
AW a16,v240
AR v241,a17
v_mul_f32 v241,v241,v234
AW a17,v241
AR v242,a18
v_mul_f32 v242,v242,v234
AW a18,v242
AR v243,a19
v_mul_f32 v243,v243,v234
AW a19,v243
AR v240,a20
v_mul_f32 v240,v240,v234
AW a20,v240
AR v241,a21
v_mul_f32 v241,v241,v234
AW a21,v241
AR v242,a22
v_mul_f32 v242,v242,v234
AW a22,v242
AR v243,a23
v_mul_f32 v243,v243,v234
AW a23,v243
AR v240,a24
v_mul_f32 v240,v240,v234
AW a24,v240
AR v241,a25
v_mul_f32 v241,v241,v234
AW a25,v241
AR v242,a26
v_mul_f32 v242,v242,v234
AW a26,v242
AR v243,a27
v_mul_f32 v243,v243,v234
AW a27,v243
AR v240,a28
v_mul_f32 v240,v240,v234
AW a28,v240
AR v241,a29
v_mul_f32 v241,v241,v234
AW a29,v241
AR v242,a30
v_mul_f32 v242,v242,v234
AW a30,v242
AR v243,a31
v_mul_f32 v243,v243,v234
AW a31,v243
E v235,-v230
v_sub_f32 v24,v24,v230
v_sub_f32 v25,v25,v230
v_sub_f32 v26,v26,v230
v_sub_f32 v27,v27,v230
v_sub_f32 v104,v104,v230
v_sub_f32 v105,v105,v230
v_sub_f32 v106,v106,v230
v_sub_f32 v107,v107,v230
v_sub_f32 v120,v120,v230
v_sub_f32 v121,v121,v230
v_sub_f32 v122,v122,v230
v_sub_f32 v123,v123,v230
v_sub_f32 v72,v72,v230
v_sub_f32 v73,v73,v230
v_sub_f32 v74,v74,v230
v_sub_f32 v75,v75,v230
v_sub_f32 v88,v88,v230
v_sub_f32 v89,v89,v230
v_sub_f32 v90,v90,v230
v_sub_f32 v91,v91,v230
v_mul_f32 v40,v40,v235
v_mul_f32 v41,v41,v235
v_mul_f32 v42,v42,v235
v_mul_f32 v43,v43,v235
AR v240,a32
v_mul_f32 v240,v240,v235
AW a32,v240
AR v241,a33
v_mul_f32 v241,v241,v235
AW a33,v241
AR v242,a34
v_mul_f32 v242,v242,v235
AW a34,v242
AR v243,a35
v_mul_f32 v243,v243,v235
AW a35,v243
AR v240,a36
v_mul_f32 v240,v240,v235
AW a36,v240
AR v241,a37
v_mul_f32 v241,v241,v235
AW a37,v241
AR v242,a38
v_mul_f32 v242,v242,v235
AW a38,v242
)"
R"(AR v243,a39
v_mul_f32 v243,v243,v235
AW a39,v243
AR v240,a40
v_mul_f32 v240,v240,v235
AW a40,v240
AR v241,a41
v_mul_f32 v241,v241,v235
AW a41,v241
AR v242,a42
v_mul_f32 v242,v242,v235
AW a42,v242
AR v243,a43
v_mul_f32 v243,v243,v235
AW a43,v243
AR v240,a44
v_mul_f32 v240,v240,v235
AW a44,v240
AR v241,a45
v_mul_f32 v241,v241,v235
AW a45,v241
AR v242,a46
v_mul_f32 v242,v242,v235
AW a46,v242
AR v243,a47
v_mul_f32 v243,v243,v235
AW a47,v243
AR v240,a48
v_mul_f32 v240,v240,v235
AW a48,v240
AR v241,a49
v_mul_f32 v241,v241,v235
AW a49,v241
AR v242,a50
v_mul_f32 v242,v242,v235
AW a50,v242
AR v243,a51
v_mul_f32 v243,v243,v235
AW a51,v243
AR v240,a52
v_mul_f32 v240,v240,v235
AW a52,v240
AR v241,a53
v_mul_f32 v241,v241,v235
AW a53,v241
AR v242,a54
v_mul_f32 v242,v242,v235
AW a54,v242
AR v243,a55
v_mul_f32 v243,v243,v235
AW a55,v243
AR v240,a56
v_mul_f32 v240,v240,v235
AW a56,v240
AR v241,a57
v_mul_f32 v241,v241,v235
AW a57,v241
AR v242,a58
v_mul_f32 v242,v242,v235
AW a58,v242
AR v243,a59
v_mul_f32 v243,v243,v235
AW a59,v243
AR v240,a60
v_mul_f32 v240,v240,v235
AW a60,v240
AR v241,a61
v_mul_f32 v241,v241,v235
AW a61,v241
AR v242,a62
v_mul_f32 v242,v242,v235
AW a62,v242
AR v243,a63
v_mul_f32 v243,v243,v235
AW a63,v243
E v236,-v231
v_sub_f32 v28,v28,v231
v_sub_f32 v29,v29,v231
v_sub_f32 v30,v30,v231
v_sub_f32 v31,v31,v231
v_sub_f32 v108,v108,v231
v_sub_f32 v109,v109,v231
v_sub_f32 v110,v110,v231
v_sub_f32 v111,v111,v231
v_sub_f32 v124,v124,v231
v_sub_f32 v125,v125,v231
v_sub_f32 v126,v126,v231
v_sub_f32 v127,v127,v231
v_sub_f32 v76,v76,v231
v_sub_f32 v77,v77,v231
v_sub_f32 v78,v78,v231
v_sub_f32 v79,v79,v231
v_sub_f32 v92,v92,v231
v_sub_f32 v93,v93,v231
v_sub_f32 v94,v94,v231
v_sub_f32 v95,v95,v231
v_mul_f32 v44,v44,v236
v_mul_f32 v45,v45,v236
v_mul_f32 v46,v46,v236
v_mul_f32 v47,v47,v236
v_mul_f32 v164,v164,v236
v_mul_f32 v165,v165,v236
v_mul_f32 v166,v166,v236
v_mul_f32 v167,v167,v236
v_mul_f32 v168,v168,v236
v_mul_f32 v169,v169,v236
v_mul_f32 v170,v170,v236
v_mul_f32 v171,v171,v236
v_mul_f32 v172,v172,v236
v_mul_f32 v173,v173,v236
v_mul_f32 v174,v174,v236
v_mul_f32 v175,v175,v236
v_mul_f32 v176,v176,v236
v_mul_f32 v177,v177,v236
v_mul_f32 v178,v178,v236
v_mul_f32 v179,v179,v236
v_mul_f32 v180,v180,v236
v_mul_f32 v181,v181,v236
v_mul_f32 v182,v182,v236
v_mul_f32 v183,v183,v236
v_mul_f32 v184,v184,v236
v_mul_f32 v185,v185,v236
v_mul_f32 v186,v186,v236
v_mul_f32 v187,v187,v236
v_mul_f32 v188,v188,v236
v_mul_f32 v189,v189,v236
v_mul_f32 v190,v190,v236
v_mul_f32 v191,v191,v236
v_mul_f32 v192,v192,v236
v_mul_f32 v193,v193,v236
v_mul_f32 v194,v194,v236
v_mul_f32 v195,v195,v236
E v237,-v232
v_sub_f32 v32,v32,v232
v_sub_f32 v33,v33,v232
v_sub_f32 v34,v34,v232
v_sub_f32 v35,v35,v232
v_sub_f32 v112,v112,v232
v_sub_f32 v113,v113,v232
v_sub_f32 v114,v114,v232
v_sub_f32 v115,v115,v232
v_sub_f32 v128,v128,v232
v_sub_f32 v129,v129,v232
v_sub_f32 v130,v130,v232
v_sub_f32 v131,v131,v232
v_sub_f32 v80,v80,v232
v_sub_f32 v81,v81,v232
v_sub_f32 v82,v82,v232
v_sub_f32 v83,v83,v232
v_sub_f32 v96,v96,v232
v_sub_f32 v97,v97,v232
v_sub_f32 v98,v98,v232
v_sub_f32 v99,v99,v232
v_mul_f32 v48,v48,v237
v_mul_f32 v49,v49,v237
v_mul_f32 v50,v50,v237
v_mul_f32 v51,v51,v237
v_mul_f32 v196,v196,v237
v_mul_f32 v197,v197,v237
v_mul_f32 v198,v198,v237
v_mul_f32 v199,v199,v237
v_mul_f32 v200,v200,v237
v_mul_f32 v201,v201,v237
v_mul_f32 v202,v202,v237
v_mul_f32 v203,v203,v237
v_mul_f32 v204,v204,v237
v_mul_f32 v205,v205,v237
v_mul_f32 v206,v206,v237
v_mul_f32 v207,v207,v237
v_mul_f32 v208,v208,v237
v_mul_f32 v209,v209,v237
v_mul_f32 v210,v210,v237
v_mul_f32 v211,v211,v237
v_mul_f32 v212,v212,v237
v_mul_f32 v213,v213,v237
v_mul_f32 v214,v214,v237
v_mul_f32 v215,v215,v237
v_mul_f32 v216,v216,v237
v_mul_f32 v217,v217,v237
v_mul_f32 v218,v218,v237
v_mul_f32 v219,v219,v237
v_mul_f32 v220,v220,v237
v_mul_f32 v221,v221,v237
v_mul_f32 v222,v222,v237
v_mul_f32 v223,v223,v237
v_mul_f32 v224,v224,v237
v_mul_f32 v225,v225,v237
v_mul_f32 v226,v226,v237
v_mul_f32 v227,v227,v237
E v8,v100
E v9,v101
E v10,v102
E v11,v103
C v148,v8,v9
C v149,v10,v11
E v12,v104
E v13,v105
E v14,v106
E v15,v107
C v152,v12,v13
C v153,v14,v15
E v8,v108
E v9,v109
E v10,v110
E v11,v111
C v156,v8,v9
C v157,v10,v11
E v12,v112
E v13,v113
E v14,v114
E v15,v115
C v160,v12,v13
C v161,v14,v15
E v8,v116
E v9,v117
E v10,v118
E v11,v119
C v150,v8,v9
C v151,v10,v11
E v12,v120
E v13,v121
E v14,v122
E v15,v123
C v154,v12,v13
C v155,v14,v15
E v8,v124
E v9,v125
E v10,v126
E v11,v127
C v158,v8,v9
C v159,v10,v11
E v12,v128
E v13,v129
E v14,v130
E v15,v131
C v162,v12,v13
C v163,v14,v15
M v[52:55],v[16:19],v[148:151],v[36:39]
M v[56:59],v[16:19],v[152:155],v[40:43]
M v[60:63],v[16:19],v[156:159],v[44:47]
M v[64:67],v[16:19],v[160:163],v[48:51]
s_nop 7
s_branch f_rs_Ao
f_mask_Ao:
s_nop 7
v_mov_b32 v238,s65
s_cmp_eq_u32 s52,s58
s_cbranch_scc0 f_mask_Ao_f
v_cndmask_b32 v108,v108,v238,s[44:45]
v_cndmask_b32 v109,v109,v238,s[46:47]
v_cndmask_b32 v110,v110,v238,s[54:55]
v_cndmask_b32 v111,v111,v238,s[56:57]
v_cndmask_b32 v128,v128,v238,s[44:45]
v_cndmask_b32 v129,v129,v238,s[46:47]
v_cndmask_b32 v130,v130,v238,s[54:55]
v_cndmask_b32 v131,v131,v238,s[56:57]
v_mov_b32 v124,s65
v_mov_b32 v125,s65
v_mov_b32 v126,s65
v_mov_b32 v127,s65
s_branch f_rm_Ao
f_mask_Ao_f:
v_mov_b32 v108,s65
v_mov_b32 v109,s65
v_mov_b32 v110,s65
v_mov_b32 v111,s65
v_mov_b32 v112,s65
v_mov_b32 v113,s65
v_mov_b32 v114,s65
v_mov_b32 v115,s65
v_mov_b32 v124,s65
v_mov_b32 v125,s65
v_mov_b32 v126,s65
v_mov_b32 v127,s65
v_mov_b32 v128,s65
v_mov_b32 v129,s65
v_mov_b32 v130,s65
v_mov_b32 v131,s65
s_branch f_rm_Ao
f_slow_Ae:
s_nop 9
v_max3_f32 v229,v68,v69,v70
v_max3_f32 v229,v229,v71,v84
v_max3_f32 v229,v229,v85,v86
v_max3_f32 v229,v229,v87,s62
v_mov_b32 v233,v229
s_nop 1
v_permlane32_swap_b32 v229,v233
v_max_f32 v229,v229,v233
v_mov_b32 v233,v229
s_nop 1
v_permlane16_swap_b32 v229,v233
v_max_f32 v229,v229,v233
v_add_f32 v229,1109393408,v229
v_max3_f32 v230,v72,v73,v74
v_max3_f32 v230,v230,v75,v88
v_max3_f32 v230,v230,v89,v90
v_max3_f32 v230,v230,v91,s62
v_mov_b32 v233,v230
s_nop 1
v_permlane32_swap_b32 v230,v233
v_max_f32 v230,v230,v233
v_mov_b32 v233,v230
s_nop 1
v_permlane16_swap_b32 v230,v233
v_max_f32 v230,v230,v233
v_add_f32 v230,1109393408,v230
v_max3_f32 v231,v76,v77,v78
v_max3_f32 v231,v231,v79,v92
v_max3_f32 v231,v231,v93,v94
v_max3_f32 v231,v231,v95,s62
v_mov_b32 v233,v231
s_nop 1
v_permlane32_swap_b32 v231,v233
v_max_f32 v231,v231,v233
v_mov_b32 v233,v231
s_nop 1
v_permlane16_swap_b32 v231,v233
v_max_f32 v231,v231,v233
v_add_f32 v231,1109393408,v231
v_max3_f32 v232,v80,v81,v82
v_max3_f32 v232,v232,v83,v96
v_max3_f32 v232,v232,v97,v98
v_max3_f32 v232,v232,v99,s62
v_mov_b32 v233,v232
s_nop 1
v_permlane32_swap_b32 v232,v233
v_max_f32 v232,v232,v233
v_mov_b32 v233,v232
s_nop 1
v_permlane16_swap_b32 v232,v233
v_max_f32 v232,v232,v233
v_add_f32 v232,1109393408,v232
E v234,-v229
v_sub_f32 v20,v20,v229
v_sub_f32 v21,v21,v229
v_sub_f32 v22,v22,v229
v_sub_f32 v23,v23,v229
v_sub_f32 v68,v68,v229
v_sub_f32 v69,v69,v229
v_sub_f32 v70,v70,v229
v_sub_f32 v71,v71,v229
v_sub_f32 v84,v84,v229
v_sub_f32 v85,v85,v229
v_sub_f32 v86,v86,v229
v_sub_f32 v87,v87,v229
v_sub_f32 v100,v100,v229
v_sub_f32 v101,v101,v229
v_sub_f32 v102,v102,v229
v_sub_f32 v103,v103,v229
v_sub_f32 v116,v116,v229
v_sub_f32 v117,v117,v229
v_sub_f32 v118,v118,v229
v_sub_f32 v119,v119,v229
v_mul_f32 v52,v52,v234
v_mul_f32 v53,v53,v234
v_mul_f32 v54,v54,v234
v_mul_f32 v55,v55,v234
AR v240,a0
v_mul_f32 v240,v240,v234
AW a0,v240
AR v241,a1
v_mul_f32 v241,v241,v234
AW a1,v241
AR v242,a2
v_mul_f32 v242,v242,v234
AW a2,v242
AR v243,a3
v_mul_f32 v243,v243,v234
AW a3,v243
AR v240,a4
v_mul_f32 v240,v240,v234
AW a4,v240
AR v241,a5
v_mul_f32 v241,v241,v234
AW a5,v241
AR v242,a6
v_mul_f32 v242,v242,v234
AW a6,v242
AR v243,a7
v_mul_f32 v243,v243,v234
AW a7,v243
AR v240,a8
v_mul_f32 v240,v240,v234
AW a8,v240
AR v241,a9
v_mul_f32 v241,v241,v234
AW a9,v241
AR v242,a10
v_mul_f32 v242,v242,v234
AW a10,v242
AR v243,a11
v_mul_f32 v243,v243,v234
AW a11,v243
AR v240,a12
v_mul_f32 v240,v240,v234
AW a12,v240
AR v241,a13
v_mul_f32 v241,v241,v234
AW a13,v241
)"
R"(AR v242,a14
v_mul_f32 v242,v242,v234
AW a14,v242
AR v243,a15
v_mul_f32 v243,v243,v234
AW a15,v243
AR v240,a16
v_mul_f32 v240,v240,v234
AW a16,v240
AR v241,a17
v_mul_f32 v241,v241,v234
AW a17,v241
AR v242,a18
v_mul_f32 v242,v242,v234
AW a18,v242
AR v243,a19
v_mul_f32 v243,v243,v234
AW a19,v243
AR v240,a20
v_mul_f32 v240,v240,v234
AW a20,v240
AR v241,a21
v_mul_f32 v241,v241,v234
AW a21,v241
AR v242,a22
v_mul_f32 v242,v242,v234
AW a22,v242
AR v243,a23
v_mul_f32 v243,v243,v234
AW a23,v243
AR v240,a24
v_mul_f32 v240,v240,v234
AW a24,v240
AR v241,a25
v_mul_f32 v241,v241,v234
AW a25,v241
AR v242,a26
v_mul_f32 v242,v242,v234
AW a26,v242
AR v243,a27
v_mul_f32 v243,v243,v234
AW a27,v243
AR v240,a28
v_mul_f32 v240,v240,v234
AW a28,v240
AR v241,a29
v_mul_f32 v241,v241,v234
AW a29,v241
AR v242,a30
v_mul_f32 v242,v242,v234
AW a30,v242
AR v243,a31
v_mul_f32 v243,v243,v234
AW a31,v243
E v235,-v230
v_sub_f32 v24,v24,v230
v_sub_f32 v25,v25,v230
v_sub_f32 v26,v26,v230
v_sub_f32 v27,v27,v230
v_sub_f32 v72,v72,v230
v_sub_f32 v73,v73,v230
v_sub_f32 v74,v74,v230
v_sub_f32 v75,v75,v230
v_sub_f32 v88,v88,v230
v_sub_f32 v89,v89,v230
v_sub_f32 v90,v90,v230
v_sub_f32 v91,v91,v230
v_sub_f32 v104,v104,v230
v_sub_f32 v105,v105,v230
v_sub_f32 v106,v106,v230
v_sub_f32 v107,v107,v230
v_sub_f32 v120,v120,v230
v_sub_f32 v121,v121,v230
v_sub_f32 v122,v122,v230
v_sub_f32 v123,v123,v230
v_mul_f32 v56,v56,v235
v_mul_f32 v57,v57,v235
v_mul_f32 v58,v58,v235
v_mul_f32 v59,v59,v235
AR v240,a32
v_mul_f32 v240,v240,v235
AW a32,v240
AR v241,a33
v_mul_f32 v241,v241,v235
AW a33,v241
AR v242,a34
v_mul_f32 v242,v242,v235
AW a34,v242
AR v243,a35
v_mul_f32 v243,v243,v235
AW a35,v243
AR v240,a36
v_mul_f32 v240,v240,v235
AW a36,v240
AR v241,a37
v_mul_f32 v241,v241,v235
AW a37,v241
AR v242,a38
v_mul_f32 v242,v242,v235
AW a38,v242
AR v243,a39
v_mul_f32 v243,v243,v235
AW a39,v243
AR v240,a40
v_mul_f32 v240,v240,v235
AW a40,v240
AR v241,a41
v_mul_f32 v241,v241,v235
AW a41,v241
AR v242,a42
v_mul_f32 v242,v242,v235
AW a42,v242
AR v243,a43
v_mul_f32 v243,v243,v235
AW a43,v243
AR v240,a44
v_mul_f32 v240,v240,v235
AW a44,v240
AR v241,a45
v_mul_f32 v241,v241,v235
AW a45,v241
AR v242,a46
v_mul_f32 v242,v242,v235
AW a46,v242
AR v243,a47
v_mul_f32 v243,v243,v235
AW a47,v243
AR v240,a48
v_mul_f32 v240,v240,v235
AW a48,v240
AR v241,a49
v_mul_f32 v241,v241,v235
AW a49,v241
AR v242,a50
v_mul_f32 v242,v242,v235
AW a50,v242
AR v243,a51
v_mul_f32 v243,v243,v235
AW a51,v243
AR v240,a52
v_mul_f32 v240,v240,v235
AW a52,v240
AR v241,a53
v_mul_f32 v241,v241,v235
AW a53,v241
AR v242,a54
v_mul_f32 v242,v242,v235
AW a54,v242
AR v243,a55
v_mul_f32 v243,v243,v235
AW a55,v243
AR v240,a56
v_mul_f32 v240,v240,v235
AW a56,v240
AR v241,a57
v_mul_f32 v241,v241,v235
AW a57,v241
AR v242,a58
v_mul_f32 v242,v242,v235
AW a58,v242
AR v243,a59
v_mul_f32 v243,v243,v235
AW a59,v243
AR v240,a60
v_mul_f32 v240,v240,v235
AW a60,v240
AR v241,a61
v_mul_f32 v241,v241,v235
AW a61,v241
AR v242,a62
v_mul_f32 v242,v242,v235
AW a62,v242
AR v243,a63
v_mul_f32 v243,v243,v235
AW a63,v243
E v236,-v231
v_sub_f32 v28,v28,v231
v_sub_f32 v29,v29,v231
v_sub_f32 v30,v30,v231
v_sub_f32 v31,v31,v231
v_sub_f32 v76,v76,v231
v_sub_f32 v77,v77,v231
v_sub_f32 v78,v78,v231
v_sub_f32 v79,v79,v231
v_sub_f32 v92,v92,v231
v_sub_f32 v93,v93,v231
v_sub_f32 v94,v94,v231
v_sub_f32 v95,v95,v231
v_sub_f32 v108,v108,v231
v_sub_f32 v109,v109,v231
v_sub_f32 v110,v110,v231
v_sub_f32 v111,v111,v231
v_sub_f32 v124,v124,v231
v_sub_f32 v125,v125,v231
v_sub_f32 v126,v126,v231
v_sub_f32 v127,v127,v231
v_mul_f32 v60,v60,v236
v_mul_f32 v61,v61,v236
v_mul_f32 v62,v62,v236
v_mul_f32 v63,v63,v236
v_mul_f32 v164,v164,v236
v_mul_f32 v165,v165,v236
v_mul_f32 v166,v166,v236
v_mul_f32 v167,v167,v236
v_mul_f32 v168,v168,v236
v_mul_f32 v169,v169,v236
v_mul_f32 v170,v170,v236
v_mul_f32 v171,v171,v236
v_mul_f32 v172,v172,v236
v_mul_f32 v173,v173,v236
v_mul_f32 v174,v174,v236
v_mul_f32 v175,v175,v236
v_mul_f32 v176,v176,v236
v_mul_f32 v177,v177,v236
v_mul_f32 v178,v178,v236
v_mul_f32 v179,v179,v236
v_mul_f32 v180,v180,v236
v_mul_f32 v181,v181,v236
v_mul_f32 v182,v182,v236
v_mul_f32 v183,v183,v236
v_mul_f32 v184,v184,v236
v_mul_f32 v185,v185,v236
v_mul_f32 v186,v186,v236
v_mul_f32 v187,v187,v236
v_mul_f32 v188,v188,v236
v_mul_f32 v189,v189,v236
v_mul_f32 v190,v190,v236
v_mul_f32 v191,v191,v236
v_mul_f32 v192,v192,v236
v_mul_f32 v193,v193,v236
v_mul_f32 v194,v194,v236
v_mul_f32 v195,v195,v236
E v237,-v232
v_sub_f32 v32,v32,v232
v_sub_f32 v33,v33,v232
v_sub_f32 v34,v34,v232
v_sub_f32 v35,v35,v232
v_sub_f32 v80,v80,v232
v_sub_f32 v81,v81,v232
v_sub_f32 v82,v82,v232
v_sub_f32 v83,v83,v232
v_sub_f32 v96,v96,v232
v_sub_f32 v97,v97,v232
v_sub_f32 v98,v98,v232
v_sub_f32 v99,v99,v232
v_sub_f32 v112,v112,v232
v_sub_f32 v113,v113,v232
v_sub_f32 v114,v114,v232
v_sub_f32 v115,v115,v232
v_sub_f32 v128,v128,v232
v_sub_f32 v129,v129,v232
v_sub_f32 v130,v130,v232
v_sub_f32 v131,v131,v232
v_mul_f32 v64,v64,v237
v_mul_f32 v65,v65,v237
v_mul_f32 v66,v66,v237
v_mul_f32 v67,v67,v237
v_mul_f32 v196,v196,v237
v_mul_f32 v197,v197,v237
v_mul_f32 v198,v198,v237
v_mul_f32 v199,v199,v237
v_mul_f32 v200,v200,v237
v_mul_f32 v201,v201,v237
v_mul_f32 v202,v202,v237
v_mul_f32 v203,v203,v237
v_mul_f32 v204,v204,v237
v_mul_f32 v205,v205,v237
v_mul_f32 v206,v206,v237
v_mul_f32 v207,v207,v237
v_mul_f32 v208,v208,v237
v_mul_f32 v209,v209,v237
v_mul_f32 v210,v210,v237
v_mul_f32 v211,v211,v237
v_mul_f32 v212,v212,v237
v_mul_f32 v213,v213,v237
v_mul_f32 v214,v214,v237
v_mul_f32 v215,v215,v237
v_mul_f32 v216,v216,v237
v_mul_f32 v217,v217,v237
v_mul_f32 v218,v218,v237
v_mul_f32 v219,v219,v237
v_mul_f32 v220,v220,v237
v_mul_f32 v221,v221,v237
v_mul_f32 v222,v222,v237
v_mul_f32 v223,v223,v237
v_mul_f32 v224,v224,v237
v_mul_f32 v225,v225,v237
v_mul_f32 v226,v226,v237
v_mul_f32 v227,v227,v237
E v8,v68
E v9,v69
E v10,v70
E v11,v71
C v132,v8,v9
C v133,v10,v11
E v12,v72
E v13,v73
E v14,v74
E v15,v75
C v136,v12,v13
C v137,v14,v15
E v8,v76
E v9,v77
E v10,v78
E v11,v79
C v140,v8,v9
C v141,v10,v11
E v12,v80
E v13,v81
E v14,v82
E v15,v83
C v144,v12,v13
C v145,v14,v15
E v8,v84
E v9,v85
E v10,v86
E v11,v87
C v134,v8,v9
C v135,v10,v11
E v12,v88
E v13,v89
E v14,v90
E v15,v91
C v138,v12,v13
C v139,v14,v15
E v8,v92
E v9,v93
E v10,v94
E v11,v95
C v142,v8,v9
C v143,v10,v11
E v12,v96
E v13,v97
E v14,v98
E v15,v99
C v146,v12,v13
C v147,v14,v15
M v[36:39],v[16:19],v[132:135],v[52:55]
M v[40:43],v[16:19],v[136:139],v[56:59]
M v[44:47],v[16:19],v[140:143],v[60:63]
M v[48:51],v[16:19],v[144:147],v[64:67]
s_nop 7
s_branch f_rs_Ae
f_mask_Ae:
s_nop 7
v_mov_b32 v238,s65
s_cmp_eq_u32 s52,s58
s_cbranch_scc0 f_mask_Ae_f
v_cndmask_b32 v76,v76,v238,s[44:45]
v_cndmask_b32 v77,v77,v238,s[46:47]
v_cndmask_b32 v78,v78,v238,s[54:55]
v_cndmask_b32 v79,v79,v238,s[56:57]
v_cndmask_b32 v96,v96,v238,s[44:45]
v_cndmask_b32 v97,v97,v238,s[46:47]
v_cndmask_b32 v98,v98,v238,s[54:55]
v_cndmask_b32 v99,v99,v238,s[56:57]
v_mov_b32 v92,s65
v_mov_b32 v93,s65
v_mov_b32 v94,s65
v_mov_b32 v95,s65
s_branch f_rm_Ae
f_mask_Ae_f:
v_mov_b32 v76,s65
v_mov_b32 v77,s65
v_mov_b32 v78,s65
v_mov_b32 v79,s65
v_mov_b32 v80,s65
v_mov_b32 v81,s65
v_mov_b32 v82,s65
v_mov_b32 v83,s65
v_mov_b32 v92,s65
v_mov_b32 v93,s65
v_mov_b32 v94,s65
v_mov_b32 v95,s65
v_mov_b32 v96,s65
v_mov_b32 v97,s65
v_mov_b32 v98,s65
v_mov_b32 v99,s65
s_branch f_rm_Ae
f_slow_B:
s_nop 9
v_max3_f32 v229,v100,v101,v102
v_max3_f32 v229,v229,v103,v116
v_max3_f32 v229,v229,v117,v118
v_max3_f32 v229,v229,v119,s62
v_mov_b32 v233,v229
s_nop 1
v_permlane32_swap_b32 v229,v233
v_max_f32 v229,v229,v233
v_mov_b32 v233,v229
s_nop 1
v_permlane16_swap_b32 v229,v233
v_max_f32 v229,v229,v233
v_add_f32 v229,1109393408,v229
v_max3_f32 v230,v104,v105,v106
v_max3_f32 v230,v230,v107,v120
v_max3_f32 v230,v230,v121,v122
v_max3_f32 v230,v230,v123,s62
v_mov_b32 v233,v230
s_nop 1
)"
R"(v_permlane32_swap_b32 v230,v233
v_max_f32 v230,v230,v233
v_mov_b32 v233,v230
s_nop 1
v_permlane16_swap_b32 v230,v233
v_max_f32 v230,v230,v233
v_add_f32 v230,1109393408,v230
v_max3_f32 v231,v108,v109,v110
v_max3_f32 v231,v231,v111,v124
v_max3_f32 v231,v231,v125,v126
v_max3_f32 v231,v231,v127,s62
v_mov_b32 v233,v231
s_nop 1
v_permlane32_swap_b32 v231,v233
v_max_f32 v231,v231,v233
v_mov_b32 v233,v231
s_nop 1
v_permlane16_swap_b32 v231,v233
v_max_f32 v231,v231,v233
v_add_f32 v231,1109393408,v231
v_max3_f32 v232,v112,v113,v114
v_max3_f32 v232,v232,v115,v128
v_max3_f32 v232,v232,v129,v130
v_max3_f32 v232,v232,v131,s62
v_mov_b32 v233,v232
s_nop 1
v_permlane32_swap_b32 v232,v233
v_max_f32 v232,v232,v233
v_mov_b32 v233,v232
s_nop 1
v_permlane16_swap_b32 v232,v233
v_max_f32 v232,v232,v233
v_add_f32 v232,1109393408,v232
E v234,-v229
v_sub_f32 v20,v20,v229
v_sub_f32 v21,v21,v229
v_sub_f32 v22,v22,v229
v_sub_f32 v23,v23,v229
v_sub_f32 v100,v100,v229
v_sub_f32 v101,v101,v229
v_sub_f32 v102,v102,v229
v_sub_f32 v103,v103,v229
v_sub_f32 v116,v116,v229
v_sub_f32 v117,v117,v229
v_sub_f32 v118,v118,v229
v_sub_f32 v119,v119,v229
v_sub_f32 v68,v68,v229
v_sub_f32 v69,v69,v229
v_sub_f32 v70,v70,v229
v_sub_f32 v71,v71,v229
v_sub_f32 v76,v76,v229
v_sub_f32 v77,v77,v229
v_sub_f32 v78,v78,v229
v_sub_f32 v79,v79,v229
v_sub_f32 v84,v84,v229
v_sub_f32 v85,v85,v229
v_sub_f32 v86,v86,v229
v_sub_f32 v87,v87,v229
v_sub_f32 v92,v92,v229
v_sub_f32 v93,v93,v229
v_sub_f32 v94,v94,v229
v_sub_f32 v95,v95,v229
v_mul_f32 v36,v36,v234
v_mul_f32 v37,v37,v234
v_mul_f32 v38,v38,v234
v_mul_f32 v39,v39,v234
AR v240,a0
v_mul_f32 v240,v240,v234
AW a0,v240
AR v241,a1
v_mul_f32 v241,v241,v234
AW a1,v241
AR v242,a2
v_mul_f32 v242,v242,v234
AW a2,v242
AR v243,a3
v_mul_f32 v243,v243,v234
AW a3,v243
AR v240,a4
v_mul_f32 v240,v240,v234
AW a4,v240
AR v241,a5
v_mul_f32 v241,v241,v234
AW a5,v241
AR v242,a6
v_mul_f32 v242,v242,v234
AW a6,v242
AR v243,a7
v_mul_f32 v243,v243,v234
AW a7,v243
AR v240,a8
v_mul_f32 v240,v240,v234
AW a8,v240
AR v241,a9
v_mul_f32 v241,v241,v234
AW a9,v241
AR v242,a10
v_mul_f32 v242,v242,v234
AW a10,v242
AR v243,a11
v_mul_f32 v243,v243,v234
AW a11,v243
AR v240,a12
v_mul_f32 v240,v240,v234
AW a12,v240
AR v241,a13
v_mul_f32 v241,v241,v234
AW a13,v241
AR v242,a14
v_mul_f32 v242,v242,v234
AW a14,v242
AR v243,a15
v_mul_f32 v243,v243,v234
AW a15,v243
AR v240,a16
v_mul_f32 v240,v240,v234
AW a16,v240
AR v241,a17
v_mul_f32 v241,v241,v234
AW a17,v241
AR v242,a18
v_mul_f32 v242,v242,v234
AW a18,v242
AR v243,a19
v_mul_f32 v243,v243,v234
AW a19,v243
AR v240,a20
v_mul_f32 v240,v240,v234
AW a20,v240
AR v241,a21
v_mul_f32 v241,v241,v234
AW a21,v241
AR v242,a22
v_mul_f32 v242,v242,v234
AW a22,v242
AR v243,a23
v_mul_f32 v243,v243,v234
AW a23,v243
AR v240,a24
v_mul_f32 v240,v240,v234
AW a24,v240
AR v241,a25
v_mul_f32 v241,v241,v234
AW a25,v241
AR v242,a26
v_mul_f32 v242,v242,v234
AW a26,v242
AR v243,a27
v_mul_f32 v243,v243,v234
AW a27,v243
AR v240,a28
v_mul_f32 v240,v240,v234
AW a28,v240
AR v241,a29
v_mul_f32 v241,v241,v234
AW a29,v241
AR v242,a30
v_mul_f32 v242,v242,v234
AW a30,v242
AR v243,a31
v_mul_f32 v243,v243,v234
AW a31,v243
E v235,-v230
v_sub_f32 v24,v24,v230
v_sub_f32 v25,v25,v230
v_sub_f32 v26,v26,v230
v_sub_f32 v27,v27,v230
v_sub_f32 v104,v104,v230
v_sub_f32 v105,v105,v230
v_sub_f32 v106,v106,v230
v_sub_f32 v107,v107,v230
v_sub_f32 v120,v120,v230
v_sub_f32 v121,v121,v230
v_sub_f32 v122,v122,v230
v_sub_f32 v123,v123,v230
v_sub_f32 v72,v72,v230
v_sub_f32 v73,v73,v230
v_sub_f32 v74,v74,v230
v_sub_f32 v75,v75,v230
v_sub_f32 v80,v80,v230
v_sub_f32 v81,v81,v230
v_sub_f32 v82,v82,v230
v_sub_f32 v83,v83,v230
v_sub_f32 v88,v88,v230
v_sub_f32 v89,v89,v230
v_sub_f32 v90,v90,v230
v_sub_f32 v91,v91,v230
v_sub_f32 v96,v96,v230
v_sub_f32 v97,v97,v230
v_sub_f32 v98,v98,v230
v_sub_f32 v99,v99,v230
v_mul_f32 v40,v40,v235
v_mul_f32 v41,v41,v235
v_mul_f32 v42,v42,v235
v_mul_f32 v43,v43,v235
AR v240,a32
v_mul_f32 v240,v240,v235
AW a32,v240
AR v241,a33
v_mul_f32 v241,v241,v235
AW a33,v241
AR v242,a34
v_mul_f32 v242,v242,v235
AW a34,v242
AR v243,a35
v_mul_f32 v243,v243,v235
AW a35,v243
AR v240,a36
v_mul_f32 v240,v240,v235
AW a36,v240
AR v241,a37
v_mul_f32 v241,v241,v235
AW a37,v241
AR v242,a38
v_mul_f32 v242,v242,v235
AW a38,v242
AR v243,a39
v_mul_f32 v243,v243,v235
AW a39,v243
AR v240,a40
v_mul_f32 v240,v240,v235
AW a40,v240
AR v241,a41
v_mul_f32 v241,v241,v235
AW a41,v241
AR v242,a42
v_mul_f32 v242,v242,v235
AW a42,v242
AR v243,a43
v_mul_f32 v243,v243,v235
AW a43,v243
AR v240,a44
v_mul_f32 v240,v240,v235
AW a44,v240
AR v241,a45
v_mul_f32 v241,v241,v235
AW a45,v241
AR v242,a46
v_mul_f32 v242,v242,v235
AW a46,v242
AR v243,a47
v_mul_f32 v243,v243,v235
AW a47,v243
AR v240,a48
v_mul_f32 v240,v240,v235
AW a48,v240
AR v241,a49
v_mul_f32 v241,v241,v235
AW a49,v241
AR v242,a50
v_mul_f32 v242,v242,v235
AW a50,v242
AR v243,a51
v_mul_f32 v243,v243,v235
AW a51,v243
AR v240,a52
v_mul_f32 v240,v240,v235
AW a52,v240
AR v241,a53
v_mul_f32 v241,v241,v235
AW a53,v241
AR v242,a54
v_mul_f32 v242,v242,v235
AW a54,v242
AR v243,a55
v_mul_f32 v243,v243,v235
AW a55,v243
AR v240,a56
v_mul_f32 v240,v240,v235
AW a56,v240
AR v241,a57
v_mul_f32 v241,v241,v235
AW a57,v241
AR v242,a58
v_mul_f32 v242,v242,v235
AW a58,v242
AR v243,a59
v_mul_f32 v243,v243,v235
AW a59,v243
AR v240,a60
v_mul_f32 v240,v240,v235
AW a60,v240
AR v241,a61
v_mul_f32 v241,v241,v235
AW a61,v241
AR v242,a62
v_mul_f32 v242,v242,v235
AW a62,v242
AR v243,a63
v_mul_f32 v243,v243,v235
AW a63,v243
E v236,-v231
v_sub_f32 v28,v28,v231
v_sub_f32 v29,v29,v231
v_sub_f32 v30,v30,v231
v_sub_f32 v31,v31,v231
v_sub_f32 v108,v108,v231
v_sub_f32 v109,v109,v231
v_sub_f32 v110,v110,v231
v_sub_f32 v111,v111,v231
v_sub_f32 v124,v124,v231
v_sub_f32 v125,v125,v231
v_sub_f32 v126,v126,v231
v_sub_f32 v127,v127,v231
v_mul_f32 v44,v44,v236
v_mul_f32 v45,v45,v236
v_mul_f32 v46,v46,v236
v_mul_f32 v47,v47,v236
v_mul_f32 v164,v164,v236
v_mul_f32 v165,v165,v236
v_mul_f32 v166,v166,v236
v_mul_f32 v167,v167,v236
v_mul_f32 v168,v168,v236
v_mul_f32 v169,v169,v236
v_mul_f32 v170,v170,v236
v_mul_f32 v171,v171,v236
v_mul_f32 v172,v172,v236
v_mul_f32 v173,v173,v236
v_mul_f32 v174,v174,v236
v_mul_f32 v175,v175,v236
v_mul_f32 v176,v176,v236
v_mul_f32 v177,v177,v236
v_mul_f32 v178,v178,v236
v_mul_f32 v179,v179,v236
v_mul_f32 v180,v180,v236
v_mul_f32 v181,v181,v236
v_mul_f32 v182,v182,v236
v_mul_f32 v183,v183,v236
v_mul_f32 v184,v184,v236
v_mul_f32 v185,v185,v236
v_mul_f32 v186,v186,v236
v_mul_f32 v187,v187,v236
v_mul_f32 v188,v188,v236
v_mul_f32 v189,v189,v236
v_mul_f32 v190,v190,v236
v_mul_f32 v191,v191,v236
v_mul_f32 v192,v192,v236
v_mul_f32 v193,v193,v236
v_mul_f32 v194,v194,v236
v_mul_f32 v195,v195,v236
E v237,-v232
v_sub_f32 v32,v32,v232
v_sub_f32 v33,v33,v232
v_sub_f32 v34,v34,v232
v_sub_f32 v35,v35,v232
v_sub_f32 v112,v112,v232
v_sub_f32 v113,v113,v232
v_sub_f32 v114,v114,v232
v_sub_f32 v115,v115,v232
v_sub_f32 v128,v128,v232
v_sub_f32 v129,v129,v232
v_sub_f32 v130,v130,v232
v_sub_f32 v131,v131,v232
v_mul_f32 v48,v48,v237
v_mul_f32 v49,v49,v237
v_mul_f32 v50,v50,v237
v_mul_f32 v51,v51,v237
v_mul_f32 v196,v196,v237
v_mul_f32 v197,v197,v237
v_mul_f32 v198,v198,v237
v_mul_f32 v199,v199,v237
v_mul_f32 v200,v200,v237
v_mul_f32 v201,v201,v237
v_mul_f32 v202,v202,v237
v_mul_f32 v203,v203,v237
v_mul_f32 v204,v204,v237
v_mul_f32 v205,v205,v237
v_mul_f32 v206,v206,v237
v_mul_f32 v207,v207,v237
v_mul_f32 v208,v208,v237
v_mul_f32 v209,v209,v237
v_mul_f32 v210,v210,v237
v_mul_f32 v211,v211,v237
v_mul_f32 v212,v212,v237
v_mul_f32 v213,v213,v237
v_mul_f32 v214,v214,v237
v_mul_f32 v215,v215,v237
v_mul_f32 v216,v216,v237
v_mul_f32 v217,v217,v237
v_mul_f32 v218,v218,v237
v_mul_f32 v219,v219,v237
v_mul_f32 v220,v220,v237
v_mul_f32 v221,v221,v237
v_mul_f32 v222,v222,v237
v_mul_f32 v223,v223,v237
v_mul_f32 v224,v224,v237
v_mul_f32 v225,v225,v237
v_mul_f32 v226,v226,v237
v_mul_f32 v227,v227,v237
E v8,v100
E v9,v101
E v10,v102
E v11,v103
C v148,v8,v9
C v149,v10,v11
E v12,v104
E v13,v105
E v14,v106
E v15,v107
C v152,v12,v13
)"
R"(C v153,v14,v15
E v8,v108
E v9,v109
E v10,v110
E v11,v111
C v156,v8,v9
C v157,v10,v11
E v12,v112
E v13,v113
E v14,v114
E v15,v115
C v160,v12,v13
C v161,v14,v15
E v8,v116
E v9,v117
E v10,v118
E v11,v119
C v150,v8,v9
C v151,v10,v11
E v12,v120
E v13,v121
E v14,v122
E v15,v123
C v154,v12,v13
C v155,v14,v15
E v8,v124
E v9,v125
E v10,v126
E v11,v127
C v158,v8,v9
C v159,v10,v11
E v12,v128
E v13,v129
E v14,v130
E v15,v131
C v162,v12,v13
C v163,v14,v15
M v[52:55],v[16:19],v[148:151],v[36:39]
M v[56:59],v[16:19],v[152:155],v[40:43]
M v[60:63],v[16:19],v[156:159],v[44:47]
M v[64:67],v[16:19],v[160:163],v[48:51]
s_nop 7
s_branch f_rs_B
f_mask_B:
s_nop 7
v_mov_b32 v238,s65
s_cmp_eq_u32 s52,s58
s_cbranch_scc0 f_mask_B_f
v_cndmask_b32 v108,v108,v238,s[44:45]
v_cndmask_b32 v109,v109,v238,s[46:47]
v_cndmask_b32 v110,v110,v238,s[54:55]
v_cndmask_b32 v111,v111,v238,s[56:57]
v_cndmask_b32 v128,v128,v238,s[44:45]
v_cndmask_b32 v129,v129,v238,s[46:47]
v_cndmask_b32 v130,v130,v238,s[54:55]
v_cndmask_b32 v131,v131,v238,s[56:57]
v_mov_b32 v124,s65
v_mov_b32 v125,s65
v_mov_b32 v126,s65
v_mov_b32 v127,s65
s_branch f_rm_B
f_mask_B_f:
v_mov_b32 v108,s65
v_mov_b32 v109,s65
v_mov_b32 v110,s65
v_mov_b32 v111,s65
v_mov_b32 v112,s65
v_mov_b32 v113,s65
v_mov_b32 v114,s65
v_mov_b32 v115,s65
v_mov_b32 v124,s65
v_mov_b32 v125,s65
v_mov_b32 v126,s65
v_mov_b32 v127,s65
v_mov_b32 v128,s65
v_mov_b32 v129,s65
v_mov_b32 v130,s65
v_mov_b32 v131,s65
s_branch f_rm_B
f_slow_C:
s_nop 9
v_max3_f32 v229,v68,v69,v70
v_max3_f32 v229,v229,v71,v76
v_max3_f32 v229,v229,v77,v78
v_max3_f32 v229,v229,v79,v84
v_max3_f32 v229,v229,v85,v86
v_max3_f32 v229,v229,v87,v92
v_max3_f32 v229,v229,v93,v94
v_max3_f32 v229,v229,v95,s62
v_mov_b32 v233,v229
s_nop 1
v_permlane32_swap_b32 v229,v233
v_max_f32 v229,v229,v233
v_mov_b32 v233,v229
s_nop 1
v_permlane16_swap_b32 v229,v233
v_max_f32 v229,v229,v233
v_add_f32 v229,1109393408,v229
v_max3_f32 v230,v72,v73,v74
v_max3_f32 v230,v230,v75,v80
v_max3_f32 v230,v230,v81,v82
v_max3_f32 v230,v230,v83,v88
v_max3_f32 v230,v230,v89,v90
v_max3_f32 v230,v230,v91,v96
v_max3_f32 v230,v230,v97,v98
v_max3_f32 v230,v230,v99,s62
v_mov_b32 v233,v230
s_nop 1
v_permlane32_swap_b32 v230,v233
v_max_f32 v230,v230,v233
v_mov_b32 v233,v230
s_nop 1
v_permlane16_swap_b32 v230,v233
v_max_f32 v230,v230,v233
v_add_f32 v230,1109393408,v230
E v234,-v229
v_sub_f32 v20,v20,v229
v_sub_f32 v21,v21,v229
v_sub_f32 v22,v22,v229
v_sub_f32 v23,v23,v229
v_sub_f32 v68,v68,v229
v_sub_f32 v69,v69,v229
v_sub_f32 v70,v70,v229
v_sub_f32 v71,v71,v229
v_sub_f32 v76,v76,v229
v_sub_f32 v77,v77,v229
v_sub_f32 v78,v78,v229
v_sub_f32 v79,v79,v229
v_sub_f32 v84,v84,v229
v_sub_f32 v85,v85,v229
v_sub_f32 v86,v86,v229
v_sub_f32 v87,v87,v229
v_sub_f32 v92,v92,v229
v_sub_f32 v93,v93,v229
v_sub_f32 v94,v94,v229
v_sub_f32 v95,v95,v229
v_sub_f32 v100,v100,v229
v_sub_f32 v101,v101,v229
v_sub_f32 v102,v102,v229
v_sub_f32 v103,v103,v229
v_sub_f32 v108,v108,v229
v_sub_f32 v109,v109,v229
v_sub_f32 v110,v110,v229
v_sub_f32 v111,v111,v229
v_sub_f32 v116,v116,v229
v_sub_f32 v117,v117,v229
v_sub_f32 v118,v118,v229
v_sub_f32 v119,v119,v229
v_sub_f32 v124,v124,v229
v_sub_f32 v125,v125,v229
v_sub_f32 v126,v126,v229
v_sub_f32 v127,v127,v229
v_mul_f32 v52,v52,v234
v_mul_f32 v53,v53,v234
v_mul_f32 v54,v54,v234
v_mul_f32 v55,v55,v234
AR v240,a0
v_mul_f32 v240,v240,v234
AW a0,v240
AR v241,a1
v_mul_f32 v241,v241,v234
AW a1,v241
AR v242,a2
v_mul_f32 v242,v242,v234
AW a2,v242
AR v243,a3
v_mul_f32 v243,v243,v234
AW a3,v243
AR v240,a4
v_mul_f32 v240,v240,v234
AW a4,v240
AR v241,a5
v_mul_f32 v241,v241,v234
AW a5,v241
AR v242,a6
v_mul_f32 v242,v242,v234
AW a6,v242
AR v243,a7
v_mul_f32 v243,v243,v234
AW a7,v243
AR v240,a8
v_mul_f32 v240,v240,v234
AW a8,v240
AR v241,a9
v_mul_f32 v241,v241,v234
AW a9,v241
AR v242,a10
v_mul_f32 v242,v242,v234
AW a10,v242
AR v243,a11
v_mul_f32 v243,v243,v234
AW a11,v243
AR v240,a12
v_mul_f32 v240,v240,v234
AW a12,v240
AR v241,a13
v_mul_f32 v241,v241,v234
AW a13,v241
AR v242,a14
v_mul_f32 v242,v242,v234
AW a14,v242
AR v243,a15
v_mul_f32 v243,v243,v234
AW a15,v243
AR v240,a16
v_mul_f32 v240,v240,v234
AW a16,v240
AR v241,a17
v_mul_f32 v241,v241,v234
AW a17,v241
AR v242,a18
v_mul_f32 v242,v242,v234
AW a18,v242
AR v243,a19
v_mul_f32 v243,v243,v234
AW a19,v243
AR v240,a20
v_mul_f32 v240,v240,v234
AW a20,v240
AR v241,a21
v_mul_f32 v241,v241,v234
AW a21,v241
AR v242,a22
v_mul_f32 v242,v242,v234
AW a22,v242
AR v243,a23
v_mul_f32 v243,v243,v234
AW a23,v243
AR v240,a24
v_mul_f32 v240,v240,v234
AW a24,v240
AR v241,a25
v_mul_f32 v241,v241,v234
AW a25,v241
AR v242,a26
v_mul_f32 v242,v242,v234
AW a26,v242
AR v243,a27
v_mul_f32 v243,v243,v234
AW a27,v243
AR v240,a28
v_mul_f32 v240,v240,v234
AW a28,v240
AR v241,a29
v_mul_f32 v241,v241,v234
AW a29,v241
AR v242,a30
v_mul_f32 v242,v242,v234
AW a30,v242
AR v243,a31
v_mul_f32 v243,v243,v234
AW a31,v243
E v235,-v230
v_sub_f32 v24,v24,v230
v_sub_f32 v25,v25,v230
v_sub_f32 v26,v26,v230
v_sub_f32 v27,v27,v230
v_sub_f32 v72,v72,v230
v_sub_f32 v73,v73,v230
v_sub_f32 v74,v74,v230
v_sub_f32 v75,v75,v230
v_sub_f32 v80,v80,v230
v_sub_f32 v81,v81,v230
v_sub_f32 v82,v82,v230
v_sub_f32 v83,v83,v230
v_sub_f32 v88,v88,v230
v_sub_f32 v89,v89,v230
v_sub_f32 v90,v90,v230
v_sub_f32 v91,v91,v230
v_sub_f32 v96,v96,v230
v_sub_f32 v97,v97,v230
v_sub_f32 v98,v98,v230
v_sub_f32 v99,v99,v230
v_sub_f32 v104,v104,v230
v_sub_f32 v105,v105,v230
v_sub_f32 v106,v106,v230
v_sub_f32 v107,v107,v230
v_sub_f32 v112,v112,v230
v_sub_f32 v113,v113,v230
v_sub_f32 v114,v114,v230
v_sub_f32 v115,v115,v230
v_sub_f32 v120,v120,v230
v_sub_f32 v121,v121,v230
v_sub_f32 v122,v122,v230
v_sub_f32 v123,v123,v230
v_sub_f32 v128,v128,v230
v_sub_f32 v129,v129,v230
v_sub_f32 v130,v130,v230
v_sub_f32 v131,v131,v230
v_mul_f32 v56,v56,v235
v_mul_f32 v57,v57,v235
v_mul_f32 v58,v58,v235
v_mul_f32 v59,v59,v235
AR v240,a32
v_mul_f32 v240,v240,v235
AW a32,v240
AR v241,a33
v_mul_f32 v241,v241,v235
AW a33,v241
AR v242,a34
v_mul_f32 v242,v242,v235
AW a34,v242
AR v243,a35
v_mul_f32 v243,v243,v235
AW a35,v243
AR v240,a36
v_mul_f32 v240,v240,v235
AW a36,v240
AR v241,a37
v_mul_f32 v241,v241,v235
AW a37,v241
AR v242,a38
v_mul_f32 v242,v242,v235
AW a38,v242
AR v243,a39
v_mul_f32 v243,v243,v235
AW a39,v243
AR v240,a40
v_mul_f32 v240,v240,v235
AW a40,v240
AR v241,a41
v_mul_f32 v241,v241,v235
AW a41,v241
AR v242,a42
v_mul_f32 v242,v242,v235
AW a42,v242
AR v243,a43
v_mul_f32 v243,v243,v235
AW a43,v243
AR v240,a44
v_mul_f32 v240,v240,v235
AW a44,v240
AR v241,a45
v_mul_f32 v241,v241,v235
AW a45,v241
AR v242,a46
v_mul_f32 v242,v242,v235
AW a46,v242
AR v243,a47
v_mul_f32 v243,v243,v235
AW a47,v243
AR v240,a48
v_mul_f32 v240,v240,v235
AW a48,v240
AR v241,a49
v_mul_f32 v241,v241,v235
AW a49,v241
AR v242,a50
v_mul_f32 v242,v242,v235
AW a50,v242
AR v243,a51
v_mul_f32 v243,v243,v235
AW a51,v243
AR v240,a52
v_mul_f32 v240,v240,v235
AW a52,v240
AR v241,a53
v_mul_f32 v241,v241,v235
AW a53,v241
AR v242,a54
v_mul_f32 v242,v242,v235
AW a54,v242
AR v243,a55
v_mul_f32 v243,v243,v235
AW a55,v243
AR v240,a56
v_mul_f32 v240,v240,v235
AW a56,v240
AR v241,a57
v_mul_f32 v241,v241,v235
AW a57,v241
AR v242,a58
v_mul_f32 v242,v242,v235
AW a58,v242
AR v243,a59
v_mul_f32 v243,v243,v235
AW a59,v243
AR v240,a60
v_mul_f32 v240,v240,v235
AW a60,v240
AR v241,a61
v_mul_f32 v241,v241,v235
AW a61,v241
AR v242,a62
v_mul_f32 v242,v242,v235
AW a62,v242
AR v243,a63
v_mul_f32 v243,v243,v235
AW a63,v243
E v8,v68
E v9,v69
E v10,v70
E v11,v71
C v132,v8,v9
C v133,v10,v11
E v12,v72
E v13,v73
E v14,v74
E v15,v75
C v136,v12,v13
)"
R"(C v137,v14,v15
E v8,v76
E v9,v77
E v10,v78
E v11,v79
C v134,v8,v9
C v135,v10,v11
E v12,v80
E v13,v81
E v14,v82
E v15,v83
C v138,v12,v13
C v139,v14,v15
E v8,v84
E v9,v85
E v10,v86
E v11,v87
C v140,v8,v9
C v141,v10,v11
E v12,v88
E v13,v89
E v14,v90
E v15,v91
C v144,v12,v13
C v145,v14,v15
E v8,v92
E v9,v93
E v10,v94
E v11,v95
C v142,v8,v9
C v143,v10,v11
E v12,v96
E v13,v97
E v14,v98
E v15,v99
C v146,v12,v13
C v147,v14,v15
M v[36:39],v[16:19],v[132:135],v[52:55]
M v[40:43],v[16:19],v[136:139],v[56:59]
M v[36:39],v[16:19],v[140:143],v[36:39]
M v[40:43],v[16:19],v[144:147],v[40:43]
s_nop 7
s_branch f_rs_C
f_mask_C:
s_nop 7
v_mov_b32 v238,s65
s_cmp_eq_u32 s52,s59
s_cbranch_scc0 f_mask_C_a
v_cndmask_b32 v84,v84,v238,s[44:45]
v_cndmask_b32 v85,v85,v238,s[46:47]
v_cndmask_b32 v86,v86,v238,s[54:55]
v_cndmask_b32 v87,v87,v238,s[56:57]
v_cndmask_b32 v96,v96,v238,s[44:45]
v_cndmask_b32 v97,v97,v238,s[46:47]
v_cndmask_b32 v98,v98,v238,s[54:55]
v_cndmask_b32 v99,v99,v238,s[56:57]
v_mov_b32 v92,s65
v_mov_b32 v93,s65
v_mov_b32 v94,s65
v_mov_b32 v95,s65
s_branch f_rm_C
f_mask_C_a:
v_mov_b32 v84,s65
v_mov_b32 v85,s65
v_mov_b32 v86,s65
v_mov_b32 v87,s65
v_mov_b32 v88,s65
v_mov_b32 v89,s65
v_mov_b32 v90,s65
v_mov_b32 v91,s65
v_mov_b32 v92,s65
v_mov_b32 v93,s65
v_mov_b32 v94,s65
v_mov_b32 v95,s65
v_mov_b32 v96,s65
v_mov_b32 v97,s65
v_mov_b32 v98,s65
v_mov_b32 v99,s65
s_add_u32 s69,s59,8192
s_cmp_eq_u32 s52,s69
s_cbranch_scc0 f_mask_C_f
v_cndmask_b32 v68,v68,v238,s[44:45]
v_cndmask_b32 v69,v69,v238,s[46:47]
v_cndmask_b32 v70,v70,v238,s[54:55]
v_cndmask_b32 v71,v71,v238,s[56:57]
v_cndmask_b32 v80,v80,v238,s[44:45]
v_cndmask_b32 v81,v81,v238,s[46:47]
v_cndmask_b32 v82,v82,v238,s[54:55]
v_cndmask_b32 v83,v83,v238,s[56:57]
v_mov_b32 v76,s65
v_mov_b32 v77,s65
v_mov_b32 v78,s65
v_mov_b32 v79,s65
s_branch f_rm_C
f_mask_C_f:
v_mov_b32 v68,s65
v_mov_b32 v69,s65
v_mov_b32 v70,s65
v_mov_b32 v71,s65
v_mov_b32 v72,s65
v_mov_b32 v73,s65
v_mov_b32 v74,s65
v_mov_b32 v75,s65
v_mov_b32 v76,s65
v_mov_b32 v77,s65
v_mov_b32 v78,s65
v_mov_b32 v79,s65
v_mov_b32 v80,s65
v_mov_b32 v81,s65
v_mov_b32 v82,s65
v_mov_b32 v83,s65
s_branch f_rm_C
f_slow_Do:
s_nop 9
v_max3_f32 v229,v100,v101,v102
v_max3_f32 v229,v229,v103,v108
v_max3_f32 v229,v229,v109,v110
v_max3_f32 v229,v229,v111,v116
v_max3_f32 v229,v229,v117,v118
v_max3_f32 v229,v229,v119,v124
v_max3_f32 v229,v229,v125,v126
v_max3_f32 v229,v229,v127,s62
v_mov_b32 v233,v229
s_nop 1
v_permlane32_swap_b32 v229,v233
v_max_f32 v229,v229,v233
v_mov_b32 v233,v229
s_nop 1
v_permlane16_swap_b32 v229,v233
v_max_f32 v229,v229,v233
v_add_f32 v229,1109393408,v229
v_max3_f32 v230,v104,v105,v106
v_max3_f32 v230,v230,v107,v112
v_max3_f32 v230,v230,v113,v114
v_max3_f32 v230,v230,v115,v120
v_max3_f32 v230,v230,v121,v122
v_max3_f32 v230,v230,v123,v128
v_max3_f32 v230,v230,v129,v130
v_max3_f32 v230,v230,v131,s62
v_mov_b32 v233,v230
s_nop 1
v_permlane32_swap_b32 v230,v233
v_max_f32 v230,v230,v233
v_mov_b32 v233,v230
s_nop 1
v_permlane16_swap_b32 v230,v233
v_max_f32 v230,v230,v233
v_add_f32 v230,1109393408,v230
E v234,-v229
v_sub_f32 v20,v20,v229
v_sub_f32 v21,v21,v229
v_sub_f32 v22,v22,v229
v_sub_f32 v23,v23,v229
v_sub_f32 v100,v100,v229
v_sub_f32 v101,v101,v229
v_sub_f32 v102,v102,v229
v_sub_f32 v103,v103,v229
v_sub_f32 v108,v108,v229
v_sub_f32 v109,v109,v229
v_sub_f32 v110,v110,v229
v_sub_f32 v111,v111,v229
v_sub_f32 v116,v116,v229
v_sub_f32 v117,v117,v229
v_sub_f32 v118,v118,v229
v_sub_f32 v119,v119,v229
v_sub_f32 v124,v124,v229
v_sub_f32 v125,v125,v229
v_sub_f32 v126,v126,v229
v_sub_f32 v127,v127,v229
v_sub_f32 v68,v68,v229
v_sub_f32 v69,v69,v229
v_sub_f32 v70,v70,v229
v_sub_f32 v71,v71,v229
v_sub_f32 v76,v76,v229
v_sub_f32 v77,v77,v229
v_sub_f32 v78,v78,v229
v_sub_f32 v79,v79,v229
v_sub_f32 v84,v84,v229
v_sub_f32 v85,v85,v229
v_sub_f32 v86,v86,v229
v_sub_f32 v87,v87,v229
v_sub_f32 v92,v92,v229
v_sub_f32 v93,v93,v229
v_sub_f32 v94,v94,v229
v_sub_f32 v95,v95,v229
v_mul_f32 v36,v36,v234
v_mul_f32 v37,v37,v234
v_mul_f32 v38,v38,v234
v_mul_f32 v39,v39,v234
AR v240,a0
v_mul_f32 v240,v240,v234
AW a0,v240
AR v241,a1
v_mul_f32 v241,v241,v234
AW a1,v241
AR v242,a2
v_mul_f32 v242,v242,v234
AW a2,v242
AR v243,a3
v_mul_f32 v243,v243,v234
AW a3,v243
AR v240,a4
v_mul_f32 v240,v240,v234
AW a4,v240
AR v241,a5
v_mul_f32 v241,v241,v234
AW a5,v241
AR v242,a6
v_mul_f32 v242,v242,v234
AW a6,v242
AR v243,a7
v_mul_f32 v243,v243,v234
AW a7,v243
AR v240,a8
v_mul_f32 v240,v240,v234
AW a8,v240
AR v241,a9
v_mul_f32 v241,v241,v234
AW a9,v241
AR v242,a10
v_mul_f32 v242,v242,v234
AW a10,v242
AR v243,a11
v_mul_f32 v243,v243,v234
AW a11,v243
AR v240,a12
v_mul_f32 v240,v240,v234
AW a12,v240
AR v241,a13
v_mul_f32 v241,v241,v234
AW a13,v241
AR v242,a14
v_mul_f32 v242,v242,v234
AW a14,v242
AR v243,a15
v_mul_f32 v243,v243,v234
AW a15,v243
AR v240,a16
v_mul_f32 v240,v240,v234
AW a16,v240
AR v241,a17
v_mul_f32 v241,v241,v234
AW a17,v241
AR v242,a18
v_mul_f32 v242,v242,v234
AW a18,v242
AR v243,a19
v_mul_f32 v243,v243,v234
AW a19,v243
AR v240,a20
v_mul_f32 v240,v240,v234
AW a20,v240
AR v241,a21
v_mul_f32 v241,v241,v234
AW a21,v241
AR v242,a22
v_mul_f32 v242,v242,v234
AW a22,v242
AR v243,a23
v_mul_f32 v243,v243,v234
AW a23,v243
AR v240,a24
v_mul_f32 v240,v240,v234
AW a24,v240
AR v241,a25
v_mul_f32 v241,v241,v234
AW a25,v241
AR v242,a26
v_mul_f32 v242,v242,v234
AW a26,v242
AR v243,a27
v_mul_f32 v243,v243,v234
AW a27,v243
AR v240,a28
v_mul_f32 v240,v240,v234
AW a28,v240
AR v241,a29
v_mul_f32 v241,v241,v234
AW a29,v241
AR v242,a30
v_mul_f32 v242,v242,v234
AW a30,v242
AR v243,a31
v_mul_f32 v243,v243,v234
AW a31,v243
E v235,-v230
v_sub_f32 v24,v24,v230
v_sub_f32 v25,v25,v230
v_sub_f32 v26,v26,v230
v_sub_f32 v27,v27,v230
v_sub_f32 v104,v104,v230
v_sub_f32 v105,v105,v230
v_sub_f32 v106,v106,v230
v_sub_f32 v107,v107,v230
v_sub_f32 v112,v112,v230
v_sub_f32 v113,v113,v230
v_sub_f32 v114,v114,v230
v_sub_f32 v115,v115,v230
v_sub_f32 v120,v120,v230
v_sub_f32 v121,v121,v230
v_sub_f32 v122,v122,v230
v_sub_f32 v123,v123,v230
v_sub_f32 v128,v128,v230
v_sub_f32 v129,v129,v230
v_sub_f32 v130,v130,v230
v_sub_f32 v131,v131,v230
v_sub_f32 v72,v72,v230
v_sub_f32 v73,v73,v230
v_sub_f32 v74,v74,v230
v_sub_f32 v75,v75,v230
v_sub_f32 v80,v80,v230
v_sub_f32 v81,v81,v230
v_sub_f32 v82,v82,v230
v_sub_f32 v83,v83,v230
v_sub_f32 v88,v88,v230
v_sub_f32 v89,v89,v230
v_sub_f32 v90,v90,v230
v_sub_f32 v91,v91,v230
v_sub_f32 v96,v96,v230
v_sub_f32 v97,v97,v230
v_sub_f32 v98,v98,v230
v_sub_f32 v99,v99,v230
v_mul_f32 v40,v40,v235
v_mul_f32 v41,v41,v235
v_mul_f32 v42,v42,v235
v_mul_f32 v43,v43,v235
AR v240,a32
v_mul_f32 v240,v240,v235
AW a32,v240
AR v241,a33
v_mul_f32 v241,v241,v235
AW a33,v241
AR v242,a34
v_mul_f32 v242,v242,v235
AW a34,v242
AR v243,a35
v_mul_f32 v243,v243,v235
AW a35,v243
AR v240,a36
v_mul_f32 v240,v240,v235
AW a36,v240
AR v241,a37
v_mul_f32 v241,v241,v235
AW a37,v241
AR v242,a38
v_mul_f32 v242,v242,v235
AW a38,v242
AR v243,a39
v_mul_f32 v243,v243,v235
AW a39,v243
AR v240,a40
v_mul_f32 v240,v240,v235
AW a40,v240
AR v241,a41
v_mul_f32 v241,v241,v235
AW a41,v241
AR v242,a42
v_mul_f32 v242,v242,v235
AW a42,v242
AR v243,a43
v_mul_f32 v243,v243,v235
AW a43,v243
AR v240,a44
v_mul_f32 v240,v240,v235
AW a44,v240
AR v241,a45
v_mul_f32 v241,v241,v235
AW a45,v241
AR v242,a46
v_mul_f32 v242,v242,v235
AW a46,v242
AR v243,a47
v_mul_f32 v243,v243,v235
AW a47,v243
AR v240,a48
v_mul_f32 v240,v240,v235
AW a48,v240
AR v241,a49
v_mul_f32 v241,v241,v235
AW a49,v241
AR v242,a50
v_mul_f32 v242,v242,v235
AW a50,v242
AR v243,a51
v_mul_f32 v243,v243,v235
AW a51,v243
AR v240,a52
v_mul_f32 v240,v240,v235
AW a52,v240
AR v241,a53
v_mul_f32 v241,v241,v235
AW a53,v241
AR v242,a54
v_mul_f32 v242,v242,v235
AW a54,v242
AR v243,a55
v_mul_f32 v243,v243,v235
AW a55,v243
AR v240,a56
v_mul_f32 v240,v240,v235
)"
R"(AW a56,v240
AR v241,a57
v_mul_f32 v241,v241,v235
AW a57,v241
AR v242,a58
v_mul_f32 v242,v242,v235
AW a58,v242
AR v243,a59
v_mul_f32 v243,v243,v235
AW a59,v243
AR v240,a60
v_mul_f32 v240,v240,v235
AW a60,v240
AR v241,a61
v_mul_f32 v241,v241,v235
AW a61,v241
AR v242,a62
v_mul_f32 v242,v242,v235
AW a62,v242
AR v243,a63
v_mul_f32 v243,v243,v235
AW a63,v243
E v8,v100
E v9,v101
E v10,v102
E v11,v103
C v148,v8,v9
C v149,v10,v11
E v12,v104
E v13,v105
E v14,v106
E v15,v107
C v152,v12,v13
C v153,v14,v15
E v8,v108
E v9,v109
E v10,v110
E v11,v111
C v150,v8,v9
C v151,v10,v11
E v12,v112
E v13,v113
E v14,v114
E v15,v115
C v154,v12,v13
C v155,v14,v15
E v8,v116
E v9,v117
E v10,v118
E v11,v119
C v156,v8,v9
C v157,v10,v11
E v12,v120
E v13,v121
E v14,v122
E v15,v123
C v160,v12,v13
C v161,v14,v15
E v8,v124
E v9,v125
E v10,v126
E v11,v127
C v158,v8,v9
C v159,v10,v11
E v12,v128
E v13,v129
E v14,v130
E v15,v131
C v162,v12,v13
C v163,v14,v15
M v[52:55],v[16:19],v[148:151],v[36:39]
M v[56:59],v[16:19],v[152:155],v[40:43]
M v[52:55],v[16:19],v[156:159],v[52:55]
M v[56:59],v[16:19],v[160:163],v[56:59]
s_nop 7
s_branch f_rs_Do
f_slow_De:
s_nop 9
v_max3_f32 v229,v68,v69,v70
v_max3_f32 v229,v229,v71,v76
v_max3_f32 v229,v229,v77,v78
v_max3_f32 v229,v229,v79,v84
v_max3_f32 v229,v229,v85,v86
v_max3_f32 v229,v229,v87,v92
v_max3_f32 v229,v229,v93,v94
v_max3_f32 v229,v229,v95,s62
v_mov_b32 v233,v229
s_nop 1
v_permlane32_swap_b32 v229,v233
v_max_f32 v229,v229,v233
v_mov_b32 v233,v229
s_nop 1
v_permlane16_swap_b32 v229,v233
v_max_f32 v229,v229,v233
v_add_f32 v229,1109393408,v229
v_max3_f32 v230,v72,v73,v74
v_max3_f32 v230,v230,v75,v80
v_max3_f32 v230,v230,v81,v82
v_max3_f32 v230,v230,v83,v88
v_max3_f32 v230,v230,v89,v90
v_max3_f32 v230,v230,v91,v96
v_max3_f32 v230,v230,v97,v98
v_max3_f32 v230,v230,v99,s62
v_mov_b32 v233,v230
s_nop 1
v_permlane32_swap_b32 v230,v233
v_max_f32 v230,v230,v233
v_mov_b32 v233,v230
s_nop 1
v_permlane16_swap_b32 v230,v233
v_max_f32 v230,v230,v233
v_add_f32 v230,1109393408,v230
E v234,-v229
v_sub_f32 v20,v20,v229
v_sub_f32 v21,v21,v229
v_sub_f32 v22,v22,v229
v_sub_f32 v23,v23,v229
v_sub_f32 v68,v68,v229
v_sub_f32 v69,v69,v229
v_sub_f32 v70,v70,v229
v_sub_f32 v71,v71,v229
v_sub_f32 v76,v76,v229
v_sub_f32 v77,v77,v229
v_sub_f32 v78,v78,v229
v_sub_f32 v79,v79,v229
v_sub_f32 v84,v84,v229
v_sub_f32 v85,v85,v229
v_sub_f32 v86,v86,v229
v_sub_f32 v87,v87,v229
v_sub_f32 v92,v92,v229
v_sub_f32 v93,v93,v229
v_sub_f32 v94,v94,v229
v_sub_f32 v95,v95,v229
v_sub_f32 v100,v100,v229
v_sub_f32 v101,v101,v229
v_sub_f32 v102,v102,v229
v_sub_f32 v103,v103,v229
v_sub_f32 v108,v108,v229
v_sub_f32 v109,v109,v229
v_sub_f32 v110,v110,v229
v_sub_f32 v111,v111,v229
v_sub_f32 v116,v116,v229
v_sub_f32 v117,v117,v229
v_sub_f32 v118,v118,v229
v_sub_f32 v119,v119,v229
v_sub_f32 v124,v124,v229
v_sub_f32 v125,v125,v229
v_sub_f32 v126,v126,v229
v_sub_f32 v127,v127,v229
v_mul_f32 v52,v52,v234
v_mul_f32 v53,v53,v234
v_mul_f32 v54,v54,v234
v_mul_f32 v55,v55,v234
AR v240,a0
v_mul_f32 v240,v240,v234
AW a0,v240
AR v241,a1
v_mul_f32 v241,v241,v234
AW a1,v241
AR v242,a2
v_mul_f32 v242,v242,v234
AW a2,v242
AR v243,a3
v_mul_f32 v243,v243,v234
AW a3,v243
AR v240,a4
v_mul_f32 v240,v240,v234
AW a4,v240
AR v241,a5
v_mul_f32 v241,v241,v234
AW a5,v241
AR v242,a6
v_mul_f32 v242,v242,v234
AW a6,v242
AR v243,a7
v_mul_f32 v243,v243,v234
AW a7,v243
AR v240,a8
v_mul_f32 v240,v240,v234
AW a8,v240
AR v241,a9
v_mul_f32 v241,v241,v234
AW a9,v241
AR v242,a10
v_mul_f32 v242,v242,v234
AW a10,v242
AR v243,a11
v_mul_f32 v243,v243,v234
AW a11,v243
AR v240,a12
v_mul_f32 v240,v240,v234
AW a12,v240
AR v241,a13
v_mul_f32 v241,v241,v234
AW a13,v241
AR v242,a14
v_mul_f32 v242,v242,v234
AW a14,v242
AR v243,a15
v_mul_f32 v243,v243,v234
AW a15,v243
AR v240,a16
v_mul_f32 v240,v240,v234
AW a16,v240
AR v241,a17
v_mul_f32 v241,v241,v234
AW a17,v241
AR v242,a18
v_mul_f32 v242,v242,v234
AW a18,v242
AR v243,a19
v_mul_f32 v243,v243,v234
AW a19,v243
AR v240,a20
v_mul_f32 v240,v240,v234
AW a20,v240
AR v241,a21
v_mul_f32 v241,v241,v234
AW a21,v241
AR v242,a22
v_mul_f32 v242,v242,v234
AW a22,v242
AR v243,a23
v_mul_f32 v243,v243,v234
AW a23,v243
AR v240,a24
v_mul_f32 v240,v240,v234
AW a24,v240
AR v241,a25
v_mul_f32 v241,v241,v234
AW a25,v241
AR v242,a26
v_mul_f32 v242,v242,v234
AW a26,v242
AR v243,a27
v_mul_f32 v243,v243,v234
AW a27,v243
AR v240,a28
v_mul_f32 v240,v240,v234
AW a28,v240
AR v241,a29
v_mul_f32 v241,v241,v234
AW a29,v241
AR v242,a30
v_mul_f32 v242,v242,v234
AW a30,v242
AR v243,a31
v_mul_f32 v243,v243,v234
AW a31,v243
E v235,-v230
v_sub_f32 v24,v24,v230
v_sub_f32 v25,v25,v230
v_sub_f32 v26,v26,v230
v_sub_f32 v27,v27,v230
v_sub_f32 v72,v72,v230
v_sub_f32 v73,v73,v230
v_sub_f32 v74,v74,v230
v_sub_f32 v75,v75,v230
v_sub_f32 v80,v80,v230
v_sub_f32 v81,v81,v230
v_sub_f32 v82,v82,v230
v_sub_f32 v83,v83,v230
v_sub_f32 v88,v88,v230
v_sub_f32 v89,v89,v230
v_sub_f32 v90,v90,v230
v_sub_f32 v91,v91,v230
v_sub_f32 v96,v96,v230
v_sub_f32 v97,v97,v230
v_sub_f32 v98,v98,v230
v_sub_f32 v99,v99,v230
v_sub_f32 v104,v104,v230
v_sub_f32 v105,v105,v230
v_sub_f32 v106,v106,v230
v_sub_f32 v107,v107,v230
v_sub_f32 v112,v112,v230
v_sub_f32 v113,v113,v230
v_sub_f32 v114,v114,v230
v_sub_f32 v115,v115,v230
v_sub_f32 v120,v120,v230
v_sub_f32 v121,v121,v230
v_sub_f32 v122,v122,v230
v_sub_f32 v123,v123,v230
v_sub_f32 v128,v128,v230
v_sub_f32 v129,v129,v230
v_sub_f32 v130,v130,v230
v_sub_f32 v131,v131,v230
v_mul_f32 v56,v56,v235
v_mul_f32 v57,v57,v235
v_mul_f32 v58,v58,v235
v_mul_f32 v59,v59,v235
AR v240,a32
v_mul_f32 v240,v240,v235
AW a32,v240
AR v241,a33
v_mul_f32 v241,v241,v235
AW a33,v241
AR v242,a34
v_mul_f32 v242,v242,v235
AW a34,v242
AR v243,a35
v_mul_f32 v243,v243,v235
AW a35,v243
AR v240,a36
v_mul_f32 v240,v240,v235
AW a36,v240
AR v241,a37
v_mul_f32 v241,v241,v235
AW a37,v241
AR v242,a38
v_mul_f32 v242,v242,v235
AW a38,v242
AR v243,a39
v_mul_f32 v243,v243,v235
AW a39,v243
AR v240,a40
v_mul_f32 v240,v240,v235
AW a40,v240
AR v241,a41
v_mul_f32 v241,v241,v235
AW a41,v241
AR v242,a42
v_mul_f32 v242,v242,v235
AW a42,v242
AR v243,a43
v_mul_f32 v243,v243,v235
AW a43,v243
AR v240,a44
v_mul_f32 v240,v240,v235
AW a44,v240
AR v241,a45
v_mul_f32 v241,v241,v235
AW a45,v241
AR v242,a46
v_mul_f32 v242,v242,v235
AW a46,v242
AR v243,a47
v_mul_f32 v243,v243,v235
AW a47,v243
AR v240,a48
v_mul_f32 v240,v240,v235
AW a48,v240
AR v241,a49
v_mul_f32 v241,v241,v235
AW a49,v241
AR v242,a50
v_mul_f32 v242,v242,v235
AW a50,v242
AR v243,a51
v_mul_f32 v243,v243,v235
AW a51,v243
AR v240,a52
v_mul_f32 v240,v240,v235
AW a52,v240
AR v241,a53
v_mul_f32 v241,v241,v235
AW a53,v241
AR v242,a54
v_mul_f32 v242,v242,v235
AW a54,v242
AR v243,a55
v_mul_f32 v243,v243,v235
AW a55,v243
AR v240,a56
v_mul_f32 v240,v240,v235
AW a56,v240
AR v241,a57
v_mul_f32 v241,v241,v235
AW a57,v241
AR v242,a58
v_mul_f32 v242,v242,v235
AW a58,v242
AR v243,a59
v_mul_f32 v243,v243,v235
AW a59,v243
AR v240,a60
v_mul_f32 v240,v240,v235
AW a60,v240
AR v241,a61
v_mul_f32 v241,v241,v235
AW a61,v241
AR v242,a62
v_mul_f32 v242,v242,v235
AW a62,v242
AR v243,a63
v_mul_f32 v243,v243,v235
AW a63,v243
E v8,v68
E v9,v69
E v10,v70
E v11,v71
C v132,v8,v9
C v133,v10,v11
E v12,v72
E v13,v73
E v14,v74
E v15,v75
C v136,v12,v13
C v137,v14,v15
E v8,v76
E v9,v77
)"
R"(E v10,v78
E v11,v79
C v134,v8,v9
C v135,v10,v11
E v12,v80
E v13,v81
E v14,v82
E v15,v83
C v138,v12,v13
C v139,v14,v15
E v8,v84
E v9,v85
E v10,v86
E v11,v87
C v140,v8,v9
C v141,v10,v11
E v12,v88
E v13,v89
E v14,v90
E v15,v91
C v144,v12,v13
C v145,v14,v15
E v8,v92
E v9,v93
E v10,v94
E v11,v95
C v142,v8,v9
C v143,v10,v11
E v12,v96
E v13,v97
E v14,v98
E v15,v99
C v146,v12,v13
C v147,v14,v15
M v[36:39],v[16:19],v[132:135],v[52:55]
M v[40:43],v[16:19],v[136:139],v[56:59]
M v[36:39],v[16:19],v[140:143],v[36:39]
M v[40:43],v[16:19],v[144:147],v[40:43]
s_nop 7
s_branch f_rs_De
f_slow_Xo:
s_nop 9
v_max3_f32 v229,v100,v101,v102
v_max3_f32 v229,v229,v103,v108
v_max3_f32 v229,v229,v109,v110
v_max3_f32 v229,v229,v111,v116
v_max3_f32 v229,v229,v117,v118
v_max3_f32 v229,v229,v119,v124
v_max3_f32 v229,v229,v125,v126
v_max3_f32 v229,v229,v127,s62
v_mov_b32 v233,v229
s_nop 1
v_permlane32_swap_b32 v229,v233
v_max_f32 v229,v229,v233
v_mov_b32 v233,v229
s_nop 1
v_permlane16_swap_b32 v229,v233
v_max_f32 v229,v229,v233
v_add_f32 v229,1109393408,v229
v_max3_f32 v230,v104,v105,v106
v_max3_f32 v230,v230,v107,v112
v_max3_f32 v230,v230,v113,v114
v_max3_f32 v230,v230,v115,v120
v_max3_f32 v230,v230,v121,v122
v_max3_f32 v230,v230,v123,v128
v_max3_f32 v230,v230,v129,v130
v_max3_f32 v230,v230,v131,s62
v_mov_b32 v233,v230
s_nop 1
v_permlane32_swap_b32 v230,v233
v_max_f32 v230,v230,v233
v_mov_b32 v233,v230
s_nop 1
v_permlane16_swap_b32 v230,v233
v_max_f32 v230,v230,v233
v_add_f32 v230,1109393408,v230
E v234,-v229
v_sub_f32 v20,v20,v229
v_sub_f32 v21,v21,v229
v_sub_f32 v22,v22,v229
v_sub_f32 v23,v23,v229
v_sub_f32 v100,v100,v229
v_sub_f32 v101,v101,v229
v_sub_f32 v102,v102,v229
v_sub_f32 v103,v103,v229
v_sub_f32 v108,v108,v229
v_sub_f32 v109,v109,v229
v_sub_f32 v110,v110,v229
v_sub_f32 v111,v111,v229
v_sub_f32 v116,v116,v229
v_sub_f32 v117,v117,v229
v_sub_f32 v118,v118,v229
v_sub_f32 v119,v119,v229
v_sub_f32 v124,v124,v229
v_sub_f32 v125,v125,v229
v_sub_f32 v126,v126,v229
v_sub_f32 v127,v127,v229
v_sub_f32 v68,v68,v229
v_sub_f32 v69,v69,v229
v_sub_f32 v70,v70,v229
v_sub_f32 v71,v71,v229
v_sub_f32 v76,v76,v229
v_sub_f32 v77,v77,v229
v_sub_f32 v78,v78,v229
v_sub_f32 v79,v79,v229
v_sub_f32 v84,v84,v229
v_sub_f32 v85,v85,v229
v_sub_f32 v86,v86,v229
v_sub_f32 v87,v87,v229
v_sub_f32 v92,v92,v229
v_sub_f32 v93,v93,v229
v_sub_f32 v94,v94,v229
v_sub_f32 v95,v95,v229
v_mul_f32 v36,v36,v234
v_mul_f32 v37,v37,v234
v_mul_f32 v38,v38,v234
v_mul_f32 v39,v39,v234
AR v240,a0
v_mul_f32 v240,v240,v234
AW a0,v240
AR v241,a1
v_mul_f32 v241,v241,v234
AW a1,v241
AR v242,a2
v_mul_f32 v242,v242,v234
AW a2,v242
AR v243,a3
v_mul_f32 v243,v243,v234
AW a3,v243
AR v240,a4
v_mul_f32 v240,v240,v234
AW a4,v240
AR v241,a5
v_mul_f32 v241,v241,v234
AW a5,v241
AR v242,a6
v_mul_f32 v242,v242,v234
AW a6,v242
AR v243,a7
v_mul_f32 v243,v243,v234
AW a7,v243
AR v240,a8
v_mul_f32 v240,v240,v234
AW a8,v240
AR v241,a9
v_mul_f32 v241,v241,v234
AW a9,v241
AR v242,a10
v_mul_f32 v242,v242,v234
AW a10,v242
AR v243,a11
v_mul_f32 v243,v243,v234
AW a11,v243
AR v240,a12
v_mul_f32 v240,v240,v234
AW a12,v240
AR v241,a13
v_mul_f32 v241,v241,v234
AW a13,v241
AR v242,a14
v_mul_f32 v242,v242,v234
AW a14,v242
AR v243,a15
v_mul_f32 v243,v243,v234
AW a15,v243
AR v240,a16
v_mul_f32 v240,v240,v234
AW a16,v240
AR v241,a17
v_mul_f32 v241,v241,v234
AW a17,v241
AR v242,a18
v_mul_f32 v242,v242,v234
AW a18,v242
AR v243,a19
v_mul_f32 v243,v243,v234
AW a19,v243
AR v240,a20
v_mul_f32 v240,v240,v234
AW a20,v240
AR v241,a21
v_mul_f32 v241,v241,v234
AW a21,v241
AR v242,a22
v_mul_f32 v242,v242,v234
AW a22,v242
AR v243,a23
v_mul_f32 v243,v243,v234
AW a23,v243
AR v240,a24
v_mul_f32 v240,v240,v234
AW a24,v240
AR v241,a25
v_mul_f32 v241,v241,v234
AW a25,v241
AR v242,a26
v_mul_f32 v242,v242,v234
AW a26,v242
AR v243,a27
v_mul_f32 v243,v243,v234
AW a27,v243
AR v240,a28
v_mul_f32 v240,v240,v234
AW a28,v240
AR v241,a29
v_mul_f32 v241,v241,v234
AW a29,v241
AR v242,a30
v_mul_f32 v242,v242,v234
AW a30,v242
AR v243,a31
v_mul_f32 v243,v243,v234
AW a31,v243
E v235,-v230
v_sub_f32 v24,v24,v230
v_sub_f32 v25,v25,v230
v_sub_f32 v26,v26,v230
v_sub_f32 v27,v27,v230
v_sub_f32 v104,v104,v230
v_sub_f32 v105,v105,v230
v_sub_f32 v106,v106,v230
v_sub_f32 v107,v107,v230
v_sub_f32 v112,v112,v230
v_sub_f32 v113,v113,v230
v_sub_f32 v114,v114,v230
v_sub_f32 v115,v115,v230
v_sub_f32 v120,v120,v230
v_sub_f32 v121,v121,v230
v_sub_f32 v122,v122,v230
v_sub_f32 v123,v123,v230
v_sub_f32 v128,v128,v230
v_sub_f32 v129,v129,v230
v_sub_f32 v130,v130,v230
v_sub_f32 v131,v131,v230
v_sub_f32 v72,v72,v230
v_sub_f32 v73,v73,v230
v_sub_f32 v74,v74,v230
v_sub_f32 v75,v75,v230
v_sub_f32 v80,v80,v230
v_sub_f32 v81,v81,v230
v_sub_f32 v82,v82,v230
v_sub_f32 v83,v83,v230
v_sub_f32 v88,v88,v230
v_sub_f32 v89,v89,v230
v_sub_f32 v90,v90,v230
v_sub_f32 v91,v91,v230
v_sub_f32 v96,v96,v230
v_sub_f32 v97,v97,v230
v_sub_f32 v98,v98,v230
v_sub_f32 v99,v99,v230
v_mul_f32 v40,v40,v235
v_mul_f32 v41,v41,v235
v_mul_f32 v42,v42,v235
v_mul_f32 v43,v43,v235
AR v240,a32
v_mul_f32 v240,v240,v235
AW a32,v240
AR v241,a33
v_mul_f32 v241,v241,v235
AW a33,v241
AR v242,a34
v_mul_f32 v242,v242,v235
AW a34,v242
AR v243,a35
v_mul_f32 v243,v243,v235
AW a35,v243
AR v240,a36
v_mul_f32 v240,v240,v235
AW a36,v240
AR v241,a37
v_mul_f32 v241,v241,v235
AW a37,v241
AR v242,a38
v_mul_f32 v242,v242,v235
AW a38,v242
AR v243,a39
v_mul_f32 v243,v243,v235
AW a39,v243
AR v240,a40
v_mul_f32 v240,v240,v235
AW a40,v240
AR v241,a41
v_mul_f32 v241,v241,v235
AW a41,v241
AR v242,a42
v_mul_f32 v242,v242,v235
AW a42,v242
AR v243,a43
v_mul_f32 v243,v243,v235
AW a43,v243
AR v240,a44
v_mul_f32 v240,v240,v235
AW a44,v240
AR v241,a45
v_mul_f32 v241,v241,v235
AW a45,v241
AR v242,a46
v_mul_f32 v242,v242,v235
AW a46,v242
AR v243,a47
v_mul_f32 v243,v243,v235
AW a47,v243
AR v240,a48
v_mul_f32 v240,v240,v235
AW a48,v240
AR v241,a49
v_mul_f32 v241,v241,v235
AW a49,v241
AR v242,a50
v_mul_f32 v242,v242,v235
AW a50,v242
AR v243,a51
v_mul_f32 v243,v243,v235
AW a51,v243
AR v240,a52
v_mul_f32 v240,v240,v235
AW a52,v240
AR v241,a53
v_mul_f32 v241,v241,v235
AW a53,v241
AR v242,a54
v_mul_f32 v242,v242,v235
AW a54,v242
AR v243,a55
v_mul_f32 v243,v243,v235
AW a55,v243
AR v240,a56
v_mul_f32 v240,v240,v235
AW a56,v240
AR v241,a57
v_mul_f32 v241,v241,v235
AW a57,v241
AR v242,a58
v_mul_f32 v242,v242,v235
AW a58,v242
AR v243,a59
v_mul_f32 v243,v243,v235
AW a59,v243
AR v240,a60
v_mul_f32 v240,v240,v235
AW a60,v240
AR v241,a61
v_mul_f32 v241,v241,v235
AW a61,v241
AR v242,a62
v_mul_f32 v242,v242,v235
AW a62,v242
AR v243,a63
v_mul_f32 v243,v243,v235
AW a63,v243
E v8,v100
E v9,v101
E v10,v102
E v11,v103
C v148,v8,v9
C v149,v10,v11
E v12,v104
E v13,v105
E v14,v106
E v15,v107
C v152,v12,v13
C v153,v14,v15
E v8,v108
E v9,v109
E v10,v110
E v11,v111
C v150,v8,v9
C v151,v10,v11
E v12,v112
E v13,v113
E v14,v114
E v15,v115
C v154,v12,v13
C v155,v14,v15
E v8,v116
E v9,v117
E v10,v118
E v11,v119
C v156,v8,v9
C v157,v10,v11
E v12,v120
E v13,v121
E v14,v122
E v15,v123
C v160,v12,v13
C v161,v14,v15
E v8,v124
E v9,v125
E v10,v126
E v11,v127
C v158,v8,v9
C v159,v10,v11
E v12,v128
E v13,v129
E v14,v130
E v15,v131
C v162,v12,v13
C v163,v14,v15
M v[52:55],v[16:19],v[148:151],v[36:39]
M v[56:59],v[16:19],v[152:155],v[40:43]
)"
R"(M v[52:55],v[16:19],v[156:159],v[52:55]
M v[56:59],v[16:19],v[160:163],v[56:59]
s_nop 7
s_branch f_rs_Xo
f_slow_Xe:
s_nop 9
v_max3_f32 v229,v68,v69,v70
v_max3_f32 v229,v229,v71,v76
v_max3_f32 v229,v229,v77,v78
v_max3_f32 v229,v229,v79,v84
v_max3_f32 v229,v229,v85,v86
v_max3_f32 v229,v229,v87,v92
v_max3_f32 v229,v229,v93,v94
v_max3_f32 v229,v229,v95,s62
v_mov_b32 v233,v229
s_nop 1
v_permlane32_swap_b32 v229,v233
v_max_f32 v229,v229,v233
v_mov_b32 v233,v229
s_nop 1
v_permlane16_swap_b32 v229,v233
v_max_f32 v229,v229,v233
v_add_f32 v229,1109393408,v229
v_max3_f32 v230,v72,v73,v74
v_max3_f32 v230,v230,v75,v80
v_max3_f32 v230,v230,v81,v82
v_max3_f32 v230,v230,v83,v88
v_max3_f32 v230,v230,v89,v90
v_max3_f32 v230,v230,v91,v96
v_max3_f32 v230,v230,v97,v98
v_max3_f32 v230,v230,v99,s62
v_mov_b32 v233,v230
s_nop 1
v_permlane32_swap_b32 v230,v233
v_max_f32 v230,v230,v233
v_mov_b32 v233,v230
s_nop 1
v_permlane16_swap_b32 v230,v233
v_max_f32 v230,v230,v233
v_add_f32 v230,1109393408,v230
E v234,-v229
v_sub_f32 v20,v20,v229
v_sub_f32 v21,v21,v229
v_sub_f32 v22,v22,v229
v_sub_f32 v23,v23,v229
v_sub_f32 v68,v68,v229
v_sub_f32 v69,v69,v229
v_sub_f32 v70,v70,v229
v_sub_f32 v71,v71,v229
v_sub_f32 v76,v76,v229
v_sub_f32 v77,v77,v229
v_sub_f32 v78,v78,v229
v_sub_f32 v79,v79,v229
v_sub_f32 v84,v84,v229
v_sub_f32 v85,v85,v229
v_sub_f32 v86,v86,v229
v_sub_f32 v87,v87,v229
v_sub_f32 v92,v92,v229
v_sub_f32 v93,v93,v229
v_sub_f32 v94,v94,v229
v_sub_f32 v95,v95,v229
v_sub_f32 v100,v100,v229
v_sub_f32 v101,v101,v229
v_sub_f32 v102,v102,v229
v_sub_f32 v103,v103,v229
v_sub_f32 v108,v108,v229
v_sub_f32 v109,v109,v229
v_sub_f32 v110,v110,v229
v_sub_f32 v111,v111,v229
v_sub_f32 v116,v116,v229
v_sub_f32 v117,v117,v229
v_sub_f32 v118,v118,v229
v_sub_f32 v119,v119,v229
v_sub_f32 v124,v124,v229
v_sub_f32 v125,v125,v229
v_sub_f32 v126,v126,v229
v_sub_f32 v127,v127,v229
v_mul_f32 v52,v52,v234
v_mul_f32 v53,v53,v234
v_mul_f32 v54,v54,v234
v_mul_f32 v55,v55,v234
AR v240,a0
v_mul_f32 v240,v240,v234
AW a0,v240
AR v241,a1
v_mul_f32 v241,v241,v234
AW a1,v241
AR v242,a2
v_mul_f32 v242,v242,v234
AW a2,v242
AR v243,a3
v_mul_f32 v243,v243,v234
AW a3,v243
AR v240,a4
v_mul_f32 v240,v240,v234
AW a4,v240
AR v241,a5
v_mul_f32 v241,v241,v234
AW a5,v241
AR v242,a6
v_mul_f32 v242,v242,v234
AW a6,v242
AR v243,a7
v_mul_f32 v243,v243,v234
AW a7,v243
AR v240,a8
v_mul_f32 v240,v240,v234
AW a8,v240
AR v241,a9
v_mul_f32 v241,v241,v234
AW a9,v241
AR v242,a10
v_mul_f32 v242,v242,v234
AW a10,v242
AR v243,a11
v_mul_f32 v243,v243,v234
AW a11,v243
AR v240,a12
v_mul_f32 v240,v240,v234
AW a12,v240
AR v241,a13
v_mul_f32 v241,v241,v234
AW a13,v241
AR v242,a14
v_mul_f32 v242,v242,v234
AW a14,v242
AR v243,a15
v_mul_f32 v243,v243,v234
AW a15,v243
AR v240,a16
v_mul_f32 v240,v240,v234
AW a16,v240
AR v241,a17
v_mul_f32 v241,v241,v234
AW a17,v241
AR v242,a18
v_mul_f32 v242,v242,v234
AW a18,v242
AR v243,a19
v_mul_f32 v243,v243,v234
AW a19,v243
AR v240,a20
v_mul_f32 v240,v240,v234
AW a20,v240
AR v241,a21
v_mul_f32 v241,v241,v234
AW a21,v241
AR v242,a22
v_mul_f32 v242,v242,v234
AW a22,v242
AR v243,a23
v_mul_f32 v243,v243,v234
AW a23,v243
AR v240,a24
v_mul_f32 v240,v240,v234
AW a24,v240
AR v241,a25
v_mul_f32 v241,v241,v234
AW a25,v241
AR v242,a26
v_mul_f32 v242,v242,v234
AW a26,v242
AR v243,a27
v_mul_f32 v243,v243,v234
AW a27,v243
AR v240,a28
v_mul_f32 v240,v240,v234
AW a28,v240
AR v241,a29
v_mul_f32 v241,v241,v234
AW a29,v241
AR v242,a30
v_mul_f32 v242,v242,v234
AW a30,v242
AR v243,a31
v_mul_f32 v243,v243,v234
AW a31,v243
E v235,-v230
v_sub_f32 v24,v24,v230
v_sub_f32 v25,v25,v230
v_sub_f32 v26,v26,v230
v_sub_f32 v27,v27,v230
v_sub_f32 v72,v72,v230
v_sub_f32 v73,v73,v230
v_sub_f32 v74,v74,v230
v_sub_f32 v75,v75,v230
v_sub_f32 v80,v80,v230
v_sub_f32 v81,v81,v230
v_sub_f32 v82,v82,v230
v_sub_f32 v83,v83,v230
v_sub_f32 v88,v88,v230
v_sub_f32 v89,v89,v230
v_sub_f32 v90,v90,v230
v_sub_f32 v91,v91,v230
v_sub_f32 v96,v96,v230
v_sub_f32 v97,v97,v230
v_sub_f32 v98,v98,v230
v_sub_f32 v99,v99,v230
v_sub_f32 v104,v104,v230
v_sub_f32 v105,v105,v230
v_sub_f32 v106,v106,v230
v_sub_f32 v107,v107,v230
v_sub_f32 v112,v112,v230
v_sub_f32 v113,v113,v230
v_sub_f32 v114,v114,v230
v_sub_f32 v115,v115,v230
v_sub_f32 v120,v120,v230
v_sub_f32 v121,v121,v230
v_sub_f32 v122,v122,v230
v_sub_f32 v123,v123,v230
v_sub_f32 v128,v128,v230
v_sub_f32 v129,v129,v230
v_sub_f32 v130,v130,v230
v_sub_f32 v131,v131,v230
v_mul_f32 v56,v56,v235
v_mul_f32 v57,v57,v235
v_mul_f32 v58,v58,v235
v_mul_f32 v59,v59,v235
AR v240,a32
v_mul_f32 v240,v240,v235
AW a32,v240
AR v241,a33
v_mul_f32 v241,v241,v235
AW a33,v241
AR v242,a34
v_mul_f32 v242,v242,v235
AW a34,v242
AR v243,a35
v_mul_f32 v243,v243,v235
AW a35,v243
AR v240,a36
v_mul_f32 v240,v240,v235
AW a36,v240
AR v241,a37
v_mul_f32 v241,v241,v235
AW a37,v241
AR v242,a38
v_mul_f32 v242,v242,v235
AW a38,v242
AR v243,a39
v_mul_f32 v243,v243,v235
AW a39,v243
AR v240,a40
v_mul_f32 v240,v240,v235
AW a40,v240
AR v241,a41
v_mul_f32 v241,v241,v235
AW a41,v241
AR v242,a42
v_mul_f32 v242,v242,v235
AW a42,v242
AR v243,a43
v_mul_f32 v243,v243,v235
AW a43,v243
AR v240,a44
v_mul_f32 v240,v240,v235
AW a44,v240
AR v241,a45
v_mul_f32 v241,v241,v235
AW a45,v241
AR v242,a46
v_mul_f32 v242,v242,v235
AW a46,v242
AR v243,a47
v_mul_f32 v243,v243,v235
AW a47,v243
AR v240,a48
v_mul_f32 v240,v240,v235
AW a48,v240
AR v241,a49
v_mul_f32 v241,v241,v235
AW a49,v241
AR v242,a50
v_mul_f32 v242,v242,v235
AW a50,v242
AR v243,a51
v_mul_f32 v243,v243,v235
AW a51,v243
AR v240,a52
v_mul_f32 v240,v240,v235
AW a52,v240
AR v241,a53
v_mul_f32 v241,v241,v235
AW a53,v241
AR v242,a54
v_mul_f32 v242,v242,v235
AW a54,v242
AR v243,a55
v_mul_f32 v243,v243,v235
AW a55,v243
AR v240,a56
v_mul_f32 v240,v240,v235
AW a56,v240
AR v241,a57
v_mul_f32 v241,v241,v235
AW a57,v241
AR v242,a58
v_mul_f32 v242,v242,v235
AW a58,v242
AR v243,a59
v_mul_f32 v243,v243,v235
AW a59,v243
AR v240,a60
v_mul_f32 v240,v240,v235
AW a60,v240
AR v241,a61
v_mul_f32 v241,v241,v235
AW a61,v241
AR v242,a62
v_mul_f32 v242,v242,v235
AW a62,v242
AR v243,a63
v_mul_f32 v243,v243,v235
AW a63,v243
E v8,v68
E v9,v69
E v10,v70
E v11,v71
C v132,v8,v9
C v133,v10,v11
E v12,v72
E v13,v73
E v14,v74
E v15,v75
C v136,v12,v13
C v137,v14,v15
E v8,v76
E v9,v77
E v10,v78
E v11,v79
C v134,v8,v9
C v135,v10,v11
E v12,v80
E v13,v81
E v14,v82
E v15,v83
C v138,v12,v13
C v139,v14,v15
E v8,v84
E v9,v85
E v10,v86
E v11,v87
C v140,v8,v9
C v141,v10,v11
E v12,v88
E v13,v89
E v14,v90
E v15,v91
C v144,v12,v13
C v145,v14,v15
E v8,v92
E v9,v93
E v10,v94
E v11,v95
C v142,v8,v9
C v143,v10,v11
E v12,v96
E v13,v97
E v14,v98
E v15,v99
C v146,v12,v13
C v147,v14,v15
M v[36:39],v[16:19],v[132:135],v[52:55]
M v[40:43],v[16:19],v[136:139],v[56:59]
M v[36:39],v[16:19],v[140:143],v[36:39]
M v[40:43],v[16:19],v[144:147],v[40:43]
s_nop 7
s_branch f_rs_Xe
f_mask_Xe:
s_nop 7
v_mov_b32 v238,s65
s_cmp_eq_u32 s52,s59
s_cbranch_scc0 f_mask_Xe_a
v_cndmask_b32 v84,v84,v238,s[44:45]
v_cndmask_b32 v85,v85,v238,s[46:47]
v_cndmask_b32 v86,v86,v238,s[54:55]
v_cndmask_b32 v87,v87,v238,s[56:57]
v_cndmask_b32 v96,v96,v238,s[44:45]
v_cndmask_b32 v97,v97,v238,s[46:47]
v_cndmask_b32 v98,v98,v238,s[54:55]
v_cndmask_b32 v99,v99,v238,s[56:57]
v_mov_b32 v92,s65
v_mov_b32 v93,s65
v_mov_b32 v94,s65
v_mov_b32 v95,s65
s_branch f_rm_Xe
f_mask_Xe_a:
v_mov_b32 v84,s65
v_mov_b32 v85,s65
v_mov_b32 v86,s65
v_mov_b32 v87,s65
v_mov_b32 v88,s65
v_mov_b32 v89,s65
v_mov_b32 v90,s65
v_mov_b32 v91,s65
v_mov_b32 v92,s65
v_mov_b32 v93,s65
v_mov_b32 v94,s65
v_mov_b32 v95,s65
v_mov_b32 v96,s65
)"
R"(v_mov_b32 v97,s65
v_mov_b32 v98,s65
v_mov_b32 v99,s65
s_add_u32 s69,s59,8192
s_cmp_eq_u32 s52,s69
s_cbranch_scc0 f_mask_Xe_f
v_cndmask_b32 v68,v68,v238,s[44:45]
v_cndmask_b32 v69,v69,v238,s[46:47]
v_cndmask_b32 v70,v70,v238,s[54:55]
v_cndmask_b32 v71,v71,v238,s[56:57]
v_cndmask_b32 v80,v80,v238,s[44:45]
v_cndmask_b32 v81,v81,v238,s[46:47]
v_cndmask_b32 v82,v82,v238,s[54:55]
v_cndmask_b32 v83,v83,v238,s[56:57]
v_mov_b32 v76,s65
v_mov_b32 v77,s65
v_mov_b32 v78,s65
v_mov_b32 v79,s65
s_branch f_rm_Xe
f_mask_Xe_f:
v_mov_b32 v68,s65
v_mov_b32 v69,s65
v_mov_b32 v70,s65
v_mov_b32 v71,s65
v_mov_b32 v72,s65
v_mov_b32 v73,s65
v_mov_b32 v74,s65
v_mov_b32 v75,s65
v_mov_b32 v76,s65
v_mov_b32 v77,s65
v_mov_b32 v78,s65
v_mov_b32 v79,s65
v_mov_b32 v80,s65
v_mov_b32 v81,s65
v_mov_b32 v82,s65
v_mov_b32 v83,s65
s_branch f_rm_Xe
f_slow_E:
s_nop 9
v_max3_f32 v229,v100,v101,v102
v_max3_f32 v229,v229,v103,v108
v_max3_f32 v229,v229,v109,v110
v_max3_f32 v229,v229,v111,v116
v_max3_f32 v229,v229,v117,v118
v_max3_f32 v229,v229,v119,v124
v_max3_f32 v229,v229,v125,v126
v_max3_f32 v229,v229,v127,s62
v_mov_b32 v233,v229
s_nop 1
v_permlane32_swap_b32 v229,v233
v_max_f32 v229,v229,v233
v_mov_b32 v233,v229
s_nop 1
v_permlane16_swap_b32 v229,v233
v_max_f32 v229,v229,v233
v_add_f32 v229,1109393408,v229
v_max3_f32 v230,v104,v105,v106
v_max3_f32 v230,v230,v107,v112
v_max3_f32 v230,v230,v113,v114
v_max3_f32 v230,v230,v115,v120
v_max3_f32 v230,v230,v121,v122
v_max3_f32 v230,v230,v123,v128
v_max3_f32 v230,v230,v129,v130
v_max3_f32 v230,v230,v131,s62
v_mov_b32 v233,v230
s_nop 1
v_permlane32_swap_b32 v230,v233
v_max_f32 v230,v230,v233
v_mov_b32 v233,v230
s_nop 1
v_permlane16_swap_b32 v230,v233
v_max_f32 v230,v230,v233
v_add_f32 v230,1109393408,v230
E v234,-v229
v_sub_f32 v20,v20,v229
v_sub_f32 v21,v21,v229
v_sub_f32 v22,v22,v229
v_sub_f32 v23,v23,v229
v_sub_f32 v100,v100,v229
v_sub_f32 v101,v101,v229
v_sub_f32 v102,v102,v229
v_sub_f32 v103,v103,v229
v_sub_f32 v108,v108,v229
v_sub_f32 v109,v109,v229
v_sub_f32 v110,v110,v229
v_sub_f32 v111,v111,v229
v_sub_f32 v116,v116,v229
v_sub_f32 v117,v117,v229
v_sub_f32 v118,v118,v229
v_sub_f32 v119,v119,v229
v_sub_f32 v124,v124,v229
v_sub_f32 v125,v125,v229
v_sub_f32 v126,v126,v229
v_sub_f32 v127,v127,v229
v_mul_f32 v36,v36,v234
v_mul_f32 v37,v37,v234
v_mul_f32 v38,v38,v234
v_mul_f32 v39,v39,v234
AR v240,a0
v_mul_f32 v240,v240,v234
AW a0,v240
AR v241,a1
v_mul_f32 v241,v241,v234
AW a1,v241
AR v242,a2
v_mul_f32 v242,v242,v234
AW a2,v242
AR v243,a3
v_mul_f32 v243,v243,v234
AW a3,v243
AR v240,a4
v_mul_f32 v240,v240,v234
AW a4,v240
AR v241,a5
v_mul_f32 v241,v241,v234
AW a5,v241
AR v242,a6
v_mul_f32 v242,v242,v234
AW a6,v242
AR v243,a7
v_mul_f32 v243,v243,v234
AW a7,v243
AR v240,a8
v_mul_f32 v240,v240,v234
AW a8,v240
AR v241,a9
v_mul_f32 v241,v241,v234
AW a9,v241
AR v242,a10
v_mul_f32 v242,v242,v234
AW a10,v242
AR v243,a11
v_mul_f32 v243,v243,v234
AW a11,v243
AR v240,a12
v_mul_f32 v240,v240,v234
AW a12,v240
AR v241,a13
v_mul_f32 v241,v241,v234
AW a13,v241
AR v242,a14
v_mul_f32 v242,v242,v234
AW a14,v242
AR v243,a15
v_mul_f32 v243,v243,v234
AW a15,v243
AR v240,a16
v_mul_f32 v240,v240,v234
AW a16,v240
AR v241,a17
v_mul_f32 v241,v241,v234
AW a17,v241
AR v242,a18
v_mul_f32 v242,v242,v234
AW a18,v242
AR v243,a19
v_mul_f32 v243,v243,v234
AW a19,v243
AR v240,a20
v_mul_f32 v240,v240,v234
AW a20,v240
AR v241,a21
v_mul_f32 v241,v241,v234
AW a21,v241
AR v242,a22
v_mul_f32 v242,v242,v234
AW a22,v242
AR v243,a23
v_mul_f32 v243,v243,v234
AW a23,v243
AR v240,a24
v_mul_f32 v240,v240,v234
AW a24,v240
AR v241,a25
v_mul_f32 v241,v241,v234
AW a25,v241
AR v242,a26
v_mul_f32 v242,v242,v234
AW a26,v242
AR v243,a27
v_mul_f32 v243,v243,v234
AW a27,v243
AR v240,a28
v_mul_f32 v240,v240,v234
AW a28,v240
AR v241,a29
v_mul_f32 v241,v241,v234
AW a29,v241
AR v242,a30
v_mul_f32 v242,v242,v234
AW a30,v242
AR v243,a31
v_mul_f32 v243,v243,v234
AW a31,v243
E v235,-v230
v_sub_f32 v24,v24,v230
v_sub_f32 v25,v25,v230
v_sub_f32 v26,v26,v230
v_sub_f32 v27,v27,v230
v_sub_f32 v104,v104,v230
v_sub_f32 v105,v105,v230
v_sub_f32 v106,v106,v230
v_sub_f32 v107,v107,v230
v_sub_f32 v112,v112,v230
v_sub_f32 v113,v113,v230
v_sub_f32 v114,v114,v230
v_sub_f32 v115,v115,v230
v_sub_f32 v120,v120,v230
v_sub_f32 v121,v121,v230
v_sub_f32 v122,v122,v230
v_sub_f32 v123,v123,v230
v_sub_f32 v128,v128,v230
v_sub_f32 v129,v129,v230
v_sub_f32 v130,v130,v230
v_sub_f32 v131,v131,v230
v_mul_f32 v40,v40,v235
v_mul_f32 v41,v41,v235
v_mul_f32 v42,v42,v235
v_mul_f32 v43,v43,v235
AR v240,a32
v_mul_f32 v240,v240,v235
AW a32,v240
AR v241,a33
v_mul_f32 v241,v241,v235
AW a33,v241
AR v242,a34
v_mul_f32 v242,v242,v235
AW a34,v242
AR v243,a35
v_mul_f32 v243,v243,v235
AW a35,v243
AR v240,a36
v_mul_f32 v240,v240,v235
AW a36,v240
AR v241,a37
v_mul_f32 v241,v241,v235
AW a37,v241
AR v242,a38
v_mul_f32 v242,v242,v235
AW a38,v242
AR v243,a39
v_mul_f32 v243,v243,v235
AW a39,v243
AR v240,a40
v_mul_f32 v240,v240,v235
AW a40,v240
AR v241,a41
v_mul_f32 v241,v241,v235
AW a41,v241
AR v242,a42
v_mul_f32 v242,v242,v235
AW a42,v242
AR v243,a43
v_mul_f32 v243,v243,v235
AW a43,v243
AR v240,a44
v_mul_f32 v240,v240,v235
AW a44,v240
AR v241,a45
v_mul_f32 v241,v241,v235
AW a45,v241
AR v242,a46
v_mul_f32 v242,v242,v235
AW a46,v242
AR v243,a47
v_mul_f32 v243,v243,v235
AW a47,v243
AR v240,a48
v_mul_f32 v240,v240,v235
AW a48,v240
AR v241,a49
v_mul_f32 v241,v241,v235
AW a49,v241
AR v242,a50
v_mul_f32 v242,v242,v235
AW a50,v242
AR v243,a51
v_mul_f32 v243,v243,v235
AW a51,v243
AR v240,a52
v_mul_f32 v240,v240,v235
AW a52,v240
AR v241,a53
v_mul_f32 v241,v241,v235
AW a53,v241
AR v242,a54
v_mul_f32 v242,v242,v235
AW a54,v242
AR v243,a55
v_mul_f32 v243,v243,v235
AW a55,v243
AR v240,a56
v_mul_f32 v240,v240,v235
AW a56,v240
AR v241,a57
v_mul_f32 v241,v241,v235
AW a57,v241
AR v242,a58
v_mul_f32 v242,v242,v235
AW a58,v242
AR v243,a59
v_mul_f32 v243,v243,v235
AW a59,v243
AR v240,a60
v_mul_f32 v240,v240,v235
AW a60,v240
AR v241,a61
v_mul_f32 v241,v241,v235
AW a61,v241
AR v242,a62
v_mul_f32 v242,v242,v235
AW a62,v242
AR v243,a63
v_mul_f32 v243,v243,v235
AW a63,v243
E v8,v100
E v9,v101
E v10,v102
E v11,v103
C v148,v8,v9
C v149,v10,v11
E v12,v104
E v13,v105
E v14,v106
E v15,v107
C v152,v12,v13
C v153,v14,v15
E v8,v108
E v9,v109
E v10,v110
E v11,v111
C v150,v8,v9
C v151,v10,v11
E v12,v112
E v13,v113
E v14,v114
E v15,v115
C v154,v12,v13
C v155,v14,v15
E v8,v116
E v9,v117
E v10,v118
E v11,v119
C v156,v8,v9
C v157,v10,v11
E v12,v120
E v13,v121
E v14,v122
E v15,v123
C v160,v12,v13
C v161,v14,v15
E v8,v124
E v9,v125
E v10,v126
E v11,v127
C v158,v8,v9
C v159,v10,v11
E v12,v128
E v13,v129
E v14,v130
E v15,v131
C v162,v12,v13
C v163,v14,v15
M v[52:55],v[16:19],v[148:151],v[36:39]
M v[56:59],v[16:19],v[152:155],v[40:43]
M v[52:55],v[16:19],v[156:159],v[52:55]
M v[56:59],v[16:19],v[160:163],v[56:59]
s_nop 7
s_branch f_rs_E
f_mask_E:
s_nop 7
v_mov_b32 v238,s65
s_cmp_eq_u32 s52,s59
s_cbranch_scc0 f_mask_E_a
v_cndmask_b32 v116,v116,v238,s[44:45]
v_cndmask_b32 v117,v117,v238,s[46:47]
v_cndmask_b32 v118,v118,v238,s[54:55]
v_cndmask_b32 v119,v119,v238,s[56:57]
v_cndmask_b32 v128,v128,v238,s[44:45]
v_cndmask_b32 v129,v129,v238,s[46:47]
v_cndmask_b32 v130,v130,v238,s[54:55]
v_cndmask_b32 v131,v131,v238,s[56:57]
v_mov_b32 v124,s65
v_mov_b32 v125,s65
v_mov_b32 v126,s65
v_mov_b32 v127,s65
s_branch f_rm_E
f_mask_E_a:
v_mov_b32 v116,s65
v_mov_b32 v117,s65
v_mov_b32 v118,s65
v_mov_b32 v119,s65
v_mov_b32 v120,s65
v_mov_b32 v121,s65
v_mov_b32 v122,s65
v_mov_b32 v123,s65
v_mov_b32 v124,s65
v_mov_b32 v125,s65
v_mov_b32 v126,s65
v_mov_b32 v127,s65
)"
R"(v_mov_b32 v128,s65
v_mov_b32 v129,s65
v_mov_b32 v130,s65
v_mov_b32 v131,s65
s_add_u32 s69,s59,8192
s_cmp_eq_u32 s52,s69
s_cbranch_scc0 f_mask_E_f
v_cndmask_b32 v100,v100,v238,s[44:45]
v_cndmask_b32 v101,v101,v238,s[46:47]
v_cndmask_b32 v102,v102,v238,s[54:55]
v_cndmask_b32 v103,v103,v238,s[56:57]
v_cndmask_b32 v112,v112,v238,s[44:45]
v_cndmask_b32 v113,v113,v238,s[46:47]
v_cndmask_b32 v114,v114,v238,s[54:55]
v_cndmask_b32 v115,v115,v238,s[56:57]
v_mov_b32 v108,s65
v_mov_b32 v109,s65
v_mov_b32 v110,s65
v_mov_b32 v111,s65
s_branch f_rm_E
f_mask_E_f:
v_mov_b32 v100,s65
v_mov_b32 v101,s65
v_mov_b32 v102,s65
v_mov_b32 v103,s65
v_mov_b32 v104,s65
v_mov_b32 v105,s65
v_mov_b32 v106,s65
v_mov_b32 v107,s65
v_mov_b32 v108,s65
v_mov_b32 v109,s65
v_mov_b32 v110,s65
v_mov_b32 v111,s65
v_mov_b32 v112,s65
v_mov_b32 v113,s65
v_mov_b32 v114,s65
v_mov_b32 v115,s65
s_branch f_rm_E
f_slow_E7:
s_nop 9
v_max3_f32 v229,v100,v101,v102
v_max3_f32 v229,v229,v103,v108
v_max3_f32 v229,v229,v109,v110
v_max3_f32 v229,v229,v111,v116
v_max3_f32 v229,v229,v117,v118
v_max3_f32 v229,v229,v119,v124
v_max3_f32 v229,v229,v125,v126
v_max3_f32 v229,v229,v127,s62
v_mov_b32 v233,v229
s_nop 1
v_permlane32_swap_b32 v229,v233
v_max_f32 v229,v229,v233
v_mov_b32 v233,v229
s_nop 1
v_permlane16_swap_b32 v229,v233
v_max_f32 v229,v229,v233
v_add_f32 v229,1109393408,v229
v_max3_f32 v230,v104,v105,v106
v_max3_f32 v230,v230,v107,v112
v_max3_f32 v230,v230,v113,v114
v_max3_f32 v230,v230,v115,v120
v_max3_f32 v230,v230,v121,v122
v_max3_f32 v230,v230,v123,v128
v_max3_f32 v230,v230,v129,v130
v_max3_f32 v230,v230,v131,s62
v_mov_b32 v233,v230
s_nop 1
v_permlane32_swap_b32 v230,v233
v_max_f32 v230,v230,v233
v_mov_b32 v233,v230
s_nop 1
v_permlane16_swap_b32 v230,v233
v_max_f32 v230,v230,v233
v_add_f32 v230,1109393408,v230
E v234,-v229
v_sub_f32 v20,v20,v229
v_sub_f32 v21,v21,v229
v_sub_f32 v22,v22,v229
v_sub_f32 v23,v23,v229
v_sub_f32 v100,v100,v229
v_sub_f32 v101,v101,v229
v_sub_f32 v102,v102,v229
v_sub_f32 v103,v103,v229
v_sub_f32 v108,v108,v229
v_sub_f32 v109,v109,v229
v_sub_f32 v110,v110,v229
v_sub_f32 v111,v111,v229
v_sub_f32 v116,v116,v229
v_sub_f32 v117,v117,v229
v_sub_f32 v118,v118,v229
v_sub_f32 v119,v119,v229
v_sub_f32 v124,v124,v229
v_sub_f32 v125,v125,v229
v_sub_f32 v126,v126,v229
v_sub_f32 v127,v127,v229
v_mul_f32 v36,v36,v234
v_mul_f32 v37,v37,v234
v_mul_f32 v38,v38,v234
v_mul_f32 v39,v39,v234
AR v240,a0
v_mul_f32 v240,v240,v234
AW a0,v240
AR v241,a1
v_mul_f32 v241,v241,v234
AW a1,v241
AR v242,a2
v_mul_f32 v242,v242,v234
AW a2,v242
AR v243,a3
v_mul_f32 v243,v243,v234
AW a3,v243
AR v240,a4
v_mul_f32 v240,v240,v234
AW a4,v240
AR v241,a5
v_mul_f32 v241,v241,v234
AW a5,v241
AR v242,a6
v_mul_f32 v242,v242,v234
AW a6,v242
AR v243,a7
v_mul_f32 v243,v243,v234
AW a7,v243
AR v240,a8
v_mul_f32 v240,v240,v234
AW a8,v240
AR v241,a9
v_mul_f32 v241,v241,v234
AW a9,v241
AR v242,a10
v_mul_f32 v242,v242,v234
AW a10,v242
AR v243,a11
v_mul_f32 v243,v243,v234
AW a11,v243
AR v240,a12
v_mul_f32 v240,v240,v234
AW a12,v240
AR v241,a13
v_mul_f32 v241,v241,v234
AW a13,v241
AR v242,a14
v_mul_f32 v242,v242,v234
AW a14,v242
AR v243,a15
v_mul_f32 v243,v243,v234
AW a15,v243
AR v240,a16
v_mul_f32 v240,v240,v234
AW a16,v240
AR v241,a17
v_mul_f32 v241,v241,v234
AW a17,v241
AR v242,a18
v_mul_f32 v242,v242,v234
AW a18,v242
AR v243,a19
v_mul_f32 v243,v243,v234
AW a19,v243
AR v240,a20
v_mul_f32 v240,v240,v234
AW a20,v240
AR v241,a21
v_mul_f32 v241,v241,v234
AW a21,v241
AR v242,a22
v_mul_f32 v242,v242,v234
AW a22,v242
AR v243,a23
v_mul_f32 v243,v243,v234
AW a23,v243
AR v240,a24
v_mul_f32 v240,v240,v234
AW a24,v240
AR v241,a25
v_mul_f32 v241,v241,v234
AW a25,v241
AR v242,a26
v_mul_f32 v242,v242,v234
AW a26,v242
AR v243,a27
v_mul_f32 v243,v243,v234
AW a27,v243
AR v240,a28
v_mul_f32 v240,v240,v234
AW a28,v240
AR v241,a29
v_mul_f32 v241,v241,v234
AW a29,v241
AR v242,a30
v_mul_f32 v242,v242,v234
AW a30,v242
AR v243,a31
v_mul_f32 v243,v243,v234
AW a31,v243
E v235,-v230
v_sub_f32 v24,v24,v230
v_sub_f32 v25,v25,v230
v_sub_f32 v26,v26,v230
v_sub_f32 v27,v27,v230
v_sub_f32 v104,v104,v230
v_sub_f32 v105,v105,v230
v_sub_f32 v106,v106,v230
v_sub_f32 v107,v107,v230
v_sub_f32 v112,v112,v230
v_sub_f32 v113,v113,v230
v_sub_f32 v114,v114,v230
v_sub_f32 v115,v115,v230
v_sub_f32 v120,v120,v230
v_sub_f32 v121,v121,v230
v_sub_f32 v122,v122,v230
v_sub_f32 v123,v123,v230
v_sub_f32 v128,v128,v230
v_sub_f32 v129,v129,v230
v_sub_f32 v130,v130,v230
v_sub_f32 v131,v131,v230
v_mul_f32 v40,v40,v235
v_mul_f32 v41,v41,v235
v_mul_f32 v42,v42,v235
v_mul_f32 v43,v43,v235
AR v240,a32
v_mul_f32 v240,v240,v235
AW a32,v240
AR v241,a33
v_mul_f32 v241,v241,v235
AW a33,v241
AR v242,a34
v_mul_f32 v242,v242,v235
AW a34,v242
AR v243,a35
v_mul_f32 v243,v243,v235
AW a35,v243
AR v240,a36
v_mul_f32 v240,v240,v235
AW a36,v240
AR v241,a37
v_mul_f32 v241,v241,v235
AW a37,v241
AR v242,a38
v_mul_f32 v242,v242,v235
AW a38,v242
AR v243,a39
v_mul_f32 v243,v243,v235
AW a39,v243
AR v240,a40
v_mul_f32 v240,v240,v235
AW a40,v240
AR v241,a41
v_mul_f32 v241,v241,v235
AW a41,v241
AR v242,a42
v_mul_f32 v242,v242,v235
AW a42,v242
AR v243,a43
v_mul_f32 v243,v243,v235
AW a43,v243
AR v240,a44
v_mul_f32 v240,v240,v235
AW a44,v240
AR v241,a45
v_mul_f32 v241,v241,v235
AW a45,v241
AR v242,a46
v_mul_f32 v242,v242,v235
AW a46,v242
AR v243,a47
v_mul_f32 v243,v243,v235
AW a47,v243
AR v240,a48
v_mul_f32 v240,v240,v235
AW a48,v240
AR v241,a49
v_mul_f32 v241,v241,v235
AW a49,v241
AR v242,a50
v_mul_f32 v242,v242,v235
AW a50,v242
AR v243,a51
v_mul_f32 v243,v243,v235
AW a51,v243
AR v240,a52
v_mul_f32 v240,v240,v235
AW a52,v240
AR v241,a53
v_mul_f32 v241,v241,v235
AW a53,v241
AR v242,a54
v_mul_f32 v242,v242,v235
AW a54,v242
AR v243,a55
v_mul_f32 v243,v243,v235
AW a55,v243
AR v240,a56
v_mul_f32 v240,v240,v235
AW a56,v240
AR v241,a57
v_mul_f32 v241,v241,v235
AW a57,v241
AR v242,a58
v_mul_f32 v242,v242,v235
AW a58,v242
AR v243,a59
v_mul_f32 v243,v243,v235
AW a59,v243
AR v240,a60
v_mul_f32 v240,v240,v235
AW a60,v240
AR v241,a61
v_mul_f32 v241,v241,v235
AW a61,v241
AR v242,a62
v_mul_f32 v242,v242,v235
AW a62,v242
AR v243,a63
v_mul_f32 v243,v243,v235
AW a63,v243
E v8,v100
E v9,v101
E v10,v102
E v11,v103
C v148,v8,v9
C v149,v10,v11
E v12,v104
E v13,v105
E v14,v106
E v15,v107
C v152,v12,v13
C v153,v14,v15
E v8,v108
E v9,v109
E v10,v110
E v11,v111
C v150,v8,v9
C v151,v10,v11
E v12,v112
E v13,v113
E v14,v114
E v15,v115
C v154,v12,v13
C v155,v14,v15
E v8,v116
E v9,v117
E v10,v118
E v11,v119
C v156,v8,v9
C v157,v10,v11
E v12,v120
E v13,v121
E v14,v122
E v15,v123
C v160,v12,v13
C v161,v14,v15
E v8,v124
E v9,v125
E v10,v126
E v11,v127
C v158,v8,v9
C v159,v10,v11
E v12,v128
E v13,v129
E v14,v130
E v15,v131
C v162,v12,v13
C v163,v14,v15
M v[52:55],v[16:19],v[148:151],v[36:39]
M v[56:59],v[16:19],v[152:155],v[40:43]
M v[52:55],v[16:19],v[156:159],v[52:55]
M v[56:59],v[16:19],v[160:163],v[56:59]
s_nop 7
s_branch f_rs_E7
f_mask_E7:
s_nop 7
v_mov_b32 v238,s65
s_cmp_eq_u32 s52,s59
s_cbranch_scc0 f_mask_E7_a
v_cndmask_b32 v116,v116,v238,s[44:45]
v_cndmask_b32 v117,v117,v238,s[46:47]
v_cndmask_b32 v118,v118,v238,s[54:55]
v_cndmask_b32 v119,v119,v238,s[56:57]
v_cndmask_b32 v128,v128,v238,s[44:45]
v_cndmask_b32 v129,v129,v238,s[46:47]
v_cndmask_b32 v130,v130,v238,s[54:55]
v_cndmask_b32 v131,v131,v238,s[56:57]
v_mov_b32 v124,s65
v_mov_b32 v125,s65
v_mov_b32 v126,s65
v_mov_b32 v127,s65
s_branch f_rm_E7
f_mask_E7_a:
v_mov_b32 v116,s65
v_mov_b32 v117,s65
v_mov_b32 v118,s65
v_mov_b32 v119,s65
v_mov_b32 v120,s65
v_mov_b32 v121,s65
v_mov_b32 v122,s65
v_mov_b32 v123,s65
v_mov_b32 v124,s65
v_mov_b32 v125,s65
v_mov_b32 v126,s65
)"
R"(v_mov_b32 v127,s65
v_mov_b32 v128,s65
v_mov_b32 v129,s65
v_mov_b32 v130,s65
v_mov_b32 v131,s65
s_add_u32 s69,s59,8192
s_cmp_eq_u32 s52,s69
s_cbranch_scc0 f_mask_E7_f
v_cndmask_b32 v100,v100,v238,s[44:45]
v_cndmask_b32 v101,v101,v238,s[46:47]
v_cndmask_b32 v102,v102,v238,s[54:55]
v_cndmask_b32 v103,v103,v238,s[56:57]
v_cndmask_b32 v112,v112,v238,s[44:45]
v_cndmask_b32 v113,v113,v238,s[46:47]
v_cndmask_b32 v114,v114,v238,s[54:55]
v_cndmask_b32 v115,v115,v238,s[56:57]
v_mov_b32 v108,s65
v_mov_b32 v109,s65
v_mov_b32 v110,s65
v_mov_b32 v111,s65
s_branch f_rm_E7
f_mask_E7_f:
v_mov_b32 v100,s65
v_mov_b32 v101,s65
v_mov_b32 v102,s65
v_mov_b32 v103,s65
v_mov_b32 v104,s65
v_mov_b32 v105,s65
v_mov_b32 v106,s65
v_mov_b32 v107,s65
v_mov_b32 v108,s65
v_mov_b32 v109,s65
v_mov_b32 v110,s65
v_mov_b32 v111,s65
v_mov_b32 v112,s65
v_mov_b32 v113,s65
v_mov_b32 v114,s65
v_mov_b32 v115,s65
s_branch f_rm_E7
f_kpA:
v_add_u32 v230,16384,v5
WV 63
v_cmp_ne_u32 vcc,s96,v252
v_cmp_ne_u32 s[92:93],s97,v253
s_nop 1
s_or_b64 vcc,vcc,s[92:93]
s_cbranch_vccz f_c5A_ok
s_mov_b32 s98,200000
f_c5A_spin:
s_sleep 2
global_load_dwordx2 v[252:253],v230,s[94:95] sc1
WV 0
v_cmp_ne_u32 vcc,s96,v252
v_cmp_ne_u32 s[92:93],s97,v253
s_nop 1
s_or_b64 vcc,vcc,s[92:93]
s_cbranch_vccz f_c5A_ok
s_sub_u32 s98,s98,1
s_cbranch_scc0 f_c5A_spin
f_c5A_ok:
s_lshl_b32 s21,s20,10
v_add_u32 v5,s21,v0
v_mov_b32 v230,2130706432
v_cmp_gt_u32 vcc,3456,v5
s_nop 1
v_cndmask_b32 v5,v230,v5,vcc
s_mul_i32 s21,s35,55296
s_add_u32 s21,s21,8388608
s_add_u32 s84,s80,s21
s_addc_u32 s85,s81,0
s_mov_b32 s86,55296
v_mov_b32 v248,0
v_mov_b32 v249,0
v_mov_b32 v250,0
v_mov_b32 v251,0
v_mov_b32 v252,0
v_mov_b32 v253,0
v_mov_b32 v254,0
v_mov_b32 v255,0
s_mov_b32 s26,3456
s_mov_b32 s78,6912
s_branch f_krA
f_kpD:
v_add_u32 v230,16384,v5
WV 63
v_cmp_ne_u32 vcc,s96,v252
v_cmp_ne_u32 s[92:93],s97,v253
s_nop 1
s_or_b64 vcc,vcc,s[92:93]
s_cbranch_vccz f_c5D_ok
s_mov_b32 s98,200000
f_c5D_spin:
s_sleep 2
global_load_dwordx2 v[252:253],v230,s[94:95] sc1
WV 0
v_cmp_ne_u32 vcc,s96,v252
v_cmp_ne_u32 s[92:93],s97,v253
s_nop 1
s_or_b64 vcc,vcc,s[92:93]
s_cbranch_vccz f_c5D_ok
s_sub_u32 s98,s98,1
s_cbranch_scc0 f_c5D_spin
f_c5D_ok:
s_lshl_b32 s21,s20,10
v_add_u32 v5,s21,v0
v_mov_b32 v230,2130706432
v_cmp_gt_u32 vcc,3456,v5
s_nop 1
v_cndmask_b32 v5,v230,v5,vcc
s_mul_i32 s21,s35,55296
s_add_u32 s21,s21,8388608
s_add_u32 s84,s80,s21
s_addc_u32 s85,s81,0
s_mov_b32 s86,55296
v_mov_b32 v248,0
v_mov_b32 v249,0
v_mov_b32 v250,0
v_mov_b32 v251,0
v_mov_b32 v252,0
v_mov_b32 v253,0
v_mov_b32 v254,0
v_mov_b32 v255,0
s_mov_b32 s26,3456
s_mov_b32 s78,6912
s_branch f_krD
f_end:
WV 8
C v72,v248,v249
C v73,v250,v251
WL 0
s_barrier
ds_write_b64 v78,v[72:73] offset:0
s_mov_b64 exec,s[90:91]
ds_write_b64 v78,v[72:73] offset:512
ds_write_b64 v78,v[72:73] offset:1024
ds_write_b64 v78,v[72:73] offset:1536
s_mov_b64 exec,-1
WL 0
s_barrier
D a[0:3],v76,0
D a[4:7],v76,64
D a[8:11],v76,2048
D a[12:15],v76,2112
s_mov_b32 s24,897988541
WL 1
M v[168:171],a[8:11],a[96:99],0
M v[164:167],a[0:3],a[96:99],0
M v[176:179],a[8:11],a[104:107],0
M v[172:175],a[0:3],a[104:107],0
M v[184:187],a[8:11],a[112:115],0
M v[180:183],a[0:3],a[112:115],0
M v[192:195],a[8:11],a[120:123],0
M v[188:191],a[0:3],a[120:123],0
WL 0
M v[168:171],a[12:15],a[100:103],v[168:171]
M v[164:167],a[4:7],a[100:103],v[164:167]
M v[176:179],a[12:15],a[108:111],v[176:179]
M v[172:175],a[4:7],a[108:111],v[172:175]
M v[184:187],a[12:15],a[116:119],v[184:187]
M v[180:183],a[4:7],a[116:119],v[180:183]
M v[192:195],a[12:15],a[124:127],v[192:195]
M v[188:191],a[4:7],a[124:127],v[188:191]
s_nop 0
v_add_f32 v80,s24,v170
s_nop 0
v_add_f32 v81,s24,v178
s_nop 0
v_add_f32 v82,s24,v186
s_nop 0
v_add_f32 v83,s24,v194
v_rcp_f32 v80,v80
v_rcp_f32 v81,v81
v_rcp_f32 v82,v82
v_rcp_f32 v83,v83
v_mul_f32 v164,v164,v80
v_mul_f32 v165,v165,v80
v_mul_f32 v166,v166,v80
v_mul_f32 v167,v167,v80
v_mul_f32 v168,v168,v80
v_mul_f32 v169,v169,v80
C v164,v164,v165
C v165,v166,v167
C v168,v168,v169
v_mul_f32 v172,v172,v81
v_mul_f32 v173,v173,v81
v_mul_f32 v174,v174,v81
v_mul_f32 v175,v175,v81
v_mul_f32 v176,v176,v81
v_mul_f32 v177,v177,v81
C v172,v172,v173
C v173,v174,v175
C v176,v176,v177
v_mul_f32 v180,v180,v82
v_mul_f32 v181,v181,v82
v_mul_f32 v182,v182,v82
v_mul_f32 v183,v183,v82
v_mul_f32 v184,v184,v82
v_mul_f32 v185,v185,v82
C v180,v180,v181
C v181,v182,v183
C v184,v184,v185
v_mul_f32 v188,v188,v83
v_mul_f32 v189,v189,v83
v_mul_f32 v190,v190,v83
v_mul_f32 v191,v191,v83
v_mul_f32 v192,v192,v83
v_mul_f32 v193,v193,v83
C v188,v188,v189
C v189,v190,v191
C v192,v192,v193
WV 0
s_barrier
s_cmp_lg_u32 s20,0
s_cbranch_scc1 f_pb3
s_lshl_b32 s92,%2,6
s_add_u32 s92,s92,32768
v_mov_b32 v30,s92
v_mov_b32 v26,s96
v_mov_b32 v27,s97
s_mov_b64 exec,1
global_store_dwordx2 v30,v[26:27],s[94:95] sc1
s_mov_b64 exec,-1
f_pb3:
buffer_store_dwordx2 v[164:165],v77,s[44:47],s74 offen offset:0 sc1
buffer_store_dword v168,v68,s[44:47],s74 offen offset:0 sc1
s_add_u32 s74,s74,18432
buffer_store_dwordx2 v[172:173],v77,s[44:47],s74 offen offset:0 sc1
buffer_store_dword v176,v68,s[44:47],s74 offen offset:0 sc1
s_add_u32 s74,s74,18432
buffer_store_dwordx2 v[180:181],v77,s[44:47],s74 offen offset:0 sc1
buffer_store_dword v184,v68,s[44:47],s74 offen offset:0 sc1
s_add_u32 s74,s74,18432
buffer_store_dwordx2 v[188:189],v77,s[44:47],s74 offen offset:0 sc1
buffer_store_dword v192,v68,s[44:47],s74 offen offset:0 sc1
s_cmp_ge_u32 s73,234
s_cbranch_scc1 f_idle
s_add_u32 m0,s68,0
s_nop 0
G v1,s[40:43],s64,0
G v2,s[40:43],s64,1024
G v3,s[40:43],s64,2048
G v4,s[40:43],s64,3072
s_add_u32 s64,s64,36864
s_add_u32 m0,s68,4096
s_nop 0
G v1,s[40:43],s64,0
G v2,s[40:43],s64,1024
G v3,s[40:43],s64,2048
G v4,s[40:43],s64,3072
s_add_u32 s64,s64,36864
s_add_u32 m0,s68,8192
s_nop 0
G v1,s[40:43],s64,0
G v2,s[40:43],s64,1024
G v3,s[40:43],s64,2048
G v4,s[40:43],s64,3072
s_add_u32 s64,s64,36864
s_add_u32 m0,s68,12288
s_nop 0
G v1,s[40:43],s64,0
G v2,s[40:43],s64,1024
G v3,s[40:43],s64,2048
G v4,s[40:43],s64,3072
s_add_u32 s64,s64,36864
s_add_u32 m0,s68,16384
s_nop 0
G v1,s[40:43],s64,0
G v2,s[40:43],s64,1024
G v3,s[40:43],s64,2048
G v4,s[40:43],s64,3072
s_add_u32 s64,s64,36864
s_add_u32 m0,s68,20480
s_nop 0
G v1,s[40:43],s64,0
G v2,s[40:43],s64,1024
G v3,s[40:43],s64,2048
G v4,s[40:43],s64,3072
s_add_u32 s64,s64,36864
WV 24
s_barrier
s_cmp_lg_u32 s20,0
s_cbranch_scc1 f_pb4
s_lshl_b32 s92,%2,6
s_add_u32 s92,s92,49152
v_mov_b32 v30,s92
v_mov_b32 v24,s96
v_mov_b32 v25,s97
s_mov_b64 exec,1
global_store_dwordx2 v30,v[24:25],s[94:95] sc1
s_mov_b64 exec,-1
f_pb4:
global_load_dwordx2 v[26:27],v29,s[94:95] sc1
WV 0
v_cmp_ne_u32 vcc,s96,v26
v_cmp_ne_u32 s[92:93],s97,v27
s_nop 1
s_or_b64 vcc,vcc,s[92:93]
s_cbranch_vccz f_c3_ok
s_mov_b32 s98,200000
f_c3_spin:
s_sleep 2
global_load_dwordx2 v[26:27],v29,s[94:95] sc1
WV 0
v_cmp_ne_u32 vcc,s96,v26
v_cmp_ne_u32 s[92:93],s97,v27
s_nop 1
s_or_b64 vcc,vcc,s[92:93]
s_cbranch_vccz f_c3_ok
s_sub_u32 s98,s98,1
s_cbranch_scc0 f_c3_spin
f_c3_ok:
WV 0
buffer_load_dwordx4 v[32:35],v0,s[36:39],s56 offen offset:0 sc1
buffer_load_dwordx4 v[36:39],v0,s[36:39],s57 offen offset:0 sc1
buffer_load_dwordx4 v[40:43],v0,s[36:39],s58 offen offset:0 sc1
buffer_load_dwordx4 v[44:47],v0,s[36:39],s59 offen offset:0 sc1
buffer_load_dwordx4 v[48:51],v0,s[36:39],s60 offen offset:0 sc1
buffer_load_dwordx4 v[52:55],v0,s[36:39],s56 offen offset:1024 sc1
buffer_load_dwordx4 v[56:59],v0,s[36:39],s57 offen offset:1024 sc1
buffer_load_dwordx4 v[60:63],v0,s[36:39],s58 offen offset:1024 sc1
buffer_load_dwordx4 v[64:67],v0,s[36:39],s59 offen offset:1024 sc1
buffer_load_dwordx4 v[68:71],v0,s[36:39],s60 offen offset:1024 sc1
buffer_load_dwordx4 v[72:75],v0,s[36:39],s56 offen offset:2048 sc1
buffer_load_dwordx4 v[76:79],v0,s[36:39],s57 offen offset:2048 sc1
buffer_load_dwordx4 v[80:83],v0,s[36:39],s58 offen offset:2048 sc1
buffer_load_dwordx4 v[84:87],v0,s[36:39],s59 offen offset:2048 sc1
buffer_load_dwordx4 v[88:91],v0,s[36:39],s60 offen offset:2048 sc1
buffer_load_dwordx4 v[92:95],v0,s[36:39],s56 offen offset:3072 sc1
s_add_u32 s56,s56,4096
buffer_load_dwordx4 v[96:99],v0,s[36:39],s57 offen offset:3072 sc1
s_add_u32 s57,s57,4096
buffer_load_dwordx4 v[100:103],v0,s[36:39],s58 offen offset:3072 sc1
s_add_u32 s58,s58,4096
buffer_load_dwordx4 v[104:107],v0,s[36:39],s59 offen offset:3072 sc1
s_add_u32 s59,s59,4096
buffer_load_dwordx4 v[108:111],v0,s[36:39],s60 offen offset:3072 sc1
s_add_u32 s60,s60,4096
buffer_load_dwordx4 v[112:115],v0,s[36:39],s56 offen offset:0 sc1
buffer_load_dwordx4 v[116:119],v0,s[36:39],s57 offen offset:0 sc1
buffer_load_dwordx4 v[120:123],v0,s[36:39],s58 offen offset:0 sc1
buffer_load_dwordx4 v[124:127],v0,s[36:39],s59 offen offset:0 sc1
buffer_load_dwordx4 v[128:131],v0,s[36:39],s60 offen offset:0 sc1
buffer_load_dwordx4 v[132:135],v0,s[36:39],s56 offen offset:1024 sc1
buffer_load_dwordx4 v[136:139],v0,s[36:39],s57 offen offset:1024 sc1
buffer_load_dwordx4 v[140:143],v0,s[36:39],s58 offen offset:1024 sc1
buffer_load_dwordx4 v[144:147],v0,s[36:39],s59 offen offset:1024 sc1
buffer_load_dwordx4 v[148:151],v0,s[36:39],s60 offen offset:1024 sc1
T a[96:97],v9,0
T a[98:99],v9,2048
T a[100:101],v10,0
T a[102:103],v10,2048
T a[104:105],v11,0
T a[106:107],v11,2048
T a[108:109],v12,0
T a[110:111],v12,2048
WL 6
WV 29
M v[152:155],a[96:99],v[32:35],0
WL 4
M v[172:175],a[100:103],v[32:35],0
WL 2
T a[112:113],v9,4096
M v[192:195],a[104:107],v[32:35],0
WL 1
T a[114:115],v9,6144
M v[212:215],a[108:111],v[32:35],0
s_add_u32 m0,s68,0
T a[116:117],v10,4096
WV 28
M v[156:159],a[96:99],v[36:39],0
G v1,s[40:43],s64,0
M v[176:179],a[100:103],v[36:39],0
buffer_load_dwordx4 v[32:35],v0,s[36:39],s56 offen offset:2048 sc1
M v[196:199],a[104:107],v[36:39],0
T a[118:119],v10,6144
M v[216:219],a[108:111],v[36:39],0
G v2,s[40:43],s64,1024
WV 30
M v[160:163],a[96:99],v[40:43],0
T a[120:121],v11,4096
M v[180:183],a[100:103],v[40:43],0
T a[122:123],v11,6144
T a[124:125],v12,4096
M v[200:203],a[104:107],v[40:43],0
G v3,s[40:43],s64,2048
M v[220:223],a[108:111],v[40:43],0
buffer_load_dwordx4 v[36:39],v0,s[36:39],s57 offen offset:2048 sc1
WV 31
M v[164:167],a[96:99],v[44:47],0
T a[126:127],v12,6144
M v[184:187],a[100:103],v[44:47],0
G v4,s[40:43],s64,3072
M v[204:207],a[104:107],v[44:47],0
s_add_u32 s64,s64,36864
buffer_load_dwordx4 v[40:43],v0,s[36:39],s58 offen offset:2048 sc1
M v[224:227],a[108:111],v[44:47],0
buffer_load_dwordx4 v[44:47],v0,s[36:39],s59 offen offset:2048 sc1
WV 33
M v[168:171],a[96:99],v[48:51],0
M v[188:191],a[100:103],v[48:51],0
)"
R"(WL 1
M v[208:211],a[104:107],v[48:51],0
M v[228:231],a[108:111],v[48:51],0
buffer_load_dwordx4 v[48:51],v0,s[36:39],s60 offen offset:2048 sc1
WV 33
M v[152:155],a[112:115],v[52:55],v[152:155]
WL 0
M v[172:175],a[116:119],v[52:55],v[172:175]
s_add_u32 m0,s68,4096
T a[96:97],v9,8192
M v[192:195],a[120:123],v[52:55],v[192:195]
T a[98:99],v9,10240
M v[212:215],a[124:127],v[52:55],v[212:215]
G v1,s[40:43],s64,0
WV 33
M v[156:159],a[112:115],v[56:59],v[156:159]
T a[100:101],v10,8192
M v[176:179],a[116:119],v[56:59],v[176:179]
buffer_load_dwordx4 v[52:55],v0,s[36:39],s56 offen offset:3072 sc1
M v[196:199],a[120:123],v[56:59],v[196:199]
s_add_u32 s56,s56,4096
T a[102:103],v10,10240
M v[216:219],a[124:127],v[56:59],v[216:219]
G v2,s[40:43],s64,1024
WV 34
M v[160:163],a[112:115],v[60:63],v[160:163]
T a[104:105],v11,8192
M v[180:183],a[116:119],v[60:63],v[180:183]
T a[106:107],v11,10240
T a[108:109],v12,8192
M v[200:203],a[120:123],v[60:63],v[200:203]
G v3,s[40:43],s64,2048
M v[220:223],a[124:127],v[60:63],v[220:223]
buffer_load_dwordx4 v[56:59],v0,s[36:39],s57 offen offset:3072 sc1
WV 35
M v[164:167],a[112:115],v[64:67],v[164:167]
s_add_u32 s57,s57,4096
T a[110:111],v12,10240
M v[184:187],a[116:119],v[64:67],v[184:187]
G v4,s[40:43],s64,3072
M v[204:207],a[120:123],v[64:67],v[204:207]
s_add_u32 s64,s64,36864
buffer_load_dwordx4 v[60:63],v0,s[36:39],s58 offen offset:3072 sc1
M v[224:227],a[124:127],v[64:67],v[224:227]
s_add_u32 s58,s58,4096
buffer_load_dwordx4 v[64:67],v0,s[36:39],s59 offen offset:3072 sc1
WV 37
M v[168:171],a[112:115],v[68:71],v[168:171]
s_add_u32 s59,s59,4096
M v[188:191],a[116:119],v[68:71],v[188:191]
WL 1
M v[208:211],a[120:123],v[68:71],v[208:211]
M v[228:231],a[124:127],v[68:71],v[228:231]
buffer_load_dwordx4 v[68:71],v0,s[36:39],s60 offen offset:3072 sc1
WV 37
M v[152:155],a[96:99],v[72:75],v[152:155]
WL 0
M v[172:175],a[100:103],v[72:75],v[172:175]
s_add_u32 s60,s60,4096
s_add_u32 m0,s68,8192
T a[112:113],v9,12288
M v[192:195],a[104:107],v[72:75],v[192:195]
T a[114:115],v9,14336
M v[212:215],a[108:111],v[72:75],v[212:215]
G v1,s[40:43],s64,0
WV 37
M v[156:159],a[96:99],v[76:79],v[156:159]
T a[116:117],v10,12288
M v[176:179],a[100:103],v[76:79],v[176:179]
buffer_load_dwordx4 v[72:75],v0,s[36:39],s56 offen offset:0 sc1
M v[196:199],a[104:107],v[76:79],v[196:199]
T a[118:119],v10,14336
M v[216:219],a[108:111],v[76:79],v[216:219]
G v2,s[40:43],s64,1024
WV 38
M v[160:163],a[96:99],v[80:83],v[160:163]
T a[120:121],v11,12288
M v[180:183],a[100:103],v[80:83],v[180:183]
T a[122:123],v11,14336
T a[124:125],v12,12288
M v[200:203],a[104:107],v[80:83],v[200:203]
G v3,s[40:43],s64,2048
M v[220:223],a[108:111],v[80:83],v[220:223]
buffer_load_dwordx4 v[76:79],v0,s[36:39],s57 offen offset:0 sc1
WV 39
M v[164:167],a[96:99],v[84:87],v[164:167]
T a[126:127],v12,14336
M v[184:187],a[100:103],v[84:87],v[184:187]
G v4,s[40:43],s64,3072
M v[204:207],a[104:107],v[84:87],v[204:207]
s_add_u32 s64,s64,36864
buffer_load_dwordx4 v[80:83],v0,s[36:39],s58 offen offset:0 sc1
M v[224:227],a[108:111],v[84:87],v[224:227]
buffer_load_dwordx4 v[84:87],v0,s[36:39],s59 offen offset:0 sc1
WV 41
M v[168:171],a[96:99],v[88:91],v[168:171]
M v[188:191],a[100:103],v[88:91],v[188:191]
WL 1
M v[208:211],a[104:107],v[88:91],v[208:211]
M v[228:231],a[108:111],v[88:91],v[228:231]
buffer_load_dwordx4 v[88:91],v0,s[36:39],s60 offen offset:0 sc1
WV 41
M v[152:155],a[112:115],v[92:95],v[152:155]
WL 0
M v[172:175],a[116:119],v[92:95],v[172:175]
s_add_u32 m0,s68,12288
T a[96:97],v9,16384
M v[192:195],a[120:123],v[92:95],v[192:195]
T a[98:99],v9,18432
M v[212:215],a[124:127],v[92:95],v[212:215]
G v1,s[40:43],s64,0
WV 41
M v[156:159],a[112:115],v[96:99],v[156:159]
T a[100:101],v10,16384
M v[176:179],a[116:119],v[96:99],v[176:179]
buffer_load_dwordx4 v[92:95],v0,s[36:39],s56 offen offset:1024 sc1
M v[196:199],a[120:123],v[96:99],v[196:199]
T a[102:103],v10,18432
M v[216:219],a[124:127],v[96:99],v[216:219]
G v2,s[40:43],s64,1024
WV 42
M v[160:163],a[112:115],v[100:103],v[160:163]
T a[104:105],v11,16384
M v[180:183],a[116:119],v[100:103],v[180:183]
T a[106:107],v11,18432
T a[108:109],v12,16384
M v[200:203],a[120:123],v[100:103],v[200:203]
G v3,s[40:43],s64,2048
M v[220:223],a[124:127],v[100:103],v[220:223]
buffer_load_dwordx4 v[96:99],v0,s[36:39],s57 offen offset:1024 sc1
WV 43
M v[164:167],a[112:115],v[104:107],v[164:167]
T a[110:111],v12,18432
M v[184:187],a[116:119],v[104:107],v[184:187]
G v4,s[40:43],s64,3072
M v[204:207],a[120:123],v[104:107],v[204:207]
s_add_u32 s64,s64,36864
buffer_load_dwordx4 v[100:103],v0,s[36:39],s58 offen offset:1024 sc1
M v[224:227],a[124:127],v[104:107],v[224:227]
buffer_load_dwordx4 v[104:107],v0,s[36:39],s59 offen offset:1024 sc1
WV 45
M v[168:171],a[112:115],v[108:111],v[168:171]
M v[188:191],a[116:119],v[108:111],v[188:191]
WL 1
M v[208:211],a[120:123],v[108:111],v[208:211]
M v[228:231],a[124:127],v[108:111],v[228:231]
buffer_load_dwordx4 v[108:111],v0,s[36:39],s60 offen offset:1024 sc1
WV 45
M v[152:155],a[96:99],v[112:115],v[152:155]
WL 0
M v[172:175],a[100:103],v[112:115],v[172:175]
s_add_u32 m0,s68,16384
T a[112:113],v9,20480
M v[192:195],a[104:107],v[112:115],v[192:195]
T a[114:115],v9,22528
M v[212:215],a[108:111],v[112:115],v[212:215]
G v1,s[40:43],s64,0
WV 45
M v[156:159],a[96:99],v[116:119],v[156:159]
T a[116:117],v10,20480
M v[176:179],a[100:103],v[116:119],v[176:179]
buffer_load_dwordx4 v[112:115],v0,s[36:39],s56 offen offset:2048 sc1
M v[196:199],a[104:107],v[116:119],v[196:199]
T a[118:119],v10,22528
M v[216:219],a[108:111],v[116:119],v[216:219]
G v2,s[40:43],s64,1024
WV 46
M v[160:163],a[96:99],v[120:123],v[160:163]
T a[120:121],v11,20480
M v[180:183],a[100:103],v[120:123],v[180:183]
T a[122:123],v11,22528
T a[124:125],v12,20480
M v[200:203],a[104:107],v[120:123],v[200:203]
G v3,s[40:43],s64,2048
M v[220:223],a[108:111],v[120:123],v[220:223]
buffer_load_dwordx4 v[116:119],v0,s[36:39],s57 offen offset:2048 sc1
WV 47
M v[164:167],a[96:99],v[124:127],v[164:167]
T a[126:127],v12,22528
M v[184:187],a[100:103],v[124:127],v[184:187]
G v4,s[40:43],s64,3072
M v[204:207],a[104:107],v[124:127],v[204:207]
s_add_u32 s64,s64,36864
buffer_load_dwordx4 v[120:123],v0,s[36:39],s58 offen offset:2048 sc1
M v[224:227],a[108:111],v[124:127],v[224:227]
buffer_load_dwordx4 v[124:127],v0,s[36:39],s59 offen offset:2048 sc1
WV 49
M v[168:171],a[96:99],v[128:131],v[168:171]
M v[188:191],a[100:103],v[128:131],v[188:191]
WL 1
M v[208:211],a[104:107],v[128:131],v[208:211]
M v[228:231],a[108:111],v[128:131],v[228:231]
buffer_load_dwordx4 v[128:131],v0,s[36:39],s60 offen offset:2048 sc1
WV 49
M v[152:155],a[112:115],v[132:135],v[152:155]
WL 0
WV 39
M v[172:175],a[116:119],v[132:135],v[172:175]
s_add_u32 m0,s68,20480
T a[96:97],v9,0
M v[192:195],a[120:123],v[132:135],v[192:195]
T a[98:99],v9,2048
M v[212:215],a[124:127],v[132:135],v[212:215]
G v1,s[40:43],s64,0
M v[156:159],a[112:115],v[136:139],v[156:159]
T a[100:101],v10,0
M v[176:179],a[116:119],v[136:139],v[176:179]
buffer_load_dwordx4 v[132:135],v0,s[36:39],s56 offen offset:3072 sc1
M v[196:199],a[120:123],v[136:139],v[196:199]
s_add_u32 s56,s56,4096
T a[102:103],v10,2048
M v[216:219],a[124:127],v[136:139],v[216:219]
G v2,s[40:43],s64,1024
M v[160:163],a[112:115],v[140:143],v[160:163]
T a[104:105],v11,0
M v[180:183],a[116:119],v[140:143],v[180:183]
T a[106:107],v11,2048
T a[108:109],v12,0
M v[200:203],a[120:123],v[140:143],v[200:203]
G v3,s[40:43],s64,2048
M v[220:223],a[124:127],v[140:143],v[220:223]
buffer_load_dwordx4 v[136:139],v0,s[36:39],s57 offen offset:3072 sc1
M v[164:167],a[112:115],v[144:147],v[164:167]
s_add_u32 s57,s57,4096
T a[110:111],v12,2048
M v[184:187],a[116:119],v[144:147],v[184:187]
G v4,s[40:43],s64,3072
M v[204:207],a[120:123],v[144:147],v[204:207]
s_add_u32 s64,s64,36864
buffer_load_dwordx4 v[140:143],v0,s[36:39],s58 offen offset:3072 sc1
M v[224:227],a[124:127],v[144:147],v[224:227]
s_add_u32 s58,s58,4096
buffer_load_dwordx4 v[144:147],v0,s[36:39],s59 offen offset:3072 sc1
M v[168:171],a[112:115],v[148:151],v[168:171]
s_add_u32 s59,s59,4096
M v[188:191],a[116:119],v[148:151],v[188:191]
WL 1
M v[208:211],a[120:123],v[148:151],v[208:211]
M v[228:231],a[124:127],v[148:151],v[228:231]
buffer_load_dwordx4 v[148:151],v0,s[36:39],s60 offen offset:3072 sc1
M v[152:155],a[96:99],v[32:35],v[152:155]
WL 0
WV 39
M v[172:175],a[100:103],v[32:35],v[172:175]
s_add_u32 s60,s60,4096
s_add_u32 m0,s68,0
T a[112:113],v9,4096
M v[192:195],a[104:107],v[32:35],v[192:195]
T a[114:115],v9,6144
M v[212:215],a[108:111],v[32:35],v[212:215]
G v1,s[40:43],s64,0
M v[156:159],a[96:99],v[36:39],v[156:159]
T a[116:117],v10,4096
M v[176:179],a[100:103],v[36:39],v[176:179]
buffer_load_dwordx4 v[32:35],v0,s[36:39],s56 offen offset:0 sc1
M v[196:199],a[104:107],v[36:39],v[196:199]
T a[118:119],v10,6144
M v[216:219],a[108:111],v[36:39],v[216:219]
G v2,s[40:43],s64,1024
M v[160:163],a[96:99],v[40:43],v[160:163]
T a[120:121],v11,4096
M v[180:183],a[100:103],v[40:43],v[180:183]
T a[122:123],v11,6144
T a[124:125],v12,4096
M v[200:203],a[104:107],v[40:43],v[200:203]
G v3,s[40:43],s64,2048
M v[220:223],a[108:111],v[40:43],v[220:223]
buffer_load_dwordx4 v[36:39],v0,s[36:39],s57 offen offset:0 sc1
M v[164:167],a[96:99],v[44:47],v[164:167]
T a[126:127],v12,6144
M v[184:187],a[100:103],v[44:47],v[184:187]
G v4,s[40:43],s64,3072
M v[204:207],a[104:107],v[44:47],v[204:207]
s_add_u32 s64,s64,36864
buffer_load_dwordx4 v[40:43],v0,s[36:39],s58 offen offset:0 sc1
M v[224:227],a[108:111],v[44:47],v[224:227]
buffer_load_dwordx4 v[44:47],v0,s[36:39],s59 offen offset:0 sc1
M v[168:171],a[96:99],v[48:51],v[168:171]
M v[188:191],a[100:103],v[48:51],v[188:191]
WL 1
M v[208:211],a[104:107],v[48:51],v[208:211]
M v[228:231],a[108:111],v[48:51],v[228:231]
buffer_load_dwordx4 v[48:51],v0,s[36:39],s60 offen offset:0 sc1
M v[152:155],a[112:115],v[52:55],v[152:155]
WL 0
WV 39
M v[172:175],a[116:119],v[52:55],v[172:175]
s_add_u32 m0,s68,4096
T a[96:97],v9,8192
M v[192:195],a[120:123],v[52:55],v[192:195]
T a[98:99],v9,10240
M v[212:215],a[124:127],v[52:55],v[212:215]
G v1,s[40:43],s64,0
M v[156:159],a[112:115],v[56:59],v[156:159]
T a[100:101],v10,8192
M v[176:179],a[116:119],v[56:59],v[176:179]
buffer_load_dwordx4 v[52:55],v0,s[36:39],s56 offen offset:1024 sc1
M v[196:199],a[120:123],v[56:59],v[196:199]
T a[102:103],v10,10240
M v[216:219],a[124:127],v[56:59],v[216:219]
G v2,s[40:43],s64,1024
M v[160:163],a[112:115],v[60:63],v[160:163]
T a[104:105],v11,8192
M v[180:183],a[116:119],v[60:63],v[180:183]
T a[106:107],v11,10240
T a[108:109],v12,8192
M v[200:203],a[120:123],v[60:63],v[200:203]
G v3,s[40:43],s64,2048
M v[220:223],a[124:127],v[60:63],v[220:223]
buffer_load_dwordx4 v[56:59],v0,s[36:39],s57 offen offset:1024 sc1
M v[164:167],a[112:115],v[64:67],v[164:167]
T a[110:111],v12,10240
M v[184:187],a[116:119],v[64:67],v[184:187]
G v4,s[40:43],s64,3072
M v[204:207],a[120:123],v[64:67],v[204:207]
s_add_u32 s64,s64,36864
buffer_load_dwordx4 v[60:63],v0,s[36:39],s58 offen offset:1024 sc1
M v[224:227],a[124:127],v[64:67],v[224:227]
buffer_load_dwordx4 v[64:67],v0,s[36:39],s59 offen offset:1024 sc1
M v[168:171],a[112:115],v[68:71],v[168:171]
M v[188:191],a[116:119],v[68:71],v[188:191]
WL 1
M v[208:211],a[120:123],v[68:71],v[208:211]
M v[228:231],a[124:127],v[68:71],v[228:231]
buffer_load_dwordx4 v[68:71],v0,s[36:39],s60 offen offset:1024 sc1
M v[152:155],a[96:99],v[72:75],v[152:155]
WL 0
WV 39
M v[172:175],a[100:103],v[72:75],v[172:175]
s_add_u32 m0,s68,8192
T a[112:113],v9,12288
M v[192:195],a[104:107],v[72:75],v[192:195]
T a[114:115],v9,14336
M v[212:215],a[108:111],v[72:75],v[212:215]
G v1,s[40:43],s64,0
M v[156:159],a[96:99],v[76:79],v[156:159]
T a[116:117],v10,12288
M v[176:179],a[100:103],v[76:79],v[176:179]
buffer_load_dwordx4 v[72:75],v0,s[36:39],s56 offen offset:2048 sc1
M v[196:199],a[104:107],v[76:79],v[196:199]
T a[118:119],v10,14336
M v[216:219],a[108:111],v[76:79],v[216:219]
G v2,s[40:43],s64,1024
M v[160:163],a[96:99],v[80:83],v[160:163]
T a[120:121],v11,12288
M v[180:183],a[100:103],v[80:83],v[180:183]
T a[122:123],v11,14336
T a[124:125],v12,12288
M v[200:203],a[104:107],v[80:83],v[200:203]
G v3,s[40:43],s64,2048
M v[220:223],a[108:111],v[80:83],v[220:223]
buffer_load_dwordx4 v[76:79],v0,s[36:39],s57 offen offset:2048 sc1
M v[164:167],a[96:99],v[84:87],v[164:167]
T a[126:127],v12,14336
M v[184:187],a[100:103],v[84:87],v[184:187]
G v4,s[40:43],s64,3072
M v[204:207],a[104:107],v[84:87],v[204:207]
s_add_u32 s64,s64,36864
buffer_load_dwordx4 v[80:83],v0,s[36:39],s58 offen offset:2048 sc1
M v[224:227],a[108:111],v[84:87],v[224:227]
buffer_load_dwordx4 v[84:87],v0,s[36:39],s59 offen offset:2048 sc1
M v[168:171],a[96:99],v[88:91],v[168:171]
M v[188:191],a[100:103],v[88:91],v[188:191]
WL 1
M v[208:211],a[104:107],v[88:91],v[208:211]
M v[228:231],a[108:111],v[88:91],v[228:231]
buffer_load_dwordx4 v[88:91],v0,s[36:39],s60 offen offset:2048 sc1
M v[152:155],a[112:115],v[92:95],v[152:155]
WL 0
WV 39
M v[172:175],a[116:119],v[92:95],v[172:175]
s_add_u32 m0,s68,12288
T a[96:97],v9,16384
M v[192:195],a[120:123],v[92:95],v[192:195]
T a[98:99],v9,18432
M v[212:215],a[124:127],v[92:95],v[212:215]
G v1,s[40:43],s64,0
M v[156:159],a[112:115],v[96:99],v[156:159]
T a[100:101],v10,16384
M v[176:179],a[116:119],v[96:99],v[176:179]
buffer_load_dwordx4 v[92:95],v0,s[36:39],s56 offen offset:3072 sc1
M v[196:199],a[120:123],v[96:99],v[196:199]
s_add_u32 s56,s56,4096
T a[102:103],v10,18432
M v[216:219],a[124:127],v[96:99],v[216:219]
G v2,s[40:43],s64,1024
M v[160:163],a[112:115],v[100:103],v[160:163]
T a[104:105],v11,16384
M v[180:183],a[116:119],v[100:103],v[180:183]
T a[106:107],v11,18432
T a[108:109],v12,16384
M v[200:203],a[120:123],v[100:103],v[200:203]
G v3,s[40:43],s64,2048
M v[220:223],a[124:127],v[100:103],v[220:223]
buffer_load_dwordx4 v[96:99],v0,s[36:39],s57 offen offset:3072 sc1
M v[164:167],a[112:115],v[104:107],v[164:167]
s_add_u32 s57,s57,4096
T a[110:111],v12,18432
M v[184:187],a[116:119],v[104:107],v[184:187]
G v4,s[40:43],s64,3072
)"
R"(M v[204:207],a[120:123],v[104:107],v[204:207]
s_add_u32 s64,s64,36864
buffer_load_dwordx4 v[100:103],v0,s[36:39],s58 offen offset:3072 sc1
M v[224:227],a[124:127],v[104:107],v[224:227]
s_add_u32 s58,s58,4096
buffer_load_dwordx4 v[104:107],v0,s[36:39],s59 offen offset:3072 sc1
M v[168:171],a[112:115],v[108:111],v[168:171]
s_add_u32 s59,s59,4096
M v[188:191],a[116:119],v[108:111],v[188:191]
WL 1
M v[208:211],a[120:123],v[108:111],v[208:211]
M v[228:231],a[124:127],v[108:111],v[228:231]
buffer_load_dwordx4 v[108:111],v0,s[36:39],s60 offen offset:3072 sc1
M v[152:155],a[96:99],v[112:115],v[152:155]
WL 0
WV 39
M v[172:175],a[100:103],v[112:115],v[172:175]
s_add_u32 s60,s60,4096
s_add_u32 m0,s68,16384
T a[112:113],v9,20480
M v[192:195],a[104:107],v[112:115],v[192:195]
T a[114:115],v9,22528
M v[212:215],a[108:111],v[112:115],v[212:215]
G v1,s[40:43],s64,0
M v[156:159],a[96:99],v[116:119],v[156:159]
T a[116:117],v10,20480
M v[176:179],a[100:103],v[116:119],v[176:179]
buffer_load_dwordx4 v[112:115],v0,s[36:39],s56 offen offset:0 sc1
M v[196:199],a[104:107],v[116:119],v[196:199]
T a[118:119],v10,22528
M v[216:219],a[108:111],v[116:119],v[216:219]
G v2,s[40:43],s64,1024
M v[160:163],a[96:99],v[120:123],v[160:163]
T a[120:121],v11,20480
M v[180:183],a[100:103],v[120:123],v[180:183]
T a[122:123],v11,22528
T a[124:125],v12,20480
M v[200:203],a[104:107],v[120:123],v[200:203]
G v3,s[40:43],s64,2048
M v[220:223],a[108:111],v[120:123],v[220:223]
buffer_load_dwordx4 v[116:119],v0,s[36:39],s57 offen offset:0 sc1
M v[164:167],a[96:99],v[124:127],v[164:167]
T a[126:127],v12,22528
M v[184:187],a[100:103],v[124:127],v[184:187]
G v4,s[40:43],s64,3072
M v[204:207],a[104:107],v[124:127],v[204:207]
s_add_u32 s64,s64,36864
buffer_load_dwordx4 v[120:123],v0,s[36:39],s58 offen offset:0 sc1
M v[224:227],a[108:111],v[124:127],v[224:227]
buffer_load_dwordx4 v[124:127],v0,s[36:39],s59 offen offset:0 sc1
M v[168:171],a[96:99],v[128:131],v[168:171]
M v[188:191],a[100:103],v[128:131],v[188:191]
WL 1
M v[208:211],a[104:107],v[128:131],v[208:211]
M v[228:231],a[108:111],v[128:131],v[228:231]
buffer_load_dwordx4 v[128:131],v0,s[36:39],s60 offen offset:0 sc1
M v[152:155],a[112:115],v[132:135],v[152:155]
WL 0
WV 39
M v[172:175],a[116:119],v[132:135],v[172:175]
s_add_u32 m0,s68,20480
T a[96:97],v9,0
M v[192:195],a[120:123],v[132:135],v[192:195]
T a[98:99],v9,2048
M v[212:215],a[124:127],v[132:135],v[212:215]
G v1,s[40:43],s64,0
M v[156:159],a[112:115],v[136:139],v[156:159]
T a[100:101],v10,0
M v[176:179],a[116:119],v[136:139],v[176:179]
buffer_load_dwordx4 v[132:135],v0,s[36:39],s56 offen offset:1024 sc1
M v[196:199],a[120:123],v[136:139],v[196:199]
T a[102:103],v10,2048
M v[216:219],a[124:127],v[136:139],v[216:219]
G v2,s[40:43],s64,1024
M v[160:163],a[112:115],v[140:143],v[160:163]
T a[104:105],v11,0
M v[180:183],a[116:119],v[140:143],v[180:183]
T a[106:107],v11,2048
T a[108:109],v12,0
M v[200:203],a[120:123],v[140:143],v[200:203]
G v3,s[40:43],s64,2048
M v[220:223],a[124:127],v[140:143],v[220:223]
buffer_load_dwordx4 v[136:139],v0,s[36:39],s57 offen offset:1024 sc1
M v[164:167],a[112:115],v[144:147],v[164:167]
T a[110:111],v12,2048
M v[184:187],a[116:119],v[144:147],v[184:187]
G v4,s[40:43],s64,3072
M v[204:207],a[120:123],v[144:147],v[204:207]
s_add_u32 s64,s64,36864
buffer_load_dwordx4 v[140:143],v0,s[36:39],s58 offen offset:1024 sc1
M v[224:227],a[124:127],v[144:147],v[224:227]
buffer_load_dwordx4 v[144:147],v0,s[36:39],s59 offen offset:1024 sc1
M v[168:171],a[112:115],v[148:151],v[168:171]
M v[188:191],a[116:119],v[148:151],v[188:191]
WL 1
M v[208:211],a[120:123],v[148:151],v[208:211]
M v[228:231],a[124:127],v[148:151],v[228:231]
buffer_load_dwordx4 v[148:151],v0,s[36:39],s60 offen offset:1024 sc1
M v[152:155],a[96:99],v[32:35],v[152:155]
WL 0
WV 39
M v[172:175],a[100:103],v[32:35],v[172:175]
s_add_u32 m0,s68,0
T a[112:113],v9,4096
M v[192:195],a[104:107],v[32:35],v[192:195]
T a[114:115],v9,6144
M v[212:215],a[108:111],v[32:35],v[212:215]
G v1,s[40:43],s64,0
M v[156:159],a[96:99],v[36:39],v[156:159]
T a[116:117],v10,4096
M v[176:179],a[100:103],v[36:39],v[176:179]
buffer_load_dwordx4 v[32:35],v0,s[36:39],s56 offen offset:2048 sc1
M v[196:199],a[104:107],v[36:39],v[196:199]
T a[118:119],v10,6144
M v[216:219],a[108:111],v[36:39],v[216:219]
G v2,s[40:43],s64,1024
M v[160:163],a[96:99],v[40:43],v[160:163]
T a[120:121],v11,4096
M v[180:183],a[100:103],v[40:43],v[180:183]
T a[122:123],v11,6144
T a[124:125],v12,4096
M v[200:203],a[104:107],v[40:43],v[200:203]
G v3,s[40:43],s64,2048
M v[220:223],a[108:111],v[40:43],v[220:223]
buffer_load_dwordx4 v[36:39],v0,s[36:39],s57 offen offset:2048 sc1
M v[164:167],a[96:99],v[44:47],v[164:167]
T a[126:127],v12,6144
M v[184:187],a[100:103],v[44:47],v[184:187]
G v4,s[40:43],s64,3072
M v[204:207],a[104:107],v[44:47],v[204:207]
s_add_u32 s64,s64,36864
buffer_load_dwordx4 v[40:43],v0,s[36:39],s58 offen offset:2048 sc1
M v[224:227],a[108:111],v[44:47],v[224:227]
buffer_load_dwordx4 v[44:47],v0,s[36:39],s59 offen offset:2048 sc1
M v[168:171],a[96:99],v[48:51],v[168:171]
M v[188:191],a[100:103],v[48:51],v[188:191]
WL 1
M v[208:211],a[104:107],v[48:51],v[208:211]
M v[228:231],a[108:111],v[48:51],v[228:231]
buffer_load_dwordx4 v[48:51],v0,s[36:39],s60 offen offset:2048 sc1
M v[152:155],a[112:115],v[52:55],v[152:155]
WL 0
WV 39
M v[172:175],a[116:119],v[52:55],v[172:175]
s_add_u32 m0,s68,4096
T a[96:97],v9,8192
M v[192:195],a[120:123],v[52:55],v[192:195]
T a[98:99],v9,10240
M v[212:215],a[124:127],v[52:55],v[212:215]
G v1,s[40:43],s64,0
M v[156:159],a[112:115],v[56:59],v[156:159]
T a[100:101],v10,8192
M v[176:179],a[116:119],v[56:59],v[176:179]
buffer_load_dwordx4 v[52:55],v0,s[36:39],s56 offen offset:3072 sc1
M v[196:199],a[120:123],v[56:59],v[196:199]
s_add_u32 s56,s56,4096
T a[102:103],v10,10240
M v[216:219],a[124:127],v[56:59],v[216:219]
G v2,s[40:43],s64,1024
M v[160:163],a[112:115],v[60:63],v[160:163]
T a[104:105],v11,8192
M v[180:183],a[116:119],v[60:63],v[180:183]
T a[106:107],v11,10240
T a[108:109],v12,8192
M v[200:203],a[120:123],v[60:63],v[200:203]
G v3,s[40:43],s64,2048
M v[220:223],a[124:127],v[60:63],v[220:223]
buffer_load_dwordx4 v[56:59],v0,s[36:39],s57 offen offset:3072 sc1
M v[164:167],a[112:115],v[64:67],v[164:167]
s_add_u32 s57,s57,4096
T a[110:111],v12,10240
M v[184:187],a[116:119],v[64:67],v[184:187]
G v4,s[40:43],s64,3072
M v[204:207],a[120:123],v[64:67],v[204:207]
s_add_u32 s64,s64,36864
buffer_load_dwordx4 v[60:63],v0,s[36:39],s58 offen offset:3072 sc1
M v[224:227],a[124:127],v[64:67],v[224:227]
s_add_u32 s58,s58,4096
buffer_load_dwordx4 v[64:67],v0,s[36:39],s59 offen offset:3072 sc1
M v[168:171],a[112:115],v[68:71],v[168:171]
s_add_u32 s59,s59,4096
M v[188:191],a[116:119],v[68:71],v[188:191]
WL 1
M v[208:211],a[120:123],v[68:71],v[208:211]
M v[228:231],a[124:127],v[68:71],v[228:231]
buffer_load_dwordx4 v[68:71],v0,s[36:39],s60 offen offset:3072 sc1
M v[152:155],a[96:99],v[72:75],v[152:155]
WL 0
WV 39
M v[172:175],a[100:103],v[72:75],v[172:175]
s_add_u32 s60,s60,4096
s_add_u32 m0,s68,8192
T a[112:113],v9,12288
M v[192:195],a[104:107],v[72:75],v[192:195]
T a[114:115],v9,14336
M v[212:215],a[108:111],v[72:75],v[212:215]
G v1,s[40:43],s64,0
M v[156:159],a[96:99],v[76:79],v[156:159]
T a[116:117],v10,12288
M v[176:179],a[100:103],v[76:79],v[176:179]
buffer_load_dwordx4 v[72:75],v0,s[36:39],s56 offen offset:0 sc1
M v[196:199],a[104:107],v[76:79],v[196:199]
T a[118:119],v10,14336
M v[216:219],a[108:111],v[76:79],v[216:219]
G v2,s[40:43],s64,1024
M v[160:163],a[96:99],v[80:83],v[160:163]
T a[120:121],v11,12288
M v[180:183],a[100:103],v[80:83],v[180:183]
T a[122:123],v11,14336
T a[124:125],v12,12288
M v[200:203],a[104:107],v[80:83],v[200:203]
G v3,s[40:43],s64,2048
M v[220:223],a[108:111],v[80:83],v[220:223]
buffer_load_dwordx4 v[76:79],v0,s[36:39],s57 offen offset:0 sc1
M v[164:167],a[96:99],v[84:87],v[164:167]
T a[126:127],v12,14336
M v[184:187],a[100:103],v[84:87],v[184:187]
G v4,s[40:43],s64,3072
M v[204:207],a[104:107],v[84:87],v[204:207]
s_add_u32 s64,s64,36864
buffer_load_dwordx4 v[80:83],v0,s[36:39],s58 offen offset:0 sc1
M v[224:227],a[108:111],v[84:87],v[224:227]
buffer_load_dwordx4 v[84:87],v0,s[36:39],s59 offen offset:0 sc1
M v[168:171],a[96:99],v[88:91],v[168:171]
M v[188:191],a[100:103],v[88:91],v[188:191]
WL 1
M v[208:211],a[104:107],v[88:91],v[208:211]
M v[228:231],a[108:111],v[88:91],v[228:231]
buffer_load_dwordx4 v[88:91],v0,s[36:39],s60 offen offset:0 sc1
M v[152:155],a[112:115],v[92:95],v[152:155]
WL 0
WV 39
M v[172:175],a[116:119],v[92:95],v[172:175]
s_add_u32 m0,s68,12288
T a[96:97],v9,16384
M v[192:195],a[120:123],v[92:95],v[192:195]
T a[98:99],v9,18432
M v[212:215],a[124:127],v[92:95],v[212:215]
G v1,s[40:43],s64,0
M v[156:159],a[112:115],v[96:99],v[156:159]
T a[100:101],v10,16384
M v[176:179],a[116:119],v[96:99],v[176:179]
buffer_load_dwordx4 v[92:95],v0,s[36:39],s56 offen offset:1024 sc1
M v[196:199],a[120:123],v[96:99],v[196:199]
T a[102:103],v10,18432
M v[216:219],a[124:127],v[96:99],v[216:219]
G v2,s[40:43],s64,1024
M v[160:163],a[112:115],v[100:103],v[160:163]
T a[104:105],v11,16384
M v[180:183],a[116:119],v[100:103],v[180:183]
T a[106:107],v11,18432
T a[108:109],v12,16384
M v[200:203],a[120:123],v[100:103],v[200:203]
G v3,s[40:43],s64,2048
M v[220:223],a[124:127],v[100:103],v[220:223]
buffer_load_dwordx4 v[96:99],v0,s[36:39],s57 offen offset:1024 sc1
M v[164:167],a[112:115],v[104:107],v[164:167]
T a[110:111],v12,18432
M v[184:187],a[116:119],v[104:107],v[184:187]
G v4,s[40:43],s64,3072
M v[204:207],a[120:123],v[104:107],v[204:207]
s_add_u32 s64,s64,36864
buffer_load_dwordx4 v[100:103],v0,s[36:39],s58 offen offset:1024 sc1
M v[224:227],a[124:127],v[104:107],v[224:227]
buffer_load_dwordx4 v[104:107],v0,s[36:39],s59 offen offset:1024 sc1
M v[168:171],a[112:115],v[108:111],v[168:171]
M v[188:191],a[116:119],v[108:111],v[188:191]
WL 1
M v[208:211],a[120:123],v[108:111],v[208:211]
M v[228:231],a[124:127],v[108:111],v[228:231]
buffer_load_dwordx4 v[108:111],v0,s[36:39],s60 offen offset:1024 sc1
M v[152:155],a[96:99],v[112:115],v[152:155]
WL 0
WV 39
M v[172:175],a[100:103],v[112:115],v[172:175]
s_add_u32 m0,s68,16384
T a[112:113],v9,20480
M v[192:195],a[104:107],v[112:115],v[192:195]
T a[114:115],v9,22528
M v[212:215],a[108:111],v[112:115],v[212:215]
G v1,s[40:43],s64,0
M v[156:159],a[96:99],v[116:119],v[156:159]
T a[116:117],v10,20480
M v[176:179],a[100:103],v[116:119],v[176:179]
buffer_load_dwordx4 v[112:115],v0,s[36:39],s56 offen offset:2048 sc1
M v[196:199],a[104:107],v[116:119],v[196:199]
T a[118:119],v10,22528
M v[216:219],a[108:111],v[116:119],v[216:219]
G v2,s[40:43],s64,1024
M v[160:163],a[96:99],v[120:123],v[160:163]
T a[120:121],v11,20480
M v[180:183],a[100:103],v[120:123],v[180:183]
T a[122:123],v11,22528
T a[124:125],v12,20480
M v[200:203],a[104:107],v[120:123],v[200:203]
G v3,s[40:43],s64,2048
M v[220:223],a[108:111],v[120:123],v[220:223]
buffer_load_dwordx4 v[116:119],v0,s[36:39],s57 offen offset:2048 sc1
M v[164:167],a[96:99],v[124:127],v[164:167]
T a[126:127],v12,22528
M v[184:187],a[100:103],v[124:127],v[184:187]
G v4,s[40:43],s64,3072
M v[204:207],a[104:107],v[124:127],v[204:207]
s_add_u32 s64,s64,36864
buffer_load_dwordx4 v[120:123],v0,s[36:39],s58 offen offset:2048 sc1
M v[224:227],a[108:111],v[124:127],v[224:227]
buffer_load_dwordx4 v[124:127],v0,s[36:39],s59 offen offset:2048 sc1
M v[168:171],a[96:99],v[128:131],v[168:171]
M v[188:191],a[100:103],v[128:131],v[188:191]
WL 1
M v[208:211],a[104:107],v[128:131],v[208:211]
M v[228:231],a[108:111],v[128:131],v[228:231]
buffer_load_dwordx4 v[128:131],v0,s[36:39],s60 offen offset:2048 sc1
M v[152:155],a[112:115],v[132:135],v[152:155]
WL 0
WV 39
M v[172:175],a[116:119],v[132:135],v[172:175]
s_add_u32 m0,s68,20480
T a[96:97],v9,0
M v[192:195],a[120:123],v[132:135],v[192:195]
T a[98:99],v9,2048
M v[212:215],a[124:127],v[132:135],v[212:215]
G v1,s[40:43],s64,0
M v[156:159],a[112:115],v[136:139],v[156:159]
T a[100:101],v10,0
M v[176:179],a[116:119],v[136:139],v[176:179]
buffer_load_dwordx4 v[132:135],v0,s[36:39],s56 offen offset:3072 sc1
M v[196:199],a[120:123],v[136:139],v[196:199]
s_add_u32 s56,s56,4096
T a[102:103],v10,2048
M v[216:219],a[124:127],v[136:139],v[216:219]
G v2,s[40:43],s64,1024
M v[160:163],a[112:115],v[140:143],v[160:163]
T a[104:105],v11,0
M v[180:183],a[116:119],v[140:143],v[180:183]
T a[106:107],v11,2048
T a[108:109],v12,0
M v[200:203],a[120:123],v[140:143],v[200:203]
G v3,s[40:43],s64,2048
M v[220:223],a[124:127],v[140:143],v[220:223]
buffer_load_dwordx4 v[136:139],v0,s[36:39],s57 offen offset:3072 sc1
M v[164:167],a[112:115],v[144:147],v[164:167]
s_add_u32 s57,s57,4096
T a[110:111],v12,2048
M v[184:187],a[116:119],v[144:147],v[184:187]
G v4,s[40:43],s64,3072
M v[204:207],a[120:123],v[144:147],v[204:207]
s_add_u32 s64,s64,36864
buffer_load_dwordx4 v[140:143],v0,s[36:39],s58 offen offset:3072 sc1
M v[224:227],a[124:127],v[144:147],v[224:227]
s_add_u32 s58,s58,4096
buffer_load_dwordx4 v[144:147],v0,s[36:39],s59 offen offset:3072 sc1
M v[168:171],a[112:115],v[148:151],v[168:171]
s_add_u32 s59,s59,4096
M v[188:191],a[116:119],v[148:151],v[188:191]
WL 1
M v[208:211],a[120:123],v[148:151],v[208:211]
M v[228:231],a[124:127],v[148:151],v[228:231]
buffer_load_dwordx4 v[148:151],v0,s[36:39],s60 offen offset:3072 sc1
M v[152:155],a[96:99],v[32:35],v[152:155]
WL 0
WV 39
M v[172:175],a[100:103],v[32:35],v[172:175]
s_add_u32 s60,s60,4096
s_add_u32 m0,s68,0
T a[112:113],v9,4096
M v[192:195],a[104:107],v[32:35],v[192:195]
T a[114:115],v9,6144
M v[212:215],a[108:111],v[32:35],v[212:215]
G v1,s[40:43],s64,0
M v[156:159],a[96:99],v[36:39],v[156:159]
T a[116:117],v10,4096
M v[176:179],a[100:103],v[36:39],v[176:179]
buffer_load_dwordx4 v[32:35],v0,s[36:39],s56 offen offset:0 sc1
M v[196:199],a[104:107],v[36:39],v[196:199]
T a[118:119],v10,6144
M v[216:219],a[108:111],v[36:39],v[216:219]
G v2,s[40:43],s64,1024
M v[160:163],a[96:99],v[40:43],v[160:163]
T a[120:121],v11,4096
M v[180:183],a[100:103],v[40:43],v[180:183]
T a[122:123],v11,6144
T a[124:125],v12,4096
M v[200:203],a[104:107],v[40:43],v[200:203]
G v3,s[40:43],s64,2048
M v[220:223],a[108:111],v[40:43],v[220:223]
buffer_load_dwordx4 v[36:39],v0,s[36:39],s57 offen offset:0 sc1
M v[164:167],a[96:99],v[44:47],v[164:167]
T a[126:127],v12,6144
M v[184:187],a[100:103],v[44:47],v[184:187]
G v4,s[40:43],s64,3072
M v[204:207],a[104:107],v[44:47],v[204:207]
s_add_u32 s64,s64,36864
buffer_load_dwordx4 v[40:43],v0,s[36:39],s58 offen offset:0 sc1
M v[224:227],a[108:111],v[44:47],v[224:227]
buffer_load_dwordx4 v[44:47],v0,s[36:39],s59 offen offset:0 sc1
M v[168:171],a[96:99],v[48:51],v[168:171]
M v[188:191],a[100:103],v[48:51],v[188:191]
WL 1
M v[208:211],a[104:107],v[48:51],v[208:211]
)"
R"(M v[228:231],a[108:111],v[48:51],v[228:231]
buffer_load_dwordx4 v[48:51],v0,s[36:39],s60 offen offset:0 sc1
M v[152:155],a[112:115],v[52:55],v[152:155]
WL 0
WV 39
M v[172:175],a[116:119],v[52:55],v[172:175]
s_add_u32 m0,s68,4096
T a[96:97],v9,8192
M v[192:195],a[120:123],v[52:55],v[192:195]
T a[98:99],v9,10240
M v[212:215],a[124:127],v[52:55],v[212:215]
G v1,s[40:43],s64,0
M v[156:159],a[112:115],v[56:59],v[156:159]
T a[100:101],v10,8192
M v[176:179],a[116:119],v[56:59],v[176:179]
buffer_load_dwordx4 v[52:55],v0,s[36:39],s56 offen offset:1024 sc1
M v[196:199],a[120:123],v[56:59],v[196:199]
T a[102:103],v10,10240
M v[216:219],a[124:127],v[56:59],v[216:219]
G v2,s[40:43],s64,1024
M v[160:163],a[112:115],v[60:63],v[160:163]
T a[104:105],v11,8192
M v[180:183],a[116:119],v[60:63],v[180:183]
T a[106:107],v11,10240
T a[108:109],v12,8192
M v[200:203],a[120:123],v[60:63],v[200:203]
G v3,s[40:43],s64,2048
M v[220:223],a[124:127],v[60:63],v[220:223]
buffer_load_dwordx4 v[56:59],v0,s[36:39],s57 offen offset:1024 sc1
M v[164:167],a[112:115],v[64:67],v[164:167]
T a[110:111],v12,10240
M v[184:187],a[116:119],v[64:67],v[184:187]
G v4,s[40:43],s64,3072
M v[204:207],a[120:123],v[64:67],v[204:207]
s_add_u32 s64,s64,36864
buffer_load_dwordx4 v[60:63],v0,s[36:39],s58 offen offset:1024 sc1
M v[224:227],a[124:127],v[64:67],v[224:227]
buffer_load_dwordx4 v[64:67],v0,s[36:39],s59 offen offset:1024 sc1
M v[168:171],a[112:115],v[68:71],v[168:171]
M v[188:191],a[116:119],v[68:71],v[188:191]
WL 1
M v[208:211],a[120:123],v[68:71],v[208:211]
M v[228:231],a[124:127],v[68:71],v[228:231]
buffer_load_dwordx4 v[68:71],v0,s[36:39],s60 offen offset:1024 sc1
M v[152:155],a[96:99],v[72:75],v[152:155]
WL 0
WV 39
M v[172:175],a[100:103],v[72:75],v[172:175]
s_add_u32 m0,s68,8192
T a[112:113],v9,12288
M v[192:195],a[104:107],v[72:75],v[192:195]
T a[114:115],v9,14336
M v[212:215],a[108:111],v[72:75],v[212:215]
G v1,s[40:43],s64,0
M v[156:159],a[96:99],v[76:79],v[156:159]
T a[116:117],v10,12288
M v[176:179],a[100:103],v[76:79],v[176:179]
buffer_load_dwordx4 v[72:75],v0,s[36:39],s56 offen offset:2048 sc1
M v[196:199],a[104:107],v[76:79],v[196:199]
T a[118:119],v10,14336
M v[216:219],a[108:111],v[76:79],v[216:219]
G v2,s[40:43],s64,1024
M v[160:163],a[96:99],v[80:83],v[160:163]
T a[120:121],v11,12288
M v[180:183],a[100:103],v[80:83],v[180:183]
T a[122:123],v11,14336
T a[124:125],v12,12288
M v[200:203],a[104:107],v[80:83],v[200:203]
G v3,s[40:43],s64,2048
M v[220:223],a[108:111],v[80:83],v[220:223]
buffer_load_dwordx4 v[76:79],v0,s[36:39],s57 offen offset:2048 sc1
M v[164:167],a[96:99],v[84:87],v[164:167]
T a[126:127],v12,14336
M v[184:187],a[100:103],v[84:87],v[184:187]
G v4,s[40:43],s64,3072
M v[204:207],a[104:107],v[84:87],v[204:207]
s_add_u32 s64,s64,36864
buffer_load_dwordx4 v[80:83],v0,s[36:39],s58 offen offset:2048 sc1
M v[224:227],a[108:111],v[84:87],v[224:227]
buffer_load_dwordx4 v[84:87],v0,s[36:39],s59 offen offset:2048 sc1
M v[168:171],a[96:99],v[88:91],v[168:171]
M v[188:191],a[100:103],v[88:91],v[188:191]
WL 1
M v[208:211],a[104:107],v[88:91],v[208:211]
M v[228:231],a[108:111],v[88:91],v[228:231]
buffer_load_dwordx4 v[88:91],v0,s[36:39],s60 offen offset:2048 sc1
M v[152:155],a[112:115],v[92:95],v[152:155]
WL 0
WV 39
M v[172:175],a[116:119],v[92:95],v[172:175]
s_add_u32 m0,s68,12288
T a[96:97],v9,16384
M v[192:195],a[120:123],v[92:95],v[192:195]
T a[98:99],v9,18432
M v[212:215],a[124:127],v[92:95],v[212:215]
G v1,s[40:43],s64,0
M v[156:159],a[112:115],v[96:99],v[156:159]
T a[100:101],v10,16384
M v[176:179],a[116:119],v[96:99],v[176:179]
buffer_load_dwordx4 v[92:95],v0,s[36:39],s56 offen offset:3072 sc1
M v[196:199],a[120:123],v[96:99],v[196:199]
s_add_u32 s56,s56,4096
T a[102:103],v10,18432
M v[216:219],a[124:127],v[96:99],v[216:219]
G v2,s[40:43],s64,1024
M v[160:163],a[112:115],v[100:103],v[160:163]
T a[104:105],v11,16384
M v[180:183],a[116:119],v[100:103],v[180:183]
T a[106:107],v11,18432
T a[108:109],v12,16384
M v[200:203],a[120:123],v[100:103],v[200:203]
G v3,s[40:43],s64,2048
M v[220:223],a[124:127],v[100:103],v[220:223]
buffer_load_dwordx4 v[96:99],v0,s[36:39],s57 offen offset:3072 sc1
M v[164:167],a[112:115],v[104:107],v[164:167]
s_add_u32 s57,s57,4096
T a[110:111],v12,18432
M v[184:187],a[116:119],v[104:107],v[184:187]
G v4,s[40:43],s64,3072
M v[204:207],a[120:123],v[104:107],v[204:207]
s_add_u32 s64,s64,36864
buffer_load_dwordx4 v[100:103],v0,s[36:39],s58 offen offset:3072 sc1
M v[224:227],a[124:127],v[104:107],v[224:227]
s_add_u32 s58,s58,4096
buffer_load_dwordx4 v[104:107],v0,s[36:39],s59 offen offset:3072 sc1
M v[168:171],a[112:115],v[108:111],v[168:171]
s_add_u32 s59,s59,4096
M v[188:191],a[116:119],v[108:111],v[188:191]
WL 1
M v[208:211],a[120:123],v[108:111],v[208:211]
M v[228:231],a[124:127],v[108:111],v[228:231]
buffer_load_dwordx4 v[108:111],v0,s[36:39],s60 offen offset:3072 sc1
M v[152:155],a[96:99],v[112:115],v[152:155]
WL 0
WV 39
M v[172:175],a[100:103],v[112:115],v[172:175]
s_add_u32 s60,s60,4096
s_add_u32 m0,s68,16384
T a[112:113],v9,20480
M v[192:195],a[104:107],v[112:115],v[192:195]
T a[114:115],v9,22528
M v[212:215],a[108:111],v[112:115],v[212:215]
G v1,s[40:43],s64,0
M v[156:159],a[96:99],v[116:119],v[156:159]
T a[116:117],v10,20480
M v[176:179],a[100:103],v[116:119],v[176:179]
buffer_load_dwordx4 v[112:115],v0,s[36:39],s56 offen offset:0 sc1
M v[196:199],a[104:107],v[116:119],v[196:199]
T a[118:119],v10,22528
M v[216:219],a[108:111],v[116:119],v[216:219]
G v2,s[40:43],s64,1024
M v[160:163],a[96:99],v[120:123],v[160:163]
T a[120:121],v11,20480
M v[180:183],a[100:103],v[120:123],v[180:183]
T a[122:123],v11,22528
T a[124:125],v12,20480
M v[200:203],a[104:107],v[120:123],v[200:203]
G v3,s[40:43],s64,2048
M v[220:223],a[108:111],v[120:123],v[220:223]
buffer_load_dwordx4 v[116:119],v0,s[36:39],s57 offen offset:0 sc1
M v[164:167],a[96:99],v[124:127],v[164:167]
T a[126:127],v12,22528
M v[184:187],a[100:103],v[124:127],v[184:187]
G v4,s[40:43],s64,3072
M v[204:207],a[104:107],v[124:127],v[204:207]
s_add_u32 s64,s64,36864
buffer_load_dwordx4 v[120:123],v0,s[36:39],s58 offen offset:0 sc1
M v[224:227],a[108:111],v[124:127],v[224:227]
buffer_load_dwordx4 v[124:127],v0,s[36:39],s59 offen offset:0 sc1
M v[168:171],a[96:99],v[128:131],v[168:171]
M v[188:191],a[100:103],v[128:131],v[188:191]
WL 1
M v[208:211],a[104:107],v[128:131],v[208:211]
M v[228:231],a[108:111],v[128:131],v[228:231]
buffer_load_dwordx4 v[128:131],v0,s[36:39],s60 offen offset:0 sc1
M v[152:155],a[112:115],v[132:135],v[152:155]
WL 0
WV 39
M v[172:175],a[116:119],v[132:135],v[172:175]
s_add_u32 m0,s68,20480
T a[96:97],v9,0
M v[192:195],a[120:123],v[132:135],v[192:195]
T a[98:99],v9,2048
M v[212:215],a[124:127],v[132:135],v[212:215]
G v1,s[40:43],s64,0
M v[156:159],a[112:115],v[136:139],v[156:159]
T a[100:101],v10,0
M v[176:179],a[116:119],v[136:139],v[176:179]
buffer_load_dwordx4 v[132:135],v0,s[36:39],s56 offen offset:1024 sc1
M v[196:199],a[120:123],v[136:139],v[196:199]
T a[102:103],v10,2048
M v[216:219],a[124:127],v[136:139],v[216:219]
G v2,s[40:43],s64,1024
M v[160:163],a[112:115],v[140:143],v[160:163]
T a[104:105],v11,0
M v[180:183],a[116:119],v[140:143],v[180:183]
T a[106:107],v11,2048
T a[108:109],v12,0
M v[200:203],a[120:123],v[140:143],v[200:203]
G v3,s[40:43],s64,2048
M v[220:223],a[124:127],v[140:143],v[220:223]
buffer_load_dwordx4 v[136:139],v0,s[36:39],s57 offen offset:1024 sc1
M v[164:167],a[112:115],v[144:147],v[164:167]
T a[110:111],v12,2048
M v[184:187],a[116:119],v[144:147],v[184:187]
G v4,s[40:43],s64,3072
M v[204:207],a[120:123],v[144:147],v[204:207]
s_add_u32 s64,s64,36864
buffer_load_dwordx4 v[140:143],v0,s[36:39],s58 offen offset:1024 sc1
M v[224:227],a[124:127],v[144:147],v[224:227]
buffer_load_dwordx4 v[144:147],v0,s[36:39],s59 offen offset:1024 sc1
M v[168:171],a[112:115],v[148:151],v[168:171]
M v[188:191],a[116:119],v[148:151],v[188:191]
WL 1
M v[208:211],a[120:123],v[148:151],v[208:211]
M v[228:231],a[124:127],v[148:151],v[228:231]
buffer_load_dwordx4 v[148:151],v0,s[36:39],s60 offen offset:1024 sc1
M v[152:155],a[96:99],v[32:35],v[152:155]
WL 0
WV 39
M v[172:175],a[100:103],v[32:35],v[172:175]
s_add_u32 m0,s68,0
T a[112:113],v9,4096
M v[192:195],a[104:107],v[32:35],v[192:195]
T a[114:115],v9,6144
M v[212:215],a[108:111],v[32:35],v[212:215]
G v1,s[40:43],s64,0
M v[156:159],a[96:99],v[36:39],v[156:159]
T a[116:117],v10,4096
M v[176:179],a[100:103],v[36:39],v[176:179]
buffer_load_dwordx4 v[32:35],v0,s[36:39],s56 offen offset:2048 sc1
M v[196:199],a[104:107],v[36:39],v[196:199]
T a[118:119],v10,6144
M v[216:219],a[108:111],v[36:39],v[216:219]
G v2,s[40:43],s64,1024
M v[160:163],a[96:99],v[40:43],v[160:163]
T a[120:121],v11,4096
M v[180:183],a[100:103],v[40:43],v[180:183]
T a[122:123],v11,6144
T a[124:125],v12,4096
M v[200:203],a[104:107],v[40:43],v[200:203]
G v3,s[40:43],s64,2048
M v[220:223],a[108:111],v[40:43],v[220:223]
buffer_load_dwordx4 v[36:39],v0,s[36:39],s57 offen offset:2048 sc1
M v[164:167],a[96:99],v[44:47],v[164:167]
T a[126:127],v12,6144
M v[184:187],a[100:103],v[44:47],v[184:187]
G v4,s[40:43],s64,3072
M v[204:207],a[104:107],v[44:47],v[204:207]
s_add_u32 s64,s64,36864
buffer_load_dwordx4 v[40:43],v0,s[36:39],s58 offen offset:2048 sc1
M v[224:227],a[108:111],v[44:47],v[224:227]
buffer_load_dwordx4 v[44:47],v0,s[36:39],s59 offen offset:2048 sc1
M v[168:171],a[96:99],v[48:51],v[168:171]
M v[188:191],a[100:103],v[48:51],v[188:191]
WL 1
M v[208:211],a[104:107],v[48:51],v[208:211]
M v[228:231],a[108:111],v[48:51],v[228:231]
buffer_load_dwordx4 v[48:51],v0,s[36:39],s60 offen offset:2048 sc1
M v[152:155],a[112:115],v[52:55],v[152:155]
WL 0
WV 39
M v[172:175],a[116:119],v[52:55],v[172:175]
s_add_u32 m0,s68,4096
T a[96:97],v9,8192
M v[192:195],a[120:123],v[52:55],v[192:195]
T a[98:99],v9,10240
M v[212:215],a[124:127],v[52:55],v[212:215]
G v1,s[40:43],s64,0
M v[156:159],a[112:115],v[56:59],v[156:159]
T a[100:101],v10,8192
M v[176:179],a[116:119],v[56:59],v[176:179]
buffer_load_dwordx4 v[52:55],v0,s[36:39],s56 offen offset:3072 sc1
M v[196:199],a[120:123],v[56:59],v[196:199]
s_add_u32 s56,s56,4096
T a[102:103],v10,10240
M v[216:219],a[124:127],v[56:59],v[216:219]
G v2,s[40:43],s64,1024
M v[160:163],a[112:115],v[60:63],v[160:163]
T a[104:105],v11,8192
M v[180:183],a[116:119],v[60:63],v[180:183]
T a[106:107],v11,10240
T a[108:109],v12,8192
M v[200:203],a[120:123],v[60:63],v[200:203]
G v3,s[40:43],s64,2048
M v[220:223],a[124:127],v[60:63],v[220:223]
buffer_load_dwordx4 v[56:59],v0,s[36:39],s57 offen offset:3072 sc1
M v[164:167],a[112:115],v[64:67],v[164:167]
s_add_u32 s57,s57,4096
T a[110:111],v12,10240
M v[184:187],a[116:119],v[64:67],v[184:187]
G v4,s[40:43],s64,3072
M v[204:207],a[120:123],v[64:67],v[204:207]
s_add_u32 s64,s64,36864
buffer_load_dwordx4 v[60:63],v0,s[36:39],s58 offen offset:3072 sc1
M v[224:227],a[124:127],v[64:67],v[224:227]
s_add_u32 s58,s58,4096
buffer_load_dwordx4 v[64:67],v0,s[36:39],s59 offen offset:3072 sc1
M v[168:171],a[112:115],v[68:71],v[168:171]
s_add_u32 s59,s59,4096
M v[188:191],a[116:119],v[68:71],v[188:191]
WL 1
M v[208:211],a[120:123],v[68:71],v[208:211]
M v[228:231],a[124:127],v[68:71],v[228:231]
buffer_load_dwordx4 v[68:71],v0,s[36:39],s60 offen offset:3072 sc1
M v[152:155],a[96:99],v[72:75],v[152:155]
WL 0
WV 39
M v[172:175],a[100:103],v[72:75],v[172:175]
s_add_u32 s60,s60,4096
T a[112:113],v9,12288
M v[192:195],a[104:107],v[72:75],v[192:195]
T a[114:115],v9,14336
M v[212:215],a[108:111],v[72:75],v[212:215]
T a[116:117],v10,12288
M v[156:159],a[96:99],v[76:79],v[156:159]
T a[118:119],v10,14336
M v[176:179],a[100:103],v[76:79],v[176:179]
T a[120:121],v11,12288
M v[196:199],a[104:107],v[76:79],v[196:199]
T a[122:123],v11,14336
M v[216:219],a[108:111],v[76:79],v[216:219]
T a[124:125],v12,12288
M v[160:163],a[96:99],v[80:83],v[160:163]
T a[126:127],v12,14336
M v[180:183],a[100:103],v[80:83],v[180:183]
M v[200:203],a[104:107],v[80:83],v[200:203]
M v[220:223],a[108:111],v[80:83],v[220:223]
M v[164:167],a[96:99],v[84:87],v[164:167]
M v[184:187],a[100:103],v[84:87],v[184:187]
M v[204:207],a[104:107],v[84:87],v[204:207]
M v[224:227],a[108:111],v[84:87],v[224:227]
M v[168:171],a[96:99],v[88:91],v[168:171]
M v[188:191],a[100:103],v[88:91],v[188:191]
WL 0
M v[208:211],a[104:107],v[88:91],v[208:211]
M v[228:231],a[108:111],v[88:91],v[228:231]
WV 30
M v[152:155],a[112:115],v[92:95],v[152:155]
T a[96:97],v9,16384
M v[172:175],a[116:119],v[92:95],v[172:175]
T a[98:99],v9,18432
M v[192:195],a[120:123],v[92:95],v[192:195]
T a[100:101],v10,16384
M v[212:215],a[124:127],v[92:95],v[212:215]
T a[102:103],v10,18432
M v[156:159],a[112:115],v[96:99],v[156:159]
T a[104:105],v11,16384
M v[176:179],a[116:119],v[96:99],v[176:179]
T a[106:107],v11,18432
M v[196:199],a[120:123],v[96:99],v[196:199]
T a[108:109],v12,16384
M v[216:219],a[124:127],v[96:99],v[216:219]
T a[110:111],v12,18432
M v[160:163],a[112:115],v[100:103],v[160:163]
M v[180:183],a[116:119],v[100:103],v[180:183]
M v[200:203],a[120:123],v[100:103],v[200:203]
M v[220:223],a[124:127],v[100:103],v[220:223]
M v[164:167],a[112:115],v[104:107],v[164:167]
M v[184:187],a[116:119],v[104:107],v[184:187]
M v[204:207],a[120:123],v[104:107],v[204:207]
M v[224:227],a[124:127],v[104:107],v[224:227]
M v[168:171],a[112:115],v[108:111],v[168:171]
M v[188:191],a[116:119],v[108:111],v[188:191]
WL 0
M v[208:211],a[120:123],v[108:111],v[208:211]
M v[228:231],a[124:127],v[108:111],v[228:231]
WV 21
M v[152:155],a[96:99],v[112:115],v[152:155]
T a[112:113],v9,20480
M v[172:175],a[100:103],v[112:115],v[172:175]
T a[114:115],v9,22528
M v[192:195],a[104:107],v[112:115],v[192:195]
T a[116:117],v10,20480
M v[212:215],a[108:111],v[112:115],v[212:215]
T a[118:119],v10,22528
M v[156:159],a[96:99],v[116:119],v[156:159]
T a[120:121],v11,20480
M v[176:179],a[100:103],v[116:119],v[176:179]
T a[122:123],v11,22528
M v[196:199],a[104:107],v[116:119],v[196:199]
T a[124:125],v12,20480
M v[216:219],a[108:111],v[116:119],v[216:219]
T a[126:127],v12,22528
M v[160:163],a[96:99],v[120:123],v[160:163]
M v[180:183],a[100:103],v[120:123],v[180:183]
M v[200:203],a[104:107],v[120:123],v[200:203]
M v[220:223],a[108:111],v[120:123],v[220:223]
M v[164:167],a[96:99],v[124:127],v[164:167]
M v[184:187],a[100:103],v[124:127],v[184:187]
M v[204:207],a[104:107],v[124:127],v[204:207]
M v[224:227],a[108:111],v[124:127],v[224:227]
M v[168:171],a[96:99],v[128:131],v[168:171]
M v[188:191],a[100:103],v[128:131],v[188:191]
WL 0
M v[208:211],a[104:107],v[128:131],v[208:211]
M v[228:231],a[108:111],v[128:131],v[228:231]
WV 12
M v[152:155],a[112:115],v[132:135],v[152:155]
T a[96:97],v9,0
)"
R"(M v[172:175],a[116:119],v[132:135],v[172:175]
T a[98:99],v9,2048
M v[192:195],a[120:123],v[132:135],v[192:195]
T a[100:101],v10,0
M v[212:215],a[124:127],v[132:135],v[212:215]
T a[102:103],v10,2048
M v[156:159],a[112:115],v[136:139],v[156:159]
T a[104:105],v11,0
M v[176:179],a[116:119],v[136:139],v[176:179]
T a[106:107],v11,2048
M v[196:199],a[120:123],v[136:139],v[196:199]
T a[108:109],v12,0
M v[216:219],a[124:127],v[136:139],v[216:219]
T a[110:111],v12,2048
M v[160:163],a[112:115],v[140:143],v[160:163]
M v[180:183],a[116:119],v[140:143],v[180:183]
M v[200:203],a[120:123],v[140:143],v[200:203]
M v[220:223],a[124:127],v[140:143],v[220:223]
M v[164:167],a[112:115],v[144:147],v[164:167]
M v[184:187],a[116:119],v[144:147],v[184:187]
M v[204:207],a[120:123],v[144:147],v[204:207]
M v[224:227],a[124:127],v[144:147],v[224:227]
M v[168:171],a[112:115],v[148:151],v[168:171]
M v[188:191],a[116:119],v[148:151],v[188:191]
WL 0
M v[208:211],a[120:123],v[148:151],v[208:211]
M v[228:231],a[124:127],v[148:151],v[228:231]
WV 3
M v[152:155],a[96:99],v[32:35],v[152:155]
T a[112:113],v9,4096
M v[172:175],a[100:103],v[32:35],v[172:175]
T a[114:115],v9,6144
M v[192:195],a[104:107],v[32:35],v[192:195]
T a[116:117],v10,4096
M v[212:215],a[108:111],v[32:35],v[212:215]
T a[118:119],v10,6144
M v[156:159],a[96:99],v[36:39],v[156:159]
T a[120:121],v11,4096
M v[176:179],a[100:103],v[36:39],v[176:179]
T a[122:123],v11,6144
M v[196:199],a[104:107],v[36:39],v[196:199]
T a[124:125],v12,4096
M v[216:219],a[108:111],v[36:39],v[216:219]
T a[126:127],v12,6144
M v[160:163],a[96:99],v[40:43],v[160:163]
M v[180:183],a[100:103],v[40:43],v[180:183]
M v[200:203],a[104:107],v[40:43],v[200:203]
M v[220:223],a[108:111],v[40:43],v[220:223]
M v[164:167],a[96:99],v[44:47],v[164:167]
M v[184:187],a[100:103],v[44:47],v[184:187]
M v[204:207],a[104:107],v[44:47],v[204:207]
M v[224:227],a[108:111],v[44:47],v[224:227]
M v[168:171],a[96:99],v[48:51],v[168:171]
M v[188:191],a[100:103],v[48:51],v[188:191]
WL 0
M v[208:211],a[104:107],v[48:51],v[208:211]
M v[228:231],a[108:111],v[48:51],v[228:231]
M v[172:175],a[116:119],v[52:55],v[172:175]
M v[192:195],a[120:123],v[52:55],v[192:195]
M v[212:215],a[124:127],v[52:55],v[212:215]
M v[152:155],a[112:115],v[52:55],v[152:155]
M v[176:179],a[116:119],v[56:59],v[176:179]
M v[196:199],a[120:123],v[56:59],v[196:199]
M v[216:219],a[124:127],v[56:59],v[216:219]
M v[156:159],a[112:115],v[56:59],v[156:159]
WV 2
M v[180:183],a[116:119],v[60:63],v[180:183]
ds_write_b128 v15,v[172:175] offset:0
M v[200:203],a[120:123],v[60:63],v[200:203]
ds_write_b128 v16,v[192:195] offset:0
M v[220:223],a[124:127],v[60:63],v[220:223]
ds_write_b128 v17,v[212:215] offset:0
M v[160:163],a[112:115],v[60:63],v[160:163]
ds_write_b128 v15,v[176:179] offset:1024
WV 1
M v[184:187],a[116:119],v[64:67],v[184:187]
ds_write_b128 v16,v[196:199] offset:1024
M v[204:207],a[120:123],v[64:67],v[204:207]
ds_write_b128 v17,v[216:219] offset:1024
M v[224:227],a[124:127],v[64:67],v[224:227]
ds_write_b128 v15,v[180:183] offset:2048
M v[164:167],a[112:115],v[64:67],v[164:167]
ds_write_b128 v16,v[200:203] offset:2048
WV 0
M v[188:191],a[116:119],v[68:71],v[188:191]
ds_write_b128 v17,v[220:223] offset:2048
M v[208:211],a[120:123],v[68:71],v[208:211]
ds_write_b128 v15,v[184:187] offset:3072
M v[228:231],a[124:127],v[68:71],v[228:231]
ds_write_b128 v16,v[204:207] offset:3072
M v[168:171],a[112:115],v[68:71],v[168:171]
ds_write_b128 v17,v[224:227] offset:3072
s_nop 0
ds_write_b128 v15,v[188:191] offset:4096
s_nop 0
ds_write_b128 v16,v[208:211] offset:4096
s_nop 0
ds_write_b128 v17,v[228:231] offset:4096
WL 0
s_barrier
D v[32:35],v14,0
D v[36:39],v14,5120
D v[40:43],v14,10240
D v[44:47],v14,1024
D v[48:51],v14,6144
D v[52:55],v14,11264
D v[56:59],v14,2048
D v[60:63],v14,7168
D v[64:67],v14,12288
D v[68:71],v14,3072
D v[72:75],v14,8192
D v[76:79],v14,13312
D v[80:83],v14,4096
D v[84:87],v14,9216
D v[88:91],v14,14336
WL 14
v_pk_add_f32 v[152:153],v[152:153],v[32:33]
WL 12
v_pk_add_f32 v[36:37],v[36:37],v[40:41]
v_pk_add_f32 v[154:155],v[154:155],v[34:35]
v_pk_add_f32 v[38:39],v[38:39],v[42:43]
v_pk_add_f32 v[152:153],v[152:153],v[36:37]
v_pk_add_f32 v[154:155],v[154:155],v[38:39]
C v152,v152,v153
C v153,v154,v155
buffer_store_dwordx2 v[152:153],v13,s[52:55],s72 offen offset:0
s_add_u32 s72,s72,36864
WL 11
v_pk_add_f32 v[156:157],v[156:157],v[44:45]
WL 9
v_pk_add_f32 v[48:49],v[48:49],v[52:53]
v_pk_add_f32 v[158:159],v[158:159],v[46:47]
v_pk_add_f32 v[50:51],v[50:51],v[54:55]
v_pk_add_f32 v[156:157],v[156:157],v[48:49]
v_pk_add_f32 v[158:159],v[158:159],v[50:51]
C v156,v156,v157
C v157,v158,v159
buffer_store_dwordx2 v[156:157],v13,s[52:55],s72 offen offset:0
s_add_u32 s72,s72,36864
WL 8
v_pk_add_f32 v[160:161],v[160:161],v[56:57]
WL 6
v_pk_add_f32 v[60:61],v[60:61],v[64:65]
v_pk_add_f32 v[162:163],v[162:163],v[58:59]
v_pk_add_f32 v[62:63],v[62:63],v[66:67]
v_pk_add_f32 v[160:161],v[160:161],v[60:61]
v_pk_add_f32 v[162:163],v[162:163],v[62:63]
C v160,v160,v161
C v161,v162,v163
buffer_store_dwordx2 v[160:161],v13,s[52:55],s72 offen offset:0
s_add_u32 s72,s72,36864
WL 5
v_pk_add_f32 v[164:165],v[164:165],v[68:69]
WL 3
v_pk_add_f32 v[72:73],v[72:73],v[76:77]
v_pk_add_f32 v[166:167],v[166:167],v[70:71]
v_pk_add_f32 v[74:75],v[74:75],v[78:79]
v_pk_add_f32 v[164:165],v[164:165],v[72:73]
v_pk_add_f32 v[166:167],v[166:167],v[74:75]
C v164,v164,v165
C v165,v166,v167
buffer_store_dwordx2 v[164:165],v13,s[52:55],s72 offen offset:0
s_add_u32 s72,s72,36864
WL 2
v_pk_add_f32 v[168:169],v[168:169],v[80:81]
WL 0
v_pk_add_f32 v[84:85],v[84:85],v[88:89]
v_pk_add_f32 v[170:171],v[170:171],v[82:83]
v_pk_add_f32 v[86:87],v[86:87],v[90:91]
v_pk_add_f32 v[168:169],v[168:169],v[84:85]
v_pk_add_f32 v[170:171],v[170:171],v[86:87]
C v168,v168,v169
C v169,v170,v171
buffer_store_dwordx2 v[168:169],v13,s[52:55],s72 offen offset:0
s_branch f_fin
f_idle:
s_load_dwordx2 s[48:49],%0,72
s_mov_b32 s50,663552
s_mov_b32 s51,131072
s_and_b32 s21,%2,7
s_lshr_b32 s22,%2,3
s_lshl_b32 s21,s21,1
s_add_u32 s21,s21,s22
s_sub_u32 s21,s21,30
s_sub_u32 s23,s22,8
s_cmp_ge_u32 s22,30
s_cselect_b32 s70,s21,s23
v_lshrrev_b32 v18,4,v0
v_lshrrev_b32 v19,3,v18
v_and_b32 v20,7,v18
v_and_b32 v21,6,v19
v_xor_b32 v20,v20,v21
v_lshlrev_b32 v20,4,v20
v_lshrrev_b32 v21,2,v19
v_mul_u32_u24 v21,72,v21
v_bfe_u32 v22,v19,1,1
v_mul_u32_u24 v22,18,v22
v_add_u32 v21,v21,v22
v_and_b32 v22,1,v19
v_add_u32 v21,v21,v22
v_mul_u32_u24 v21,1152,v21
v_add_u32 v21,v21,v20
v_add_u32 v5,18432,v21
v_add_u32 v6,183296,v21
v_add_u32 v7,57856,v21
v_add_u32 v8,222720,v21
s_and_b32 s21,s20,1
s_lshr_b32 s22,s20,1
s_mul_i32 s23,s21,165888
s_mul_i32 s24,s22,41472
s_add_u32 s23,s23,s24
s_add_u32 s23,s23,19456
v_add_u32 v15,s23,v21
s_mul_i32 s23,s20,9216
s_mul_i32 s24,s22,2304
s_add_u32 s23,s23,s24
s_add_u32 s23,s23,1024
v_add_u32 v14,s23,v1
v_lshrrev_b32 v21,2,v18
v_lshlrev_b32 v22,7,v21
v_and_b32 v21,6,v21
v_and_b32 v23,3,v18
v_xor_b32 v21,v21,v23
v_lshl_add_u32 v22,v21,4,v22
s_lshl_b32 s23,s22,6
v_xor_b32 v22,s23,v22
s_mul_i32 s23,s22,16384
v_xor_b32 v9,0,v22
v_add_u32 v9,s23,v9
s_mul_i32 s23,s22,20480
v_xor_b32 v10,64,v22
v_add_u32 v10,s23,v10
v_and_b32 v21,15,v18
v_mul_u32_u24 v13,2304,v21
v_lshrrev_b32 v21,4,v18
v_lshl_add_u32 v13,v21,4,v13
s_mul_i32 s23,s70,6
s_mul_i32 s24,s21,3
s_add_u32 s23,s23,s24
s_add_u32 s24,s23,0
s_mul_i32 s56,s24,18432
s_add_u32 s24,s23,1
s_mul_i32 s57,s24,18432
s_add_u32 s24,s23,2
s_mul_i32 s58,s24,18432
s_mul_i32 s72,s23,36864
s_mul_i32 s24,s22,576
s_add_u32 s72,s72,s24
s_add_u32 s72,s72,1152
s_mul_i32 s23,s20,28416
v_and_b32 v21,15,v18
v_mul_u32_u24 v194,592,v21
v_lshrrev_b32 v21,4,v18
v_lshl_add_u32 v194,v21,4,v194
v_add_u32 v194,s23,v194
v_add_u32 v21,0,v18
v_mul_u32_u24 v22,1821,v21
v_lshrrev_b32 v22,16,v22
v_lshlrev_b32 v21,4,v21
v_mul_u32_u24 v23,1728,v22
v_add_u32 v185,v21,v23
v_lshl_add_u32 v176,v22,4,v21
v_add_u32 v176,s23,v176
v_add_u32 v21,64,v18
v_mul_u32_u24 v22,1821,v21
v_lshrrev_b32 v22,16,v22
v_lshlrev_b32 v21,4,v21
v_mul_u32_u24 v23,1728,v22
v_add_u32 v186,v21,v23
v_lshl_add_u32 v177,v22,4,v21
v_add_u32 v177,s23,v177
v_add_u32 v21,128,v18
v_mul_u32_u24 v22,1821,v21
v_lshrrev_b32 v22,16,v22
v_lshlrev_b32 v21,4,v21
v_mul_u32_u24 v23,1728,v22
v_add_u32 v187,v21,v23
v_lshl_add_u32 v178,v22,4,v21
v_add_u32 v178,s23,v178
v_add_u32 v21,192,v18
v_mul_u32_u24 v22,1821,v21
v_lshrrev_b32 v22,16,v22
v_lshlrev_b32 v21,4,v21
v_mul_u32_u24 v23,1728,v22
v_add_u32 v188,v21,v23
v_lshl_add_u32 v179,v22,4,v21
v_add_u32 v179,s23,v179
v_add_u32 v21,256,v18
v_mul_u32_u24 v22,1821,v21
v_lshrrev_b32 v22,16,v22
v_lshlrev_b32 v21,4,v21
v_mul_u32_u24 v23,1728,v22
v_add_u32 v189,v21,v23
v_lshl_add_u32 v180,v22,4,v21
v_add_u32 v180,s23,v180
v_add_u32 v21,320,v18
v_mul_u32_u24 v22,1821,v21
v_lshrrev_b32 v22,16,v22
v_lshlrev_b32 v21,4,v21
v_mul_u32_u24 v23,1728,v22
v_add_u32 v190,v21,v23
v_lshl_add_u32 v181,v22,4,v21
v_add_u32 v181,s23,v181
v_add_u32 v21,384,v18
v_mul_u32_u24 v22,1821,v21
v_lshrrev_b32 v22,16,v22
v_lshlrev_b32 v21,4,v21
v_mul_u32_u24 v23,1728,v22
v_add_u32 v191,v21,v23
v_lshl_add_u32 v182,v22,4,v21
v_add_u32 v182,s23,v182
v_add_u32 v21,448,v18
v_mul_u32_u24 v22,1821,v21
v_lshrrev_b32 v22,16,v22
v_lshlrev_b32 v21,4,v21
v_mul_u32_u24 v23,1728,v22
v_add_u32 v192,v21,v23
v_lshl_add_u32 v183,v22,4,v21
v_add_u32 v183,s23,v183
v_add_u32 v21,512,v18
v_mul_u32_u24 v22,1821,v21
v_lshrrev_b32 v22,16,v22
v_lshlrev_b32 v21,4,v21
v_mul_u32_u24 v23,1728,v22
v_add_u32 v193,v21,v23
v_lshl_add_u32 v184,v22,4,v21
v_add_u32 v184,s23,v184
s_lshl_b32 s68,s20,12
s_lshl_b32 s69,s20,10
s_add_u32 s69,s69,32768
s_lshl_b32 s71,s20,7
WL 0
s_and_b32 s49,s49,65535
WV 0
s_barrier
s_cmp_lg_u32 s20,0
s_cbranch_scc1 f_pb5
s_lshl_b32 s92,%2,6
s_add_u32 s92,s92,49152
v_mov_b32 v30,s92
v_mov_b32 v26,s96
v_mov_b32 v27,s97
s_mov_b64 exec,1
global_store_dwordx2 v30,v[26:27],s[94:95] sc1
s_mov_b64 exec,-1
f_pb5:
s_add_u32 m0,s68,0
s_add_u32 s66,s71,0
G v1,s[48:51],s66,0
G v2,s[48:51],s66,1024
s_add_u32 s67,s66,2304
G v3,s[48:51],s67,2048
G v4,s[48:51],s67,3072
s_add_u32 m0,s68,16384
s_add_u32 s66,s71,512
G v1,s[48:51],s66,0
G v2,s[48:51],s66,1024
s_add_u32 s67,s66,2304
G v3,s[48:51],s67,2048
G v4,s[48:51],s67,3072
s_add_u32 m0,s69,0
s_mov_b32 s66,0
G v14,s[48:51],s66,0
s_add_u32 m0,s68,36864
s_add_u32 s66,s71,41472
G v1,s[48:51],s66,0
G v2,s[48:51],s66,1024
s_add_u32 s67,s66,2304
G v3,s[48:51],s67,2048
G v4,s[48:51],s67,3072
s_add_u32 m0,s68,53248
s_add_u32 s66,s71,41984
G v1,s[48:51],s66,0
G v2,s[48:51],s66,1024
s_add_u32 s67,s66,2304
G v3,s[48:51],s67,2048
G v4,s[48:51],s67,3072
s_add_u32 m0,s69,36864
s_mov_b32 s66,41472
G v14,s[48:51],s66,0
s_add_u32 m0,s68,73728
s_add_u32 s66,s71,82944
G v1,s[48:51],s66,0
G v2,s[48:51],s66,1024
s_add_u32 s67,s66,2304
G v3,s[48:51],s67,2048
G v4,s[48:51],s67,3072
s_add_u32 m0,s68,90112
s_add_u32 s66,s71,83456
G v1,s[48:51],s66,0
G v2,s[48:51],s66,1024
s_add_u32 s67,s66,2304
G v3,s[48:51],s67,2048
G v4,s[48:51],s67,3072
s_add_u32 m0,s69,73728
s_mov_b32 s66,82944
G v14,s[48:51],s66,0
s_add_u32 m0,s68,110592
s_add_u32 s66,s71,124416
G v1,s[48:51],s66,0
)"
R"(G v2,s[48:51],s66,1024
s_add_u32 s67,s66,2304
G v3,s[48:51],s67,2048
G v4,s[48:51],s67,3072
s_add_u32 m0,s68,126976
s_add_u32 s66,s71,124928
G v1,s[48:51],s66,0
G v2,s[48:51],s66,1024
s_add_u32 s67,s66,2304
G v3,s[48:51],s67,2048
G v4,s[48:51],s67,3072
s_add_u32 m0,s69,110592
s_mov_b32 s66,124416
G v14,s[48:51],s66,0
s_mul_i32 s21,s70,96
s_add_u32 s22,s21,95
s_min_u32 s22,s22,2047
v_mov_b32 v18,s21
v_mov_b32 v19,s22
v_cmp_gt_u32 vcc,512,v0
s_nop 1
v_cndmask_b32 v18,v19,v18,vcc
v_bfe_u32 v19,v0,4,5
v_lshrrev_b32 v20,2,v19
v_and_b32 v19,3,v19
v_lshl_add_u32 v19,v19,6,v20
v_lshrrev_b32 v20,8,v18
v_lshl_add_u32 v20,v20,3,v19
v_lshlrev_b32 v20,6,v20
v_add_u32 v29,49152,v20
global_load_dwordx2 v[26:27],v29,s[94:95] sc1
WV 0
v_cmp_ne_u32 vcc,s96,v26
v_cmp_ne_u32 s[92:93],s97,v27
s_nop 1
s_or_b64 vcc,vcc,s[92:93]
s_cbranch_vccz f_c2_ok
s_mov_b32 s98,200000
f_c2_spin:
s_sleep 2
global_load_dwordx2 v[26:27],v29,s[94:95] sc1
WV 0
v_cmp_ne_u32 vcc,s96,v26
v_cmp_ne_u32 s[92:93],s97,v27
s_nop 1
s_or_b64 vcc,vcc,s[92:93]
s_cbranch_vccz f_c2_ok
s_sub_u32 s98,s98,1
s_cbranch_scc0 f_c2_spin
f_c2_ok:
buffer_load_dwordx4 v[32:35],v0,s[44:47],s56 offen offset:0 sc1
buffer_load_dwordx4 v[36:39],v0,s[44:47],s57 offen offset:0 sc1
buffer_load_dwordx4 v[40:43],v0,s[44:47],s58 offen offset:0 sc1
buffer_load_dwordx4 v[44:47],v0,s[44:47],s56 offen offset:1024 sc1
buffer_load_dwordx4 v[48:51],v0,s[44:47],s57 offen offset:1024 sc1
buffer_load_dwordx4 v[52:55],v0,s[44:47],s58 offen offset:1024 sc1
buffer_load_dwordx4 v[56:59],v0,s[44:47],s56 offen offset:2048 sc1
buffer_load_dwordx4 v[60:63],v0,s[44:47],s57 offen offset:2048 sc1
buffer_load_dwordx4 v[64:67],v0,s[44:47],s58 offen offset:2048 sc1
buffer_load_dwordx4 v[68:71],v0,s[44:47],s56 offen offset:3072 sc1
s_add_u32 s56,s56,4096
buffer_load_dwordx4 v[72:75],v0,s[44:47],s57 offen offset:3072 sc1
s_add_u32 s57,s57,4096
buffer_load_dwordx4 v[76:79],v0,s[44:47],s58 offen offset:3072 sc1
s_add_u32 s58,s58,4096
buffer_load_dwordx4 v[80:83],v0,s[44:47],s56 offen offset:0 sc1
buffer_load_dwordx4 v[84:87],v0,s[44:47],s57 offen offset:0 sc1
buffer_load_dwordx4 v[88:91],v0,s[44:47],s58 offen offset:0 sc1
buffer_load_dwordx4 v[92:95],v0,s[44:47],s56 offen offset:1024 sc1
buffer_load_dwordx4 v[96:99],v0,s[44:47],s57 offen offset:1024 sc1
buffer_load_dwordx4 v[100:103],v0,s[44:47],s58 offen offset:1024 sc1
s_barrier
T v[104:105],v9,0
T v[106:107],v9,2048
T v[108:109],v9,8
T v[110:111],v9,2056
T v[112:113],v10,0
T v[114:115],v10,2048
T v[116:117],v10,8
T v[118:119],v10,2056
T v[120:121],v9,4096
T v[122:123],v9,6144
T v[124:125],v9,4104
T v[126:127],v9,6152
T v[128:129],v10,4096
T v[130:131],v10,6144
T v[132:133],v10,4104
T v[134:135],v10,6152
T v[136:137],v9,8192
T v[138:139],v9,10240
T v[140:141],v9,8200
T v[142:143],v9,10248
T v[144:145],v10,8192
T v[146:147],v10,10240
T v[148:149],v10,8200
T v[150:151],v10,10248
T v[152:153],v9,12288
T v[154:155],v9,14336
T v[156:157],v9,12296
T v[158:159],v9,14344
T v[160:161],v10,12288
T v[162:163],v10,14336
T v[164:165],v10,12296
T v[166:167],v10,14344
T v[168:169],v9,16384
T v[170:171],v9,18432
T v[172:173],v9,16392
T v[174:175],v9,18440
WL 15
WV 17
M a[0:3],v[104:107],v[32:35],0
v_add_u32 v9,36864,v9
WV 16
M a[4:7],v[104:107],v[36:39],0
v_add_u32 v10,36864,v10
WV 15
M a[8:11],v[104:107],v[40:43],0
M a[12:15],v[108:111],v[32:35],0
M a[16:19],v[108:111],v[36:39],0
M a[20:23],v[108:111],v[40:43],0
WL 0
s_barrier
M a[24:27],v[112:115],v[32:35],0
s_add_u32 m0,s68,0
T v[104:105],v9,0
M a[28:31],v[112:115],v[36:39],0
s_add_u32 s66,s71,165888
T v[106:107],v9,2048
M a[32:35],v[112:115],v[40:43],0
T v[108:109],v9,8
M a[36:39],v[116:119],v[32:35],0
T v[110:111],v9,2056
M a[40:43],v[116:119],v[36:39],0
G v1,s[48:51],s66,0
M a[44:47],v[116:119],v[40:43],0
T v[112:113],v10,0
M a[48:51],v[120:123],v[32:35],0
T v[114:115],v10,2048
M a[52:55],v[120:123],v[36:39],0
G v2,s[48:51],s66,1024
M a[56:59],v[120:123],v[40:43],0
s_add_u32 s67,s66,2304
T v[116:117],v10,8
M a[60:63],v[124:127],v[32:35],0
T v[118:119],v10,2056
M a[64:67],v[124:127],v[36:39],0
T v[120:121],v9,4096
M a[68:71],v[124:127],v[40:43],0
T v[122:123],v9,6144
M a[72:75],v[128:131],v[32:35],0
G v3,s[48:51],s67,2048
M a[76:79],v[128:131],v[36:39],0
T v[124:125],v9,4104
M a[80:83],v[128:131],v[40:43],0
T v[126:127],v9,6152
M a[84:87],v[132:135],v[32:35],0
G v4,s[48:51],s67,3072
M a[88:91],v[132:135],v[36:39],0
s_add_u32 m0,s68,16384
T v[128:129],v10,4096
M a[92:95],v[132:135],v[40:43],0
s_add_u32 s66,s71,166400
T v[130:131],v10,6144
M a[96:99],v[136:139],v[32:35],0
T v[132:133],v10,4104
M a[100:103],v[136:139],v[36:39],0
T v[134:135],v10,6152
M a[104:107],v[136:139],v[40:43],0
T v[136:137],v9,8192
M a[108:111],v[140:143],v[32:35],0
T v[138:139],v9,10240
M a[112:115],v[140:143],v[36:39],0
G v1,s[48:51],s66,0
M a[116:119],v[140:143],v[40:43],0
T v[140:141],v9,8200
M a[120:123],v[144:147],v[32:35],0
T v[142:143],v9,10248
M a[124:127],v[144:147],v[36:39],0
G v2,s[48:51],s66,1024
M a[128:131],v[144:147],v[40:43],0
s_add_u32 s67,s66,2304
T v[144:145],v10,8192
M a[132:135],v[148:151],v[32:35],0
T v[146:147],v10,10240
M a[136:139],v[148:151],v[36:39],0
G v3,s[48:51],s67,2048
M a[140:143],v[148:151],v[40:43],0
T v[148:149],v10,8200
M a[144:147],v[152:155],v[32:35],0
T v[150:151],v10,10248
M a[148:151],v[152:155],v[36:39],0
G v4,s[48:51],s67,3072
M a[152:155],v[152:155],v[40:43],0
s_add_u32 m0,s69,0
T v[152:153],v9,12288
M a[156:159],v[156:159],v[32:35],0
T v[154:155],v9,14336
M a[160:163],v[156:159],v[36:39],0
s_mov_b32 s66,165888
M a[164:167],v[156:159],v[40:43],0
T v[156:157],v9,12296
M a[168:171],v[160:163],v[32:35],0
T v[158:159],v9,14344
M a[172:175],v[160:163],v[36:39],0
G v14,s[48:51],s66,0
M a[176:179],v[160:163],v[40:43],0
T v[160:161],v10,12288
M a[180:183],v[164:167],v[32:35],0
T v[162:163],v10,14336
M a[184:187],v[164:167],v[36:39],0
M a[188:191],v[164:167],v[40:43],0
T v[164:165],v10,12296
M a[192:195],v[168:171],v[32:35],0
T v[166:167],v10,14344
M a[196:199],v[168:171],v[36:39],0
M a[200:203],v[168:171],v[40:43],0
T v[168:169],v9,16384
M a[204:207],v[172:175],v[32:35],0
WL 5
T v[170:171],v9,18432
M a[208:211],v[172:175],v[36:39],0
buffer_load_dwordx4 v[32:35],v0,s[44:47],s56 offen offset:2048 sc1
M a[212:215],v[172:175],v[40:43],0
buffer_load_dwordx4 v[36:39],v0,s[44:47],s57 offen offset:2048 sc1
WV 25
M a[0:3],v[104:107],v[44:47],a[0:3]
T v[172:173],v9,16392
WV 24
M a[4:7],v[104:107],v[48:51],a[4:7]
T v[174:175],v9,18440
v_add_u32 v9,36864,v9
WV 23
M a[8:11],v[104:107],v[52:55],a[8:11]
buffer_load_dwordx4 v[40:43],v0,s[44:47],s58 offen offset:2048 sc1
M a[12:15],v[108:111],v[44:47],a[12:15]
v_add_u32 v10,36864,v10
M a[16:19],v[108:111],v[48:51],a[16:19]
M a[20:23],v[108:111],v[52:55],a[20:23]
WL 0
s_barrier
M a[24:27],v[112:115],v[44:47],a[24:27]
s_add_u32 m0,s68,36864
T v[104:105],v9,0
M a[28:31],v[112:115],v[48:51],a[28:31]
s_add_u32 s66,s71,207360
T v[106:107],v9,2048
M a[32:35],v[112:115],v[52:55],a[32:35]
T v[108:109],v9,8
M a[36:39],v[116:119],v[44:47],a[36:39]
T v[110:111],v9,2056
M a[40:43],v[116:119],v[48:51],a[40:43]
G v1,s[48:51],s66,0
M a[44:47],v[116:119],v[52:55],a[44:47]
T v[112:113],v10,0
M a[48:51],v[120:123],v[44:47],a[48:51]
T v[114:115],v10,2048
M a[52:55],v[120:123],v[48:51],a[52:55]
G v2,s[48:51],s66,1024
M a[56:59],v[120:123],v[52:55],a[56:59]
s_add_u32 s67,s66,2304
T v[116:117],v10,8
M a[60:63],v[124:127],v[44:47],a[60:63]
T v[118:119],v10,2056
M a[64:67],v[124:127],v[48:51],a[64:67]
T v[120:121],v9,4096
M a[68:71],v[124:127],v[52:55],a[68:71]
T v[122:123],v9,6144
M a[72:75],v[128:131],v[44:47],a[72:75]
G v3,s[48:51],s67,2048
M a[76:79],v[128:131],v[48:51],a[76:79]
T v[124:125],v9,4104
M a[80:83],v[128:131],v[52:55],a[80:83]
T v[126:127],v9,6152
M a[84:87],v[132:135],v[44:47],a[84:87]
G v4,s[48:51],s67,3072
M a[88:91],v[132:135],v[48:51],a[88:91]
s_add_u32 m0,s68,53248
T v[128:129],v10,4096
M a[92:95],v[132:135],v[52:55],a[92:95]
s_add_u32 s66,s71,207872
T v[130:131],v10,6144
M a[96:99],v[136:139],v[44:47],a[96:99]
T v[132:133],v10,4104
M a[100:103],v[136:139],v[48:51],a[100:103]
T v[134:135],v10,6152
M a[104:107],v[136:139],v[52:55],a[104:107]
T v[136:137],v9,8192
M a[108:111],v[140:143],v[44:47],a[108:111]
T v[138:139],v9,10240
M a[112:115],v[140:143],v[48:51],a[112:115]
G v1,s[48:51],s66,0
M a[116:119],v[140:143],v[52:55],a[116:119]
T v[140:141],v9,8200
M a[120:123],v[144:147],v[44:47],a[120:123]
T v[142:143],v9,10248
M a[124:127],v[144:147],v[48:51],a[124:127]
G v2,s[48:51],s66,1024
M a[128:131],v[144:147],v[52:55],a[128:131]
s_add_u32 s67,s66,2304
T v[144:145],v10,8192
M a[132:135],v[148:151],v[44:47],a[132:135]
T v[146:147],v10,10240
M a[136:139],v[148:151],v[48:51],a[136:139]
G v3,s[48:51],s67,2048
M a[140:143],v[148:151],v[52:55],a[140:143]
T v[148:149],v10,8200
M a[144:147],v[152:155],v[44:47],a[144:147]
T v[150:151],v10,10248
M a[148:151],v[152:155],v[48:51],a[148:151]
G v4,s[48:51],s67,3072
M a[152:155],v[152:155],v[52:55],a[152:155]
s_add_u32 m0,s69,36864
T v[152:153],v9,12288
M a[156:159],v[156:159],v[44:47],a[156:159]
T v[154:155],v9,14336
M a[160:163],v[156:159],v[48:51],a[160:163]
s_mov_b32 s66,207360
M a[164:167],v[156:159],v[52:55],a[164:167]
T v[156:157],v9,12296
M a[168:171],v[160:163],v[44:47],a[168:171]
T v[158:159],v9,14344
M a[172:175],v[160:163],v[48:51],a[172:175]
G v14,s[48:51],s66,0
M a[176:179],v[160:163],v[52:55],a[176:179]
T v[160:161],v10,12288
M a[180:183],v[164:167],v[44:47],a[180:183]
T v[162:163],v10,14336
M a[184:187],v[164:167],v[48:51],a[184:187]
M a[188:191],v[164:167],v[52:55],a[188:191]
T v[164:165],v10,12296
M a[192:195],v[168:171],v[44:47],a[192:195]
T v[166:167],v10,14344
M a[196:199],v[168:171],v[48:51],a[196:199]
M a[200:203],v[168:171],v[52:55],a[200:203]
T v[168:169],v9,16384
M a[204:207],v[172:175],v[44:47],a[204:207]
WL 5
T v[170:171],v9,18432
M a[208:211],v[172:175],v[48:51],a[208:211]
buffer_load_dwordx4 v[44:47],v0,s[44:47],s56 offen offset:3072 sc1
M a[212:215],v[172:175],v[52:55],a[212:215]
s_add_u32 s56,s56,4096
buffer_load_dwordx4 v[48:51],v0,s[44:47],s57 offen offset:3072 sc1
WV 34
M a[0:3],v[104:107],v[56:59],a[0:3]
s_add_u32 s57,s57,4096
T v[172:173],v9,16392
WV 33
M a[4:7],v[104:107],v[60:63],a[4:7]
T v[174:175],v9,18440
v_add_u32 v9,36864,v9
WV 32
M a[8:11],v[104:107],v[64:67],a[8:11]
buffer_load_dwordx4 v[52:55],v0,s[44:47],s58 offen offset:3072 sc1
M a[12:15],v[108:111],v[56:59],a[12:15]
s_add_u32 s58,s58,4096
v_add_u32 v10,36864,v10
M a[16:19],v[108:111],v[60:63],a[16:19]
M a[20:23],v[108:111],v[64:67],a[20:23]
WL 0
s_barrier
M a[24:27],v[112:115],v[56:59],a[24:27]
s_add_u32 m0,s68,73728
T v[104:105],v9,0
M a[28:31],v[112:115],v[60:63],a[28:31]
s_add_u32 s66,s71,248832
T v[106:107],v9,2048
M a[32:35],v[112:115],v[64:67],a[32:35]
T v[108:109],v9,8
M a[36:39],v[116:119],v[56:59],a[36:39]
T v[110:111],v9,2056
M a[40:43],v[116:119],v[60:63],a[40:43]
G v1,s[48:51],s66,0
M a[44:47],v[116:119],v[64:67],a[44:47]
T v[112:113],v10,0
M a[48:51],v[120:123],v[56:59],a[48:51]
T v[114:115],v10,2048
M a[52:55],v[120:123],v[60:63],a[52:55]
G v2,s[48:51],s66,1024
M a[56:59],v[120:123],v[64:67],a[56:59]
s_add_u32 s67,s66,2304
T v[116:117],v10,8
M a[60:63],v[124:127],v[56:59],a[60:63]
T v[118:119],v10,2056
M a[64:67],v[124:127],v[60:63],a[64:67]
T v[120:121],v9,4096
M a[68:71],v[124:127],v[64:67],a[68:71]
T v[122:123],v9,6144
M a[72:75],v[128:131],v[56:59],a[72:75]
G v3,s[48:51],s67,2048
M a[76:79],v[128:131],v[60:63],a[76:79]
T v[124:125],v9,4104
M a[80:83],v[128:131],v[64:67],a[80:83]
T v[126:127],v9,6152
M a[84:87],v[132:135],v[56:59],a[84:87]
G v4,s[48:51],s67,3072
M a[88:91],v[132:135],v[60:63],a[88:91]
s_add_u32 m0,s68,90112
T v[128:129],v10,4096
M a[92:95],v[132:135],v[64:67],a[92:95]
)"
R"(s_add_u32 s66,s71,249344
T v[130:131],v10,6144
M a[96:99],v[136:139],v[56:59],a[96:99]
T v[132:133],v10,4104
M a[100:103],v[136:139],v[60:63],a[100:103]
T v[134:135],v10,6152
M a[104:107],v[136:139],v[64:67],a[104:107]
T v[136:137],v9,8192
M a[108:111],v[140:143],v[56:59],a[108:111]
T v[138:139],v9,10240
M a[112:115],v[140:143],v[60:63],a[112:115]
G v1,s[48:51],s66,0
M a[116:119],v[140:143],v[64:67],a[116:119]
T v[140:141],v9,8200
M a[120:123],v[144:147],v[56:59],a[120:123]
T v[142:143],v9,10248
M a[124:127],v[144:147],v[60:63],a[124:127]
G v2,s[48:51],s66,1024
M a[128:131],v[144:147],v[64:67],a[128:131]
s_add_u32 s67,s66,2304
T v[144:145],v10,8192
M a[132:135],v[148:151],v[56:59],a[132:135]
T v[146:147],v10,10240
M a[136:139],v[148:151],v[60:63],a[136:139]
G v3,s[48:51],s67,2048
M a[140:143],v[148:151],v[64:67],a[140:143]
T v[148:149],v10,8200
M a[144:147],v[152:155],v[56:59],a[144:147]
T v[150:151],v10,10248
M a[148:151],v[152:155],v[60:63],a[148:151]
G v4,s[48:51],s67,3072
M a[152:155],v[152:155],v[64:67],a[152:155]
s_add_u32 m0,s69,73728
T v[152:153],v9,12288
M a[156:159],v[156:159],v[56:59],a[156:159]
T v[154:155],v9,14336
M a[160:163],v[156:159],v[60:63],a[160:163]
s_mov_b32 s66,248832
M a[164:167],v[156:159],v[64:67],a[164:167]
T v[156:157],v9,12296
M a[168:171],v[160:163],v[56:59],a[168:171]
T v[158:159],v9,14344
M a[172:175],v[160:163],v[60:63],a[172:175]
G v14,s[48:51],s66,0
M a[176:179],v[160:163],v[64:67],a[176:179]
T v[160:161],v10,12288
M a[180:183],v[164:167],v[56:59],a[180:183]
T v[162:163],v10,14336
M a[184:187],v[164:167],v[60:63],a[184:187]
M a[188:191],v[164:167],v[64:67],a[188:191]
T v[164:165],v10,12296
M a[192:195],v[168:171],v[56:59],a[192:195]
T v[166:167],v10,14344
M a[196:199],v[168:171],v[60:63],a[196:199]
M a[200:203],v[168:171],v[64:67],a[200:203]
T v[168:169],v9,16384
M a[204:207],v[172:175],v[56:59],a[204:207]
WL 5
T v[170:171],v9,18432
M a[208:211],v[172:175],v[60:63],a[208:211]
buffer_load_dwordx4 v[56:59],v0,s[44:47],s56 offen offset:0 sc1
M a[212:215],v[172:175],v[64:67],a[212:215]
buffer_load_dwordx4 v[60:63],v0,s[44:47],s57 offen offset:0 sc1
WV 43
M a[0:3],v[104:107],v[68:71],a[0:3]
T v[172:173],v9,16392
WV 42
M a[4:7],v[104:107],v[72:75],a[4:7]
T v[174:175],v9,18440
v_add_u32 v9,4294856704,v9
WV 41
M a[8:11],v[104:107],v[76:79],a[8:11]
buffer_load_dwordx4 v[64:67],v0,s[44:47],s58 offen offset:0 sc1
M a[12:15],v[108:111],v[68:71],a[12:15]
v_add_u32 v10,4294856704,v10
M a[16:19],v[108:111],v[72:75],a[16:19]
M a[20:23],v[108:111],v[76:79],a[20:23]
s_waitcnt vmcnt(27) lgkmcnt(0)
s_barrier
M a[24:27],v[112:115],v[68:71],a[24:27]
s_add_u32 m0,s68,110592
T v[104:105],v9,0
M a[28:31],v[112:115],v[72:75],a[28:31]
s_add_u32 s66,s71,290304
T v[106:107],v9,2048
M a[32:35],v[112:115],v[76:79],a[32:35]
T v[108:109],v9,8
M a[36:39],v[116:119],v[68:71],a[36:39]
T v[110:111],v9,2056
M a[40:43],v[116:119],v[72:75],a[40:43]
G v1,s[48:51],s66,0
M a[44:47],v[116:119],v[76:79],a[44:47]
T v[112:113],v10,0
M a[48:51],v[120:123],v[68:71],a[48:51]
T v[114:115],v10,2048
M a[52:55],v[120:123],v[72:75],a[52:55]
G v2,s[48:51],s66,1024
M a[56:59],v[120:123],v[76:79],a[56:59]
s_add_u32 s67,s66,2304
T v[116:117],v10,8
M a[60:63],v[124:127],v[68:71],a[60:63]
T v[118:119],v10,2056
M a[64:67],v[124:127],v[72:75],a[64:67]
T v[120:121],v9,4096
M a[68:71],v[124:127],v[76:79],a[68:71]
T v[122:123],v9,6144
M a[72:75],v[128:131],v[68:71],a[72:75]
G v3,s[48:51],s67,2048
M a[76:79],v[128:131],v[72:75],a[76:79]
T v[124:125],v9,4104
M a[80:83],v[128:131],v[76:79],a[80:83]
T v[126:127],v9,6152
M a[84:87],v[132:135],v[68:71],a[84:87]
G v4,s[48:51],s67,3072
M a[88:91],v[132:135],v[72:75],a[88:91]
s_add_u32 m0,s68,126976
T v[128:129],v10,4096
M a[92:95],v[132:135],v[76:79],a[92:95]
s_add_u32 s66,s71,290816
T v[130:131],v10,6144
M a[96:99],v[136:139],v[68:71],a[96:99]
T v[132:133],v10,4104
M a[100:103],v[136:139],v[72:75],a[100:103]
T v[134:135],v10,6152
M a[104:107],v[136:139],v[76:79],a[104:107]
T v[136:137],v9,8192
M a[108:111],v[140:143],v[68:71],a[108:111]
T v[138:139],v9,10240
M a[112:115],v[140:143],v[72:75],a[112:115]
G v1,s[48:51],s66,0
M a[116:119],v[140:143],v[76:79],a[116:119]
T v[140:141],v9,8200
M a[120:123],v[144:147],v[68:71],a[120:123]
T v[142:143],v9,10248
M a[124:127],v[144:147],v[72:75],a[124:127]
G v2,s[48:51],s66,1024
M a[128:131],v[144:147],v[76:79],a[128:131]
s_add_u32 s67,s66,2304
T v[144:145],v10,8192
M a[132:135],v[148:151],v[68:71],a[132:135]
T v[146:147],v10,10240
M a[136:139],v[148:151],v[72:75],a[136:139]
G v3,s[48:51],s67,2048
M a[140:143],v[148:151],v[76:79],a[140:143]
T v[148:149],v10,8200
M a[144:147],v[152:155],v[68:71],a[144:147]
T v[150:151],v10,10248
M a[148:151],v[152:155],v[72:75],a[148:151]
G v4,s[48:51],s67,3072
M a[152:155],v[152:155],v[76:79],a[152:155]
s_add_u32 m0,s69,110592
T v[152:153],v9,12288
M a[156:159],v[156:159],v[68:71],a[156:159]
T v[154:155],v9,14336
M a[160:163],v[156:159],v[72:75],a[160:163]
s_mov_b32 s66,290304
M a[164:167],v[156:159],v[76:79],a[164:167]
T v[156:157],v9,12296
M a[168:171],v[160:163],v[68:71],a[168:171]
T v[158:159],v9,14344
M a[172:175],v[160:163],v[72:75],a[172:175]
G v14,s[48:51],s66,0
M a[176:179],v[160:163],v[76:79],a[176:179]
T v[160:161],v10,12288
M a[180:183],v[164:167],v[68:71],a[180:183]
T v[162:163],v10,14336
M a[184:187],v[164:167],v[72:75],a[184:187]
M a[188:191],v[164:167],v[76:79],a[188:191]
T v[164:165],v10,12296
M a[192:195],v[168:171],v[68:71],a[192:195]
T v[166:167],v10,14344
M a[196:199],v[168:171],v[72:75],a[196:199]
M a[200:203],v[168:171],v[76:79],a[200:203]
T v[168:169],v9,16384
M a[204:207],v[172:175],v[68:71],a[204:207]
WL 5
T v[170:171],v9,18432
M a[208:211],v[172:175],v[72:75],a[208:211]
buffer_load_dwordx4 v[68:71],v0,s[44:47],s56 offen offset:1024 sc1
M a[212:215],v[172:175],v[76:79],a[212:215]
buffer_load_dwordx4 v[72:75],v0,s[44:47],s57 offen offset:1024 sc1
M a[0:3],v[104:107],v[80:83],a[0:3]
T v[172:173],v9,16392
M a[4:7],v[104:107],v[84:87],a[4:7]
T v[174:175],v9,18440
v_add_u32 v9,36864,v9
M a[8:11],v[104:107],v[88:91],a[8:11]
buffer_load_dwordx4 v[76:79],v0,s[44:47],s58 offen offset:1024 sc1
M a[12:15],v[108:111],v[80:83],a[12:15]
v_add_u32 v10,36864,v10
M a[16:19],v[108:111],v[84:87],a[16:19]
M a[20:23],v[108:111],v[88:91],a[20:23]
s_waitcnt vmcnt(27) lgkmcnt(0)
s_barrier
M a[24:27],v[112:115],v[80:83],a[24:27]
s_add_u32 m0,s68,0
T v[104:105],v9,0
M a[28:31],v[112:115],v[84:87],a[28:31]
s_add_u32 s66,s71,331776
T v[106:107],v9,2048
M a[32:35],v[112:115],v[88:91],a[32:35]
T v[108:109],v9,8
M a[36:39],v[116:119],v[80:83],a[36:39]
T v[110:111],v9,2056
M a[40:43],v[116:119],v[84:87],a[40:43]
G v1,s[48:51],s66,0
M a[44:47],v[116:119],v[88:91],a[44:47]
T v[112:113],v10,0
M a[48:51],v[120:123],v[80:83],a[48:51]
T v[114:115],v10,2048
M a[52:55],v[120:123],v[84:87],a[52:55]
G v2,s[48:51],s66,1024
M a[56:59],v[120:123],v[88:91],a[56:59]
s_add_u32 s67,s66,2304
T v[116:117],v10,8
M a[60:63],v[124:127],v[80:83],a[60:63]
T v[118:119],v10,2056
M a[64:67],v[124:127],v[84:87],a[64:67]
T v[120:121],v9,4096
M a[68:71],v[124:127],v[88:91],a[68:71]
T v[122:123],v9,6144
M a[72:75],v[128:131],v[80:83],a[72:75]
G v3,s[48:51],s67,2048
M a[76:79],v[128:131],v[84:87],a[76:79]
T v[124:125],v9,4104
M a[80:83],v[128:131],v[88:91],a[80:83]
T v[126:127],v9,6152
M a[84:87],v[132:135],v[80:83],a[84:87]
G v4,s[48:51],s67,3072
M a[88:91],v[132:135],v[84:87],a[88:91]
s_add_u32 m0,s68,16384
T v[128:129],v10,4096
M a[92:95],v[132:135],v[88:91],a[92:95]
s_add_u32 s66,s71,332288
T v[130:131],v10,6144
M a[96:99],v[136:139],v[80:83],a[96:99]
T v[132:133],v10,4104
M a[100:103],v[136:139],v[84:87],a[100:103]
T v[134:135],v10,6152
M a[104:107],v[136:139],v[88:91],a[104:107]
T v[136:137],v9,8192
M a[108:111],v[140:143],v[80:83],a[108:111]
T v[138:139],v9,10240
M a[112:115],v[140:143],v[84:87],a[112:115]
G v1,s[48:51],s66,0
M a[116:119],v[140:143],v[88:91],a[116:119]
T v[140:141],v9,8200
M a[120:123],v[144:147],v[80:83],a[120:123]
T v[142:143],v9,10248
M a[124:127],v[144:147],v[84:87],a[124:127]
G v2,s[48:51],s66,1024
M a[128:131],v[144:147],v[88:91],a[128:131]
s_add_u32 s67,s66,2304
T v[144:145],v10,8192
M a[132:135],v[148:151],v[80:83],a[132:135]
T v[146:147],v10,10240
M a[136:139],v[148:151],v[84:87],a[136:139]
G v3,s[48:51],s67,2048
M a[140:143],v[148:151],v[88:91],a[140:143]
T v[148:149],v10,8200
M a[144:147],v[152:155],v[80:83],a[144:147]
T v[150:151],v10,10248
M a[148:151],v[152:155],v[84:87],a[148:151]
G v4,s[48:51],s67,3072
M a[152:155],v[152:155],v[88:91],a[152:155]
s_add_u32 m0,s69,0
T v[152:153],v9,12288
M a[156:159],v[156:159],v[80:83],a[156:159]
T v[154:155],v9,14336
M a[160:163],v[156:159],v[84:87],a[160:163]
s_mov_b32 s66,331776
M a[164:167],v[156:159],v[88:91],a[164:167]
T v[156:157],v9,12296
M a[168:171],v[160:163],v[80:83],a[168:171]
T v[158:159],v9,14344
M a[172:175],v[160:163],v[84:87],a[172:175]
G v14,s[48:51],s66,0
M a[176:179],v[160:163],v[88:91],a[176:179]
T v[160:161],v10,12288
M a[180:183],v[164:167],v[80:83],a[180:183]
T v[162:163],v10,14336
M a[184:187],v[164:167],v[84:87],a[184:187]
M a[188:191],v[164:167],v[88:91],a[188:191]
T v[164:165],v10,12296
M a[192:195],v[168:171],v[80:83],a[192:195]
T v[166:167],v10,14344
M a[196:199],v[168:171],v[84:87],a[196:199]
M a[200:203],v[168:171],v[88:91],a[200:203]
T v[168:169],v9,16384
M a[204:207],v[172:175],v[80:83],a[204:207]
WL 5
T v[170:171],v9,18432
M a[208:211],v[172:175],v[84:87],a[208:211]
buffer_load_dwordx4 v[80:83],v0,s[44:47],s56 offen offset:2048 sc1
M a[212:215],v[172:175],v[88:91],a[212:215]
buffer_load_dwordx4 v[84:87],v0,s[44:47],s57 offen offset:2048 sc1
M a[0:3],v[104:107],v[92:95],a[0:3]
T v[172:173],v9,16392
M a[4:7],v[104:107],v[96:99],a[4:7]
T v[174:175],v9,18440
v_add_u32 v9,36864,v9
M a[8:11],v[104:107],v[100:103],a[8:11]
buffer_load_dwordx4 v[88:91],v0,s[44:47],s58 offen offset:2048 sc1
M a[12:15],v[108:111],v[92:95],a[12:15]
v_add_u32 v10,36864,v10
M a[16:19],v[108:111],v[96:99],a[16:19]
M a[20:23],v[108:111],v[100:103],a[20:23]
s_waitcnt vmcnt(27) lgkmcnt(0)
s_barrier
M a[24:27],v[112:115],v[92:95],a[24:27]
s_add_u32 m0,s68,36864
T v[104:105],v9,0
M a[28:31],v[112:115],v[96:99],a[28:31]
s_add_u32 s66,s71,373248
T v[106:107],v9,2048
M a[32:35],v[112:115],v[100:103],a[32:35]
T v[108:109],v9,8
M a[36:39],v[116:119],v[92:95],a[36:39]
T v[110:111],v9,2056
M a[40:43],v[116:119],v[96:99],a[40:43]
G v1,s[48:51],s66,0
M a[44:47],v[116:119],v[100:103],a[44:47]
T v[112:113],v10,0
M a[48:51],v[120:123],v[92:95],a[48:51]
T v[114:115],v10,2048
M a[52:55],v[120:123],v[96:99],a[52:55]
G v2,s[48:51],s66,1024
M a[56:59],v[120:123],v[100:103],a[56:59]
s_add_u32 s67,s66,2304
T v[116:117],v10,8
M a[60:63],v[124:127],v[92:95],a[60:63]
T v[118:119],v10,2056
M a[64:67],v[124:127],v[96:99],a[64:67]
T v[120:121],v9,4096
M a[68:71],v[124:127],v[100:103],a[68:71]
T v[122:123],v9,6144
M a[72:75],v[128:131],v[92:95],a[72:75]
G v3,s[48:51],s67,2048
M a[76:79],v[128:131],v[96:99],a[76:79]
T v[124:125],v9,4104
M a[80:83],v[128:131],v[100:103],a[80:83]
T v[126:127],v9,6152
M a[84:87],v[132:135],v[92:95],a[84:87]
G v4,s[48:51],s67,3072
M a[88:91],v[132:135],v[96:99],a[88:91]
s_add_u32 m0,s68,53248
T v[128:129],v10,4096
M a[92:95],v[132:135],v[100:103],a[92:95]
s_add_u32 s66,s71,373760
T v[130:131],v10,6144
M a[96:99],v[136:139],v[92:95],a[96:99]
T v[132:133],v10,4104
M a[100:103],v[136:139],v[96:99],a[100:103]
T v[134:135],v10,6152
M a[104:107],v[136:139],v[100:103],a[104:107]
T v[136:137],v9,8192
M a[108:111],v[140:143],v[92:95],a[108:111]
T v[138:139],v9,10240
M a[112:115],v[140:143],v[96:99],a[112:115]
G v1,s[48:51],s66,0
M a[116:119],v[140:143],v[100:103],a[116:119]
T v[140:141],v9,8200
M a[120:123],v[144:147],v[92:95],a[120:123]
T v[142:143],v9,10248
M a[124:127],v[144:147],v[96:99],a[124:127]
G v2,s[48:51],s66,1024
M a[128:131],v[144:147],v[100:103],a[128:131]
s_add_u32 s67,s66,2304
T v[144:145],v10,8192
M a[132:135],v[148:151],v[92:95],a[132:135]
T v[146:147],v10,10240
M a[136:139],v[148:151],v[96:99],a[136:139]
G v3,s[48:51],s67,2048
M a[140:143],v[148:151],v[100:103],a[140:143]
T v[148:149],v10,8200
M a[144:147],v[152:155],v[92:95],a[144:147]
T v[150:151],v10,10248
M a[148:151],v[152:155],v[96:99],a[148:151]
G v4,s[48:51],s67,3072
M a[152:155],v[152:155],v[100:103],a[152:155]
s_add_u32 m0,s69,36864
T v[152:153],v9,12288
M a[156:159],v[156:159],v[92:95],a[156:159]
T v[154:155],v9,14336
M a[160:163],v[156:159],v[96:99],a[160:163]
s_mov_b32 s66,373248
M a[164:167],v[156:159],v[100:103],a[164:167]
T v[156:157],v9,12296
M a[168:171],v[160:163],v[92:95],a[168:171]
T v[158:159],v9,14344
M a[172:175],v[160:163],v[96:99],a[172:175]
G v14,s[48:51],s66,0
M a[176:179],v[160:163],v[100:103],a[176:179]
T v[160:161],v10,12288
M a[180:183],v[164:167],v[92:95],a[180:183]
T v[162:163],v10,14336
M a[184:187],v[164:167],v[96:99],a[184:187]
M a[188:191],v[164:167],v[100:103],a[188:191]
T v[164:165],v10,12296
M a[192:195],v[168:171],v[92:95],a[192:195]
)"
R"(T v[166:167],v10,14344
M a[196:199],v[168:171],v[96:99],a[196:199]
M a[200:203],v[168:171],v[100:103],a[200:203]
T v[168:169],v9,16384
M a[204:207],v[172:175],v[92:95],a[204:207]
WL 5
T v[170:171],v9,18432
M a[208:211],v[172:175],v[96:99],a[208:211]
buffer_load_dwordx4 v[92:95],v0,s[44:47],s56 offen offset:3072 sc1
M a[212:215],v[172:175],v[100:103],a[212:215]
s_add_u32 s56,s56,4096
buffer_load_dwordx4 v[96:99],v0,s[44:47],s57 offen offset:3072 sc1
M a[0:3],v[104:107],v[32:35],a[0:3]
s_add_u32 s57,s57,4096
T v[172:173],v9,16392
M a[4:7],v[104:107],v[36:39],a[4:7]
T v[174:175],v9,18440
v_add_u32 v9,36864,v9
M a[8:11],v[104:107],v[40:43],a[8:11]
buffer_load_dwordx4 v[100:103],v0,s[44:47],s58 offen offset:3072 sc1
M a[12:15],v[108:111],v[32:35],a[12:15]
s_add_u32 s58,s58,4096
v_add_u32 v10,36864,v10
M a[16:19],v[108:111],v[36:39],a[16:19]
M a[20:23],v[108:111],v[40:43],a[20:23]
s_waitcnt vmcnt(27) lgkmcnt(0)
s_barrier
M a[24:27],v[112:115],v[32:35],a[24:27]
s_add_u32 m0,s68,73728
T v[104:105],v9,0
M a[28:31],v[112:115],v[36:39],a[28:31]
s_add_u32 s66,s71,414720
T v[106:107],v9,2048
M a[32:35],v[112:115],v[40:43],a[32:35]
T v[108:109],v9,8
M a[36:39],v[116:119],v[32:35],a[36:39]
T v[110:111],v9,2056
M a[40:43],v[116:119],v[36:39],a[40:43]
G v1,s[48:51],s66,0
M a[44:47],v[116:119],v[40:43],a[44:47]
T v[112:113],v10,0
M a[48:51],v[120:123],v[32:35],a[48:51]
T v[114:115],v10,2048
M a[52:55],v[120:123],v[36:39],a[52:55]
G v2,s[48:51],s66,1024
M a[56:59],v[120:123],v[40:43],a[56:59]
s_add_u32 s67,s66,2304
T v[116:117],v10,8
M a[60:63],v[124:127],v[32:35],a[60:63]
T v[118:119],v10,2056
M a[64:67],v[124:127],v[36:39],a[64:67]
T v[120:121],v9,4096
M a[68:71],v[124:127],v[40:43],a[68:71]
T v[122:123],v9,6144
M a[72:75],v[128:131],v[32:35],a[72:75]
G v3,s[48:51],s67,2048
M a[76:79],v[128:131],v[36:39],a[76:79]
T v[124:125],v9,4104
M a[80:83],v[128:131],v[40:43],a[80:83]
T v[126:127],v9,6152
M a[84:87],v[132:135],v[32:35],a[84:87]
G v4,s[48:51],s67,3072
M a[88:91],v[132:135],v[36:39],a[88:91]
s_add_u32 m0,s68,90112
T v[128:129],v10,4096
M a[92:95],v[132:135],v[40:43],a[92:95]
s_add_u32 s66,s71,415232
T v[130:131],v10,6144
M a[96:99],v[136:139],v[32:35],a[96:99]
T v[132:133],v10,4104
M a[100:103],v[136:139],v[36:39],a[100:103]
T v[134:135],v10,6152
M a[104:107],v[136:139],v[40:43],a[104:107]
T v[136:137],v9,8192
M a[108:111],v[140:143],v[32:35],a[108:111]
T v[138:139],v9,10240
M a[112:115],v[140:143],v[36:39],a[112:115]
G v1,s[48:51],s66,0
M a[116:119],v[140:143],v[40:43],a[116:119]
T v[140:141],v9,8200
M a[120:123],v[144:147],v[32:35],a[120:123]
T v[142:143],v9,10248
M a[124:127],v[144:147],v[36:39],a[124:127]
G v2,s[48:51],s66,1024
M a[128:131],v[144:147],v[40:43],a[128:131]
s_add_u32 s67,s66,2304
T v[144:145],v10,8192
M a[132:135],v[148:151],v[32:35],a[132:135]
T v[146:147],v10,10240
M a[136:139],v[148:151],v[36:39],a[136:139]
G v3,s[48:51],s67,2048
M a[140:143],v[148:151],v[40:43],a[140:143]
T v[148:149],v10,8200
M a[144:147],v[152:155],v[32:35],a[144:147]
T v[150:151],v10,10248
M a[148:151],v[152:155],v[36:39],a[148:151]
G v4,s[48:51],s67,3072
M a[152:155],v[152:155],v[40:43],a[152:155]
s_add_u32 m0,s69,73728
T v[152:153],v9,12288
M a[156:159],v[156:159],v[32:35],a[156:159]
T v[154:155],v9,14336
M a[160:163],v[156:159],v[36:39],a[160:163]
s_mov_b32 s66,414720
M a[164:167],v[156:159],v[40:43],a[164:167]
T v[156:157],v9,12296
M a[168:171],v[160:163],v[32:35],a[168:171]
T v[158:159],v9,14344
M a[172:175],v[160:163],v[36:39],a[172:175]
G v14,s[48:51],s66,0
M a[176:179],v[160:163],v[40:43],a[176:179]
T v[160:161],v10,12288
M a[180:183],v[164:167],v[32:35],a[180:183]
T v[162:163],v10,14336
M a[184:187],v[164:167],v[36:39],a[184:187]
M a[188:191],v[164:167],v[40:43],a[188:191]
T v[164:165],v10,12296
M a[192:195],v[168:171],v[32:35],a[192:195]
T v[166:167],v10,14344
M a[196:199],v[168:171],v[36:39],a[196:199]
M a[200:203],v[168:171],v[40:43],a[200:203]
T v[168:169],v9,16384
M a[204:207],v[172:175],v[32:35],a[204:207]
WL 5
T v[170:171],v9,18432
M a[208:211],v[172:175],v[36:39],a[208:211]
buffer_load_dwordx4 v[32:35],v0,s[44:47],s56 offen offset:0 sc1
M a[212:215],v[172:175],v[40:43],a[212:215]
buffer_load_dwordx4 v[36:39],v0,s[44:47],s57 offen offset:0 sc1
M a[0:3],v[104:107],v[44:47],a[0:3]
T v[172:173],v9,16392
M a[4:7],v[104:107],v[48:51],a[4:7]
T v[174:175],v9,18440
v_add_u32 v9,4294856704,v9
M a[8:11],v[104:107],v[52:55],a[8:11]
buffer_load_dwordx4 v[40:43],v0,s[44:47],s58 offen offset:0 sc1
M a[12:15],v[108:111],v[44:47],a[12:15]
v_add_u32 v10,4294856704,v10
M a[16:19],v[108:111],v[48:51],a[16:19]
M a[20:23],v[108:111],v[52:55],a[20:23]
s_waitcnt vmcnt(27) lgkmcnt(0)
s_barrier
M a[24:27],v[112:115],v[44:47],a[24:27]
s_add_u32 m0,s68,110592
T v[104:105],v9,0
M a[28:31],v[112:115],v[48:51],a[28:31]
s_add_u32 s66,s71,456192
T v[106:107],v9,2048
M a[32:35],v[112:115],v[52:55],a[32:35]
T v[108:109],v9,8
M a[36:39],v[116:119],v[44:47],a[36:39]
T v[110:111],v9,2056
M a[40:43],v[116:119],v[48:51],a[40:43]
G v1,s[48:51],s66,0
M a[44:47],v[116:119],v[52:55],a[44:47]
T v[112:113],v10,0
M a[48:51],v[120:123],v[44:47],a[48:51]
T v[114:115],v10,2048
M a[52:55],v[120:123],v[48:51],a[52:55]
G v2,s[48:51],s66,1024
M a[56:59],v[120:123],v[52:55],a[56:59]
s_add_u32 s67,s66,2304
T v[116:117],v10,8
M a[60:63],v[124:127],v[44:47],a[60:63]
T v[118:119],v10,2056
M a[64:67],v[124:127],v[48:51],a[64:67]
T v[120:121],v9,4096
M a[68:71],v[124:127],v[52:55],a[68:71]
T v[122:123],v9,6144
M a[72:75],v[128:131],v[44:47],a[72:75]
G v3,s[48:51],s67,2048
M a[76:79],v[128:131],v[48:51],a[76:79]
T v[124:125],v9,4104
M a[80:83],v[128:131],v[52:55],a[80:83]
T v[126:127],v9,6152
M a[84:87],v[132:135],v[44:47],a[84:87]
G v4,s[48:51],s67,3072
M a[88:91],v[132:135],v[48:51],a[88:91]
s_add_u32 m0,s68,126976
T v[128:129],v10,4096
M a[92:95],v[132:135],v[52:55],a[92:95]
s_add_u32 s66,s71,456704
T v[130:131],v10,6144
M a[96:99],v[136:139],v[44:47],a[96:99]
T v[132:133],v10,4104
M a[100:103],v[136:139],v[48:51],a[100:103]
T v[134:135],v10,6152
M a[104:107],v[136:139],v[52:55],a[104:107]
T v[136:137],v9,8192
M a[108:111],v[140:143],v[44:47],a[108:111]
T v[138:139],v9,10240
M a[112:115],v[140:143],v[48:51],a[112:115]
G v1,s[48:51],s66,0
M a[116:119],v[140:143],v[52:55],a[116:119]
T v[140:141],v9,8200
M a[120:123],v[144:147],v[44:47],a[120:123]
T v[142:143],v9,10248
M a[124:127],v[144:147],v[48:51],a[124:127]
G v2,s[48:51],s66,1024
M a[128:131],v[144:147],v[52:55],a[128:131]
s_add_u32 s67,s66,2304
T v[144:145],v10,8192
M a[132:135],v[148:151],v[44:47],a[132:135]
T v[146:147],v10,10240
M a[136:139],v[148:151],v[48:51],a[136:139]
G v3,s[48:51],s67,2048
M a[140:143],v[148:151],v[52:55],a[140:143]
T v[148:149],v10,8200
M a[144:147],v[152:155],v[44:47],a[144:147]
T v[150:151],v10,10248
M a[148:151],v[152:155],v[48:51],a[148:151]
G v4,s[48:51],s67,3072
M a[152:155],v[152:155],v[52:55],a[152:155]
s_add_u32 m0,s69,110592
T v[152:153],v9,12288
M a[156:159],v[156:159],v[44:47],a[156:159]
T v[154:155],v9,14336
M a[160:163],v[156:159],v[48:51],a[160:163]
s_mov_b32 s66,456192
M a[164:167],v[156:159],v[52:55],a[164:167]
T v[156:157],v9,12296
M a[168:171],v[160:163],v[44:47],a[168:171]
T v[158:159],v9,14344
M a[172:175],v[160:163],v[48:51],a[172:175]
G v14,s[48:51],s66,0
M a[176:179],v[160:163],v[52:55],a[176:179]
T v[160:161],v10,12288
M a[180:183],v[164:167],v[44:47],a[180:183]
T v[162:163],v10,14336
M a[184:187],v[164:167],v[48:51],a[184:187]
M a[188:191],v[164:167],v[52:55],a[188:191]
T v[164:165],v10,12296
M a[192:195],v[168:171],v[44:47],a[192:195]
T v[166:167],v10,14344
M a[196:199],v[168:171],v[48:51],a[196:199]
M a[200:203],v[168:171],v[52:55],a[200:203]
T v[168:169],v9,16384
M a[204:207],v[172:175],v[44:47],a[204:207]
WL 5
T v[170:171],v9,18432
M a[208:211],v[172:175],v[48:51],a[208:211]
buffer_load_dwordx4 v[44:47],v0,s[44:47],s56 offen offset:1024 sc1
M a[212:215],v[172:175],v[52:55],a[212:215]
buffer_load_dwordx4 v[48:51],v0,s[44:47],s57 offen offset:1024 sc1
M a[0:3],v[104:107],v[56:59],a[0:3]
T v[172:173],v9,16392
M a[4:7],v[104:107],v[60:63],a[4:7]
T v[174:175],v9,18440
v_add_u32 v9,36864,v9
M a[8:11],v[104:107],v[64:67],a[8:11]
buffer_load_dwordx4 v[52:55],v0,s[44:47],s58 offen offset:1024 sc1
M a[12:15],v[108:111],v[56:59],a[12:15]
v_add_u32 v10,36864,v10
M a[16:19],v[108:111],v[60:63],a[16:19]
M a[20:23],v[108:111],v[64:67],a[20:23]
s_waitcnt vmcnt(27) lgkmcnt(0)
s_barrier
M a[24:27],v[112:115],v[56:59],a[24:27]
s_add_u32 m0,s68,0
T v[104:105],v9,0
M a[28:31],v[112:115],v[60:63],a[28:31]
s_add_u32 s66,s71,497664
T v[106:107],v9,2048
M a[32:35],v[112:115],v[64:67],a[32:35]
T v[108:109],v9,8
M a[36:39],v[116:119],v[56:59],a[36:39]
T v[110:111],v9,2056
M a[40:43],v[116:119],v[60:63],a[40:43]
G v1,s[48:51],s66,0
M a[44:47],v[116:119],v[64:67],a[44:47]
T v[112:113],v10,0
M a[48:51],v[120:123],v[56:59],a[48:51]
T v[114:115],v10,2048
M a[52:55],v[120:123],v[60:63],a[52:55]
G v2,s[48:51],s66,1024
M a[56:59],v[120:123],v[64:67],a[56:59]
s_add_u32 s67,s66,2304
T v[116:117],v10,8
M a[60:63],v[124:127],v[56:59],a[60:63]
T v[118:119],v10,2056
M a[64:67],v[124:127],v[60:63],a[64:67]
T v[120:121],v9,4096
M a[68:71],v[124:127],v[64:67],a[68:71]
T v[122:123],v9,6144
M a[72:75],v[128:131],v[56:59],a[72:75]
G v3,s[48:51],s67,2048
M a[76:79],v[128:131],v[60:63],a[76:79]
T v[124:125],v9,4104
M a[80:83],v[128:131],v[64:67],a[80:83]
T v[126:127],v9,6152
M a[84:87],v[132:135],v[56:59],a[84:87]
G v4,s[48:51],s67,3072
M a[88:91],v[132:135],v[60:63],a[88:91]
s_add_u32 m0,s68,16384
T v[128:129],v10,4096
M a[92:95],v[132:135],v[64:67],a[92:95]
s_add_u32 s66,s71,498176
T v[130:131],v10,6144
M a[96:99],v[136:139],v[56:59],a[96:99]
T v[132:133],v10,4104
M a[100:103],v[136:139],v[60:63],a[100:103]
T v[134:135],v10,6152
M a[104:107],v[136:139],v[64:67],a[104:107]
T v[136:137],v9,8192
M a[108:111],v[140:143],v[56:59],a[108:111]
T v[138:139],v9,10240
M a[112:115],v[140:143],v[60:63],a[112:115]
G v1,s[48:51],s66,0
M a[116:119],v[140:143],v[64:67],a[116:119]
T v[140:141],v9,8200
M a[120:123],v[144:147],v[56:59],a[120:123]
T v[142:143],v9,10248
M a[124:127],v[144:147],v[60:63],a[124:127]
G v2,s[48:51],s66,1024
M a[128:131],v[144:147],v[64:67],a[128:131]
s_add_u32 s67,s66,2304
T v[144:145],v10,8192
M a[132:135],v[148:151],v[56:59],a[132:135]
T v[146:147],v10,10240
M a[136:139],v[148:151],v[60:63],a[136:139]
G v3,s[48:51],s67,2048
M a[140:143],v[148:151],v[64:67],a[140:143]
T v[148:149],v10,8200
M a[144:147],v[152:155],v[56:59],a[144:147]
T v[150:151],v10,10248
M a[148:151],v[152:155],v[60:63],a[148:151]
G v4,s[48:51],s67,3072
M a[152:155],v[152:155],v[64:67],a[152:155]
s_add_u32 m0,s69,0
T v[152:153],v9,12288
M a[156:159],v[156:159],v[56:59],a[156:159]
T v[154:155],v9,14336
M a[160:163],v[156:159],v[60:63],a[160:163]
s_mov_b32 s66,497664
M a[164:167],v[156:159],v[64:67],a[164:167]
T v[156:157],v9,12296
M a[168:171],v[160:163],v[56:59],a[168:171]
T v[158:159],v9,14344
M a[172:175],v[160:163],v[60:63],a[172:175]
G v14,s[48:51],s66,0
M a[176:179],v[160:163],v[64:67],a[176:179]
T v[160:161],v10,12288
M a[180:183],v[164:167],v[56:59],a[180:183]
T v[162:163],v10,14336
M a[184:187],v[164:167],v[60:63],a[184:187]
M a[188:191],v[164:167],v[64:67],a[188:191]
T v[164:165],v10,12296
M a[192:195],v[168:171],v[56:59],a[192:195]
T v[166:167],v10,14344
M a[196:199],v[168:171],v[60:63],a[196:199]
M a[200:203],v[168:171],v[64:67],a[200:203]
T v[168:169],v9,16384
M a[204:207],v[172:175],v[56:59],a[204:207]
WL 5
T v[170:171],v9,18432
M a[208:211],v[172:175],v[60:63],a[208:211]
buffer_load_dwordx4 v[56:59],v0,s[44:47],s56 offen offset:2048 sc1
M a[212:215],v[172:175],v[64:67],a[212:215]
buffer_load_dwordx4 v[60:63],v0,s[44:47],s57 offen offset:2048 sc1
M a[0:3],v[104:107],v[68:71],a[0:3]
T v[172:173],v9,16392
M a[4:7],v[104:107],v[72:75],a[4:7]
T v[174:175],v9,18440
v_add_u32 v9,36864,v9
M a[8:11],v[104:107],v[76:79],a[8:11]
buffer_load_dwordx4 v[64:67],v0,s[44:47],s58 offen offset:2048 sc1
M a[12:15],v[108:111],v[68:71],a[12:15]
v_add_u32 v10,36864,v10
M a[16:19],v[108:111],v[72:75],a[16:19]
M a[20:23],v[108:111],v[76:79],a[20:23]
s_waitcnt vmcnt(27) lgkmcnt(0)
s_barrier
M a[24:27],v[112:115],v[68:71],a[24:27]
s_add_u32 m0,s68,36864
T v[104:105],v9,0
M a[28:31],v[112:115],v[72:75],a[28:31]
s_add_u32 s66,s71,539136
T v[106:107],v9,2048
M a[32:35],v[112:115],v[76:79],a[32:35]
T v[108:109],v9,8
M a[36:39],v[116:119],v[68:71],a[36:39]
T v[110:111],v9,2056
M a[40:43],v[116:119],v[72:75],a[40:43]
G v1,s[48:51],s66,0
M a[44:47],v[116:119],v[76:79],a[44:47]
T v[112:113],v10,0
M a[48:51],v[120:123],v[68:71],a[48:51]
T v[114:115],v10,2048
M a[52:55],v[120:123],v[72:75],a[52:55]
G v2,s[48:51],s66,1024
M a[56:59],v[120:123],v[76:79],a[56:59]
s_add_u32 s67,s66,2304
T v[116:117],v10,8
M a[60:63],v[124:127],v[68:71],a[60:63]
T v[118:119],v10,2056
M a[64:67],v[124:127],v[72:75],a[64:67]
T v[120:121],v9,4096
M a[68:71],v[124:127],v[76:79],a[68:71]
T v[122:123],v9,6144
M a[72:75],v[128:131],v[68:71],a[72:75]
)"
R"(G v3,s[48:51],s67,2048
M a[76:79],v[128:131],v[72:75],a[76:79]
T v[124:125],v9,4104
M a[80:83],v[128:131],v[76:79],a[80:83]
T v[126:127],v9,6152
M a[84:87],v[132:135],v[68:71],a[84:87]
G v4,s[48:51],s67,3072
M a[88:91],v[132:135],v[72:75],a[88:91]
s_add_u32 m0,s68,53248
T v[128:129],v10,4096
M a[92:95],v[132:135],v[76:79],a[92:95]
s_add_u32 s66,s71,539648
T v[130:131],v10,6144
M a[96:99],v[136:139],v[68:71],a[96:99]
T v[132:133],v10,4104
M a[100:103],v[136:139],v[72:75],a[100:103]
T v[134:135],v10,6152
M a[104:107],v[136:139],v[76:79],a[104:107]
T v[136:137],v9,8192
M a[108:111],v[140:143],v[68:71],a[108:111]
T v[138:139],v9,10240
M a[112:115],v[140:143],v[72:75],a[112:115]
G v1,s[48:51],s66,0
M a[116:119],v[140:143],v[76:79],a[116:119]
T v[140:141],v9,8200
M a[120:123],v[144:147],v[68:71],a[120:123]
T v[142:143],v9,10248
M a[124:127],v[144:147],v[72:75],a[124:127]
G v2,s[48:51],s66,1024
M a[128:131],v[144:147],v[76:79],a[128:131]
s_add_u32 s67,s66,2304
T v[144:145],v10,8192
M a[132:135],v[148:151],v[68:71],a[132:135]
T v[146:147],v10,10240
M a[136:139],v[148:151],v[72:75],a[136:139]
G v3,s[48:51],s67,2048
M a[140:143],v[148:151],v[76:79],a[140:143]
T v[148:149],v10,8200
M a[144:147],v[152:155],v[68:71],a[144:147]
T v[150:151],v10,10248
M a[148:151],v[152:155],v[72:75],a[148:151]
G v4,s[48:51],s67,3072
M a[152:155],v[152:155],v[76:79],a[152:155]
s_add_u32 m0,s69,36864
T v[152:153],v9,12288
M a[156:159],v[156:159],v[68:71],a[156:159]
T v[154:155],v9,14336
M a[160:163],v[156:159],v[72:75],a[160:163]
s_mov_b32 s66,539136
M a[164:167],v[156:159],v[76:79],a[164:167]
T v[156:157],v9,12296
M a[168:171],v[160:163],v[68:71],a[168:171]
T v[158:159],v9,14344
M a[172:175],v[160:163],v[72:75],a[172:175]
G v14,s[48:51],s66,0
M a[176:179],v[160:163],v[76:79],a[176:179]
T v[160:161],v10,12288
M a[180:183],v[164:167],v[68:71],a[180:183]
T v[162:163],v10,14336
M a[184:187],v[164:167],v[72:75],a[184:187]
M a[188:191],v[164:167],v[76:79],a[188:191]
T v[164:165],v10,12296
M a[192:195],v[168:171],v[68:71],a[192:195]
T v[166:167],v10,14344
M a[196:199],v[168:171],v[72:75],a[196:199]
M a[200:203],v[168:171],v[76:79],a[200:203]
T v[168:169],v9,16384
M a[204:207],v[172:175],v[68:71],a[204:207]
WL 5
T v[170:171],v9,18432
M a[208:211],v[172:175],v[72:75],a[208:211]
buffer_load_dwordx4 v[68:71],v0,s[44:47],s56 offen offset:3072 sc1
M a[212:215],v[172:175],v[76:79],a[212:215]
s_add_u32 s56,s56,4096
buffer_load_dwordx4 v[72:75],v0,s[44:47],s57 offen offset:3072 sc1
M a[0:3],v[104:107],v[80:83],a[0:3]
s_add_u32 s57,s57,4096
T v[172:173],v9,16392
M a[4:7],v[104:107],v[84:87],a[4:7]
T v[174:175],v9,18440
v_add_u32 v9,36864,v9
M a[8:11],v[104:107],v[88:91],a[8:11]
buffer_load_dwordx4 v[76:79],v0,s[44:47],s58 offen offset:3072 sc1
M a[12:15],v[108:111],v[80:83],a[12:15]
s_add_u32 s58,s58,4096
v_add_u32 v10,36864,v10
M a[16:19],v[108:111],v[84:87],a[16:19]
M a[20:23],v[108:111],v[88:91],a[20:23]
s_waitcnt vmcnt(27) lgkmcnt(0)
s_barrier
M a[24:27],v[112:115],v[80:83],a[24:27]
s_add_u32 m0,s68,73728
T v[104:105],v9,0
M a[28:31],v[112:115],v[84:87],a[28:31]
s_add_u32 s66,s71,580608
T v[106:107],v9,2048
M a[32:35],v[112:115],v[88:91],a[32:35]
T v[108:109],v9,8
M a[36:39],v[116:119],v[80:83],a[36:39]
T v[110:111],v9,2056
M a[40:43],v[116:119],v[84:87],a[40:43]
G v1,s[48:51],s66,0
M a[44:47],v[116:119],v[88:91],a[44:47]
T v[112:113],v10,0
M a[48:51],v[120:123],v[80:83],a[48:51]
T v[114:115],v10,2048
M a[52:55],v[120:123],v[84:87],a[52:55]
G v2,s[48:51],s66,1024
M a[56:59],v[120:123],v[88:91],a[56:59]
s_add_u32 s67,s66,2304
T v[116:117],v10,8
M a[60:63],v[124:127],v[80:83],a[60:63]
T v[118:119],v10,2056
M a[64:67],v[124:127],v[84:87],a[64:67]
T v[120:121],v9,4096
M a[68:71],v[124:127],v[88:91],a[68:71]
T v[122:123],v9,6144
M a[72:75],v[128:131],v[80:83],a[72:75]
G v3,s[48:51],s67,2048
M a[76:79],v[128:131],v[84:87],a[76:79]
T v[124:125],v9,4104
M a[80:83],v[128:131],v[88:91],a[80:83]
T v[126:127],v9,6152
M a[84:87],v[132:135],v[80:83],a[84:87]
G v4,s[48:51],s67,3072
M a[88:91],v[132:135],v[84:87],a[88:91]
s_add_u32 m0,s68,90112
T v[128:129],v10,4096
M a[92:95],v[132:135],v[88:91],a[92:95]
s_add_u32 s66,s71,581120
T v[130:131],v10,6144
M a[96:99],v[136:139],v[80:83],a[96:99]
T v[132:133],v10,4104
M a[100:103],v[136:139],v[84:87],a[100:103]
T v[134:135],v10,6152
M a[104:107],v[136:139],v[88:91],a[104:107]
T v[136:137],v9,8192
M a[108:111],v[140:143],v[80:83],a[108:111]
T v[138:139],v9,10240
M a[112:115],v[140:143],v[84:87],a[112:115]
G v1,s[48:51],s66,0
M a[116:119],v[140:143],v[88:91],a[116:119]
T v[140:141],v9,8200
M a[120:123],v[144:147],v[80:83],a[120:123]
T v[142:143],v9,10248
M a[124:127],v[144:147],v[84:87],a[124:127]
G v2,s[48:51],s66,1024
M a[128:131],v[144:147],v[88:91],a[128:131]
s_add_u32 s67,s66,2304
T v[144:145],v10,8192
M a[132:135],v[148:151],v[80:83],a[132:135]
T v[146:147],v10,10240
M a[136:139],v[148:151],v[84:87],a[136:139]
G v3,s[48:51],s67,2048
M a[140:143],v[148:151],v[88:91],a[140:143]
T v[148:149],v10,8200
M a[144:147],v[152:155],v[80:83],a[144:147]
T v[150:151],v10,10248
M a[148:151],v[152:155],v[84:87],a[148:151]
G v4,s[48:51],s67,3072
M a[152:155],v[152:155],v[88:91],a[152:155]
s_add_u32 m0,s69,73728
T v[152:153],v9,12288
M a[156:159],v[156:159],v[80:83],a[156:159]
T v[154:155],v9,14336
M a[160:163],v[156:159],v[84:87],a[160:163]
s_mov_b32 s66,580608
M a[164:167],v[156:159],v[88:91],a[164:167]
T v[156:157],v9,12296
M a[168:171],v[160:163],v[80:83],a[168:171]
T v[158:159],v9,14344
M a[172:175],v[160:163],v[84:87],a[172:175]
G v14,s[48:51],s66,0
M a[176:179],v[160:163],v[88:91],a[176:179]
T v[160:161],v10,12288
M a[180:183],v[164:167],v[80:83],a[180:183]
T v[162:163],v10,14336
M a[184:187],v[164:167],v[84:87],a[184:187]
M a[188:191],v[164:167],v[88:91],a[188:191]
T v[164:165],v10,12296
M a[192:195],v[168:171],v[80:83],a[192:195]
T v[166:167],v10,14344
M a[196:199],v[168:171],v[84:87],a[196:199]
M a[200:203],v[168:171],v[88:91],a[200:203]
T v[168:169],v9,16384
M a[204:207],v[172:175],v[80:83],a[204:207]
WL 5
T v[170:171],v9,18432
M a[208:211],v[172:175],v[84:87],a[208:211]
buffer_load_dwordx4 v[80:83],v0,s[44:47],s56 offen offset:0 sc1
M a[212:215],v[172:175],v[88:91],a[212:215]
buffer_load_dwordx4 v[84:87],v0,s[44:47],s57 offen offset:0 sc1
M a[0:3],v[104:107],v[92:95],a[0:3]
T v[172:173],v9,16392
M a[4:7],v[104:107],v[96:99],a[4:7]
T v[174:175],v9,18440
v_add_u32 v9,4294856704,v9
M a[8:11],v[104:107],v[100:103],a[8:11]
buffer_load_dwordx4 v[88:91],v0,s[44:47],s58 offen offset:0 sc1
M a[12:15],v[108:111],v[92:95],a[12:15]
v_add_u32 v10,4294856704,v10
M a[16:19],v[108:111],v[96:99],a[16:19]
M a[20:23],v[108:111],v[100:103],a[20:23]
s_waitcnt vmcnt(27) lgkmcnt(0)
s_barrier
M a[24:27],v[112:115],v[92:95],a[24:27]
s_add_u32 m0,s68,110592
T v[104:105],v9,0
M a[28:31],v[112:115],v[96:99],a[28:31]
s_add_u32 s66,s71,622080
T v[106:107],v9,2048
M a[32:35],v[112:115],v[100:103],a[32:35]
T v[108:109],v9,8
M a[36:39],v[116:119],v[92:95],a[36:39]
T v[110:111],v9,2056
M a[40:43],v[116:119],v[96:99],a[40:43]
G v1,s[48:51],s66,0
M a[44:47],v[116:119],v[100:103],a[44:47]
T v[112:113],v10,0
M a[48:51],v[120:123],v[92:95],a[48:51]
T v[114:115],v10,2048
M a[52:55],v[120:123],v[96:99],a[52:55]
G v2,s[48:51],s66,1024
M a[56:59],v[120:123],v[100:103],a[56:59]
s_add_u32 s67,s66,2304
T v[116:117],v10,8
M a[60:63],v[124:127],v[92:95],a[60:63]
T v[118:119],v10,2056
M a[64:67],v[124:127],v[96:99],a[64:67]
T v[120:121],v9,4096
M a[68:71],v[124:127],v[100:103],a[68:71]
T v[122:123],v9,6144
M a[72:75],v[128:131],v[92:95],a[72:75]
G v3,s[48:51],s67,2048
M a[76:79],v[128:131],v[96:99],a[76:79]
T v[124:125],v9,4104
M a[80:83],v[128:131],v[100:103],a[80:83]
T v[126:127],v9,6152
M a[84:87],v[132:135],v[92:95],a[84:87]
G v4,s[48:51],s67,3072
M a[88:91],v[132:135],v[96:99],a[88:91]
s_add_u32 m0,s68,126976
T v[128:129],v10,4096
M a[92:95],v[132:135],v[100:103],a[92:95]
s_add_u32 s66,s71,622592
T v[130:131],v10,6144
M a[96:99],v[136:139],v[92:95],a[96:99]
T v[132:133],v10,4104
M a[100:103],v[136:139],v[96:99],a[100:103]
T v[134:135],v10,6152
M a[104:107],v[136:139],v[100:103],a[104:107]
T v[136:137],v9,8192
M a[108:111],v[140:143],v[92:95],a[108:111]
T v[138:139],v9,10240
M a[112:115],v[140:143],v[96:99],a[112:115]
G v1,s[48:51],s66,0
M a[116:119],v[140:143],v[100:103],a[116:119]
T v[140:141],v9,8200
M a[120:123],v[144:147],v[92:95],a[120:123]
T v[142:143],v9,10248
M a[124:127],v[144:147],v[96:99],a[124:127]
G v2,s[48:51],s66,1024
M a[128:131],v[144:147],v[100:103],a[128:131]
s_add_u32 s67,s66,2304
T v[144:145],v10,8192
M a[132:135],v[148:151],v[92:95],a[132:135]
T v[146:147],v10,10240
M a[136:139],v[148:151],v[96:99],a[136:139]
G v3,s[48:51],s67,2048
M a[140:143],v[148:151],v[100:103],a[140:143]
T v[148:149],v10,8200
M a[144:147],v[152:155],v[92:95],a[144:147]
T v[150:151],v10,10248
M a[148:151],v[152:155],v[96:99],a[148:151]
G v4,s[48:51],s67,3072
M a[152:155],v[152:155],v[100:103],a[152:155]
s_add_u32 m0,s69,110592
T v[152:153],v9,12288
M a[156:159],v[156:159],v[92:95],a[156:159]
T v[154:155],v9,14336
M a[160:163],v[156:159],v[96:99],a[160:163]
s_mov_b32 s66,622080
M a[164:167],v[156:159],v[100:103],a[164:167]
T v[156:157],v9,12296
M a[168:171],v[160:163],v[92:95],a[168:171]
T v[158:159],v9,14344
M a[172:175],v[160:163],v[96:99],a[172:175]
G v14,s[48:51],s66,0
M a[176:179],v[160:163],v[100:103],a[176:179]
T v[160:161],v10,12288
M a[180:183],v[164:167],v[92:95],a[180:183]
T v[162:163],v10,14336
M a[184:187],v[164:167],v[96:99],a[184:187]
M a[188:191],v[164:167],v[100:103],a[188:191]
T v[164:165],v10,12296
M a[192:195],v[168:171],v[92:95],a[192:195]
T v[166:167],v10,14344
M a[196:199],v[168:171],v[96:99],a[196:199]
M a[200:203],v[168:171],v[100:103],a[200:203]
T v[168:169],v9,16384
M a[204:207],v[172:175],v[92:95],a[204:207]
WL 5
T v[170:171],v9,18432
M a[208:211],v[172:175],v[96:99],a[208:211]
buffer_load_dwordx4 v[92:95],v0,s[44:47],s56 offen offset:1024 sc1
M a[212:215],v[172:175],v[100:103],a[212:215]
buffer_load_dwordx4 v[96:99],v0,s[44:47],s57 offen offset:1024 sc1
M a[0:3],v[104:107],v[32:35],a[0:3]
T v[172:173],v9,16392
M a[4:7],v[104:107],v[36:39],a[4:7]
T v[174:175],v9,18440
v_add_u32 v9,36864,v9
M a[8:11],v[104:107],v[40:43],a[8:11]
buffer_load_dwordx4 v[100:103],v0,s[44:47],s58 offen offset:1024 sc1
M a[12:15],v[108:111],v[32:35],a[12:15]
v_add_u32 v10,36864,v10
M a[16:19],v[108:111],v[36:39],a[16:19]
M a[20:23],v[108:111],v[40:43],a[20:23]
s_waitcnt vmcnt(27) lgkmcnt(0)
s_barrier
M a[24:27],v[112:115],v[32:35],a[24:27]
s_add_u32 m0,s68,0
T v[104:105],v9,0
M a[28:31],v[112:115],v[36:39],a[28:31]
s_add_u32 s66,s71,0
T v[106:107],v9,2048
M a[32:35],v[112:115],v[40:43],a[32:35]
T v[108:109],v9,8
M a[36:39],v[116:119],v[32:35],a[36:39]
T v[110:111],v9,2056
M a[40:43],v[116:119],v[36:39],a[40:43]
T v[112:113],v10,0
M a[44:47],v[116:119],v[40:43],a[44:47]
T v[114:115],v10,2048
M a[48:51],v[120:123],v[32:35],a[48:51]
G v5,s[48:51],s66,0
M a[52:55],v[120:123],v[36:39],a[52:55]
T v[116:117],v10,8
M a[56:59],v[120:123],v[40:43],a[56:59]
T v[118:119],v10,2056
M a[60:63],v[124:127],v[32:35],a[60:63]
G v6,s[48:51],s66,1024
M a[64:67],v[124:127],v[36:39],a[64:67]
T v[120:121],v9,4096
M a[68:71],v[124:127],v[40:43],a[68:71]
T v[122:123],v9,6144
M a[72:75],v[128:131],v[32:35],a[72:75]
G v7,s[48:51],s66,2048
M a[76:79],v[128:131],v[36:39],a[76:79]
T v[124:125],v9,4104
M a[80:83],v[128:131],v[40:43],a[80:83]
T v[126:127],v9,6152
M a[84:87],v[132:135],v[32:35],a[84:87]
G v8,s[48:51],s66,3072
M a[88:91],v[132:135],v[36:39],a[88:91]
s_add_u32 m0,s68,16384
T v[128:129],v10,4096
M a[92:95],v[132:135],v[40:43],a[92:95]
s_add_u32 s66,s71,512
T v[130:131],v10,6144
M a[96:99],v[136:139],v[32:35],a[96:99]
T v[132:133],v10,4104
M a[100:103],v[136:139],v[36:39],a[100:103]
T v[134:135],v10,6152
M a[104:107],v[136:139],v[40:43],a[104:107]
T v[136:137],v9,8192
M a[108:111],v[140:143],v[32:35],a[108:111]
T v[138:139],v9,10240
M a[112:115],v[140:143],v[36:39],a[112:115]
G v5,s[48:51],s66,0
M a[116:119],v[140:143],v[40:43],a[116:119]
T v[140:141],v9,8200
M a[120:123],v[144:147],v[32:35],a[120:123]
T v[142:143],v9,10248
M a[124:127],v[144:147],v[36:39],a[124:127]
G v6,s[48:51],s66,1024
M a[128:131],v[144:147],v[40:43],a[128:131]
T v[144:145],v10,8192
M a[132:135],v[148:151],v[32:35],a[132:135]
T v[146:147],v10,10240
M a[136:139],v[148:151],v[36:39],a[136:139]
G v7,s[48:51],s66,2048
M a[140:143],v[148:151],v[40:43],a[140:143]
T v[148:149],v10,8200
M a[144:147],v[152:155],v[32:35],a[144:147]
T v[150:151],v10,10248
M a[148:151],v[152:155],v[36:39],a[148:151]
G v8,s[48:51],s66,3072
M a[152:155],v[152:155],v[40:43],a[152:155]
s_add_u32 m0,s69,0
T v[152:153],v9,12288
M a[156:159],v[156:159],v[32:35],a[156:159]
T v[154:155],v9,14336
M a[160:163],v[156:159],v[36:39],a[160:163]
s_mov_b32 s66,0
M a[164:167],v[156:159],v[40:43],a[164:167]
T v[156:157],v9,12296
M a[168:171],v[160:163],v[32:35],a[168:171]
T v[158:159],v9,14344
M a[172:175],v[160:163],v[36:39],a[172:175]
)"
R"(G v15,s[48:51],s66,0
M a[176:179],v[160:163],v[40:43],a[176:179]
T v[160:161],v10,12288
M a[180:183],v[164:167],v[32:35],a[180:183]
T v[162:163],v10,14336
M a[184:187],v[164:167],v[36:39],a[184:187]
M a[188:191],v[164:167],v[40:43],a[188:191]
T v[164:165],v10,12296
M a[192:195],v[168:171],v[32:35],a[192:195]
T v[166:167],v10,14344
M a[196:199],v[168:171],v[36:39],a[196:199]
M a[200:203],v[168:171],v[40:43],a[200:203]
T v[168:169],v9,16384
M a[204:207],v[172:175],v[32:35],a[204:207]
WL 5
T v[170:171],v9,18432
M a[208:211],v[172:175],v[36:39],a[208:211]
M a[212:215],v[172:175],v[40:43],a[212:215]
T v[172:173],v9,16392
M a[0:3],v[104:107],v[44:47],a[0:3]
T v[174:175],v9,18440
v_add_u32 v9,36864,v9
M a[4:7],v[104:107],v[48:51],a[4:7]
v_add_u32 v10,36864,v10
M a[8:11],v[104:107],v[52:55],a[8:11]
M a[12:15],v[108:111],v[44:47],a[12:15]
M a[16:19],v[108:111],v[48:51],a[16:19]
M a[20:23],v[108:111],v[52:55],a[20:23]
s_waitcnt vmcnt(24) lgkmcnt(0)
s_barrier
M a[24:27],v[112:115],v[44:47],a[24:27]
s_add_u32 m0,s68,36864
T v[104:105],v9,0
M a[28:31],v[112:115],v[48:51],a[28:31]
s_add_u32 s66,s71,331776
T v[106:107],v9,2048
M a[32:35],v[112:115],v[52:55],a[32:35]
T v[108:109],v9,8
M a[36:39],v[116:119],v[44:47],a[36:39]
T v[110:111],v9,2056
M a[40:43],v[116:119],v[48:51],a[40:43]
T v[112:113],v10,0
M a[44:47],v[116:119],v[52:55],a[44:47]
T v[114:115],v10,2048
M a[48:51],v[120:123],v[44:47],a[48:51]
G v5,s[48:51],s66,0
M a[52:55],v[120:123],v[48:51],a[52:55]
T v[116:117],v10,8
M a[56:59],v[120:123],v[52:55],a[56:59]
T v[118:119],v10,2056
M a[60:63],v[124:127],v[44:47],a[60:63]
G v6,s[48:51],s66,1024
M a[64:67],v[124:127],v[48:51],a[64:67]
T v[120:121],v9,4096
M a[68:71],v[124:127],v[52:55],a[68:71]
T v[122:123],v9,6144
M a[72:75],v[128:131],v[44:47],a[72:75]
G v7,s[48:51],s66,2048
M a[76:79],v[128:131],v[48:51],a[76:79]
T v[124:125],v9,4104
M a[80:83],v[128:131],v[52:55],a[80:83]
T v[126:127],v9,6152
M a[84:87],v[132:135],v[44:47],a[84:87]
G v8,s[48:51],s66,3072
M a[88:91],v[132:135],v[48:51],a[88:91]
s_add_u32 m0,s68,53248
T v[128:129],v10,4096
M a[92:95],v[132:135],v[52:55],a[92:95]
s_add_u32 s66,s71,332288
T v[130:131],v10,6144
M a[96:99],v[136:139],v[44:47],a[96:99]
T v[132:133],v10,4104
M a[100:103],v[136:139],v[48:51],a[100:103]
T v[134:135],v10,6152
M a[104:107],v[136:139],v[52:55],a[104:107]
T v[136:137],v9,8192
M a[108:111],v[140:143],v[44:47],a[108:111]
T v[138:139],v9,10240
M a[112:115],v[140:143],v[48:51],a[112:115]
G v5,s[48:51],s66,0
M a[116:119],v[140:143],v[52:55],a[116:119]
T v[140:141],v9,8200
M a[120:123],v[144:147],v[44:47],a[120:123]
T v[142:143],v9,10248
M a[124:127],v[144:147],v[48:51],a[124:127]
G v6,s[48:51],s66,1024
M a[128:131],v[144:147],v[52:55],a[128:131]
T v[144:145],v10,8192
M a[132:135],v[148:151],v[44:47],a[132:135]
T v[146:147],v10,10240
M a[136:139],v[148:151],v[48:51],a[136:139]
G v7,s[48:51],s66,2048
M a[140:143],v[148:151],v[52:55],a[140:143]
T v[148:149],v10,8200
M a[144:147],v[152:155],v[44:47],a[144:147]
T v[150:151],v10,10248
M a[148:151],v[152:155],v[48:51],a[148:151]
G v8,s[48:51],s66,3072
M a[152:155],v[152:155],v[52:55],a[152:155]
s_add_u32 m0,s69,36864
T v[152:153],v9,12288
M a[156:159],v[156:159],v[44:47],a[156:159]
T v[154:155],v9,14336
M a[160:163],v[156:159],v[48:51],a[160:163]
s_mov_b32 s66,331776
M a[164:167],v[156:159],v[52:55],a[164:167]
T v[156:157],v9,12296
M a[168:171],v[160:163],v[44:47],a[168:171]
T v[158:159],v9,14344
M a[172:175],v[160:163],v[48:51],a[172:175]
G v15,s[48:51],s66,0
M a[176:179],v[160:163],v[52:55],a[176:179]
T v[160:161],v10,12288
M a[180:183],v[164:167],v[44:47],a[180:183]
T v[162:163],v10,14336
M a[184:187],v[164:167],v[48:51],a[184:187]
M a[188:191],v[164:167],v[52:55],a[188:191]
T v[164:165],v10,12296
M a[192:195],v[168:171],v[44:47],a[192:195]
T v[166:167],v10,14344
M a[196:199],v[168:171],v[48:51],a[196:199]
M a[200:203],v[168:171],v[52:55],a[200:203]
T v[168:169],v9,16384
M a[204:207],v[172:175],v[44:47],a[204:207]
WL 5
T v[170:171],v9,18432
M a[208:211],v[172:175],v[48:51],a[208:211]
M a[212:215],v[172:175],v[52:55],a[212:215]
T v[172:173],v9,16392
M a[0:3],v[104:107],v[56:59],a[0:3]
T v[174:175],v9,18440
v_add_u32 v9,36864,v9
M a[4:7],v[104:107],v[60:63],a[4:7]
v_add_u32 v10,36864,v10
M a[8:11],v[104:107],v[64:67],a[8:11]
M a[12:15],v[108:111],v[56:59],a[12:15]
M a[16:19],v[108:111],v[60:63],a[16:19]
M a[20:23],v[108:111],v[64:67],a[20:23]
s_waitcnt vmcnt(21) lgkmcnt(0)
s_barrier
M a[24:27],v[112:115],v[56:59],a[24:27]
T v[104:105],v9,0
M a[28:31],v[112:115],v[60:63],a[28:31]
T v[106:107],v9,2048
M a[32:35],v[112:115],v[64:67],a[32:35]
T v[108:109],v9,8
M a[36:39],v[116:119],v[56:59],a[36:39]
T v[110:111],v9,2056
M a[40:43],v[116:119],v[60:63],a[40:43]
T v[112:113],v10,0
M a[44:47],v[116:119],v[64:67],a[44:47]
T v[114:115],v10,2048
M a[48:51],v[120:123],v[56:59],a[48:51]
T v[116:117],v10,8
M a[52:55],v[120:123],v[60:63],a[52:55]
T v[118:119],v10,2056
M a[56:59],v[120:123],v[64:67],a[56:59]
T v[120:121],v9,4096
M a[60:63],v[124:127],v[56:59],a[60:63]
T v[122:123],v9,6144
M a[64:67],v[124:127],v[60:63],a[64:67]
M a[68:71],v[124:127],v[64:67],a[68:71]
T v[124:125],v9,4104
M a[72:75],v[128:131],v[56:59],a[72:75]
T v[126:127],v9,6152
M a[76:79],v[128:131],v[60:63],a[76:79]
M a[80:83],v[128:131],v[64:67],a[80:83]
T v[128:129],v10,4096
M a[84:87],v[132:135],v[56:59],a[84:87]
T v[130:131],v10,6144
M a[88:91],v[132:135],v[60:63],a[88:91]
M a[92:95],v[132:135],v[64:67],a[92:95]
T v[132:133],v10,4104
M a[96:99],v[136:139],v[56:59],a[96:99]
T v[134:135],v10,6152
M a[100:103],v[136:139],v[60:63],a[100:103]
M a[104:107],v[136:139],v[64:67],a[104:107]
T v[136:137],v9,8192
M a[108:111],v[140:143],v[56:59],a[108:111]
T v[138:139],v9,10240
M a[112:115],v[140:143],v[60:63],a[112:115]
M a[116:119],v[140:143],v[64:67],a[116:119]
T v[140:141],v9,8200
M a[120:123],v[144:147],v[56:59],a[120:123]
T v[142:143],v9,10248
M a[124:127],v[144:147],v[60:63],a[124:127]
M a[128:131],v[144:147],v[64:67],a[128:131]
T v[144:145],v10,8192
M a[132:135],v[148:151],v[56:59],a[132:135]
T v[146:147],v10,10240
M a[136:139],v[148:151],v[60:63],a[136:139]
M a[140:143],v[148:151],v[64:67],a[140:143]
T v[148:149],v10,8200
M a[144:147],v[152:155],v[56:59],a[144:147]
T v[150:151],v10,10248
M a[148:151],v[152:155],v[60:63],a[148:151]
M a[152:155],v[152:155],v[64:67],a[152:155]
T v[152:153],v9,12288
M a[156:159],v[156:159],v[56:59],a[156:159]
T v[154:155],v9,14336
M a[160:163],v[156:159],v[60:63],a[160:163]
M a[164:167],v[156:159],v[64:67],a[164:167]
T v[156:157],v9,12296
M a[168:171],v[160:163],v[56:59],a[168:171]
T v[158:159],v9,14344
M a[172:175],v[160:163],v[60:63],a[172:175]
M a[176:179],v[160:163],v[64:67],a[176:179]
T v[160:161],v10,12288
M a[180:183],v[164:167],v[56:59],a[180:183]
T v[162:163],v10,14336
M a[184:187],v[164:167],v[60:63],a[184:187]
M a[188:191],v[164:167],v[64:67],a[188:191]
T v[164:165],v10,12296
M a[192:195],v[168:171],v[56:59],a[192:195]
T v[166:167],v10,14344
M a[196:199],v[168:171],v[60:63],a[196:199]
M a[200:203],v[168:171],v[64:67],a[200:203]
T v[168:169],v9,16384
M a[204:207],v[172:175],v[56:59],a[204:207]
WL 5
T v[170:171],v9,18432
M a[208:211],v[172:175],v[60:63],a[208:211]
M a[212:215],v[172:175],v[64:67],a[212:215]
T v[172:173],v9,16392
M a[0:3],v[104:107],v[68:71],a[0:3]
T v[174:175],v9,18440
v_add_u32 v9,4294856704,v9
M a[4:7],v[104:107],v[72:75],a[4:7]
v_add_u32 v10,4294856704,v10
M a[8:11],v[104:107],v[76:79],a[8:11]
M a[12:15],v[108:111],v[68:71],a[12:15]
M a[16:19],v[108:111],v[72:75],a[16:19]
M a[20:23],v[108:111],v[76:79],a[20:23]
s_waitcnt vmcnt(9) lgkmcnt(0)
s_barrier
M a[24:27],v[112:115],v[68:71],a[24:27]
T v[104:105],v9,0
M a[28:31],v[112:115],v[72:75],a[28:31]
T v[106:107],v9,2048
M a[32:35],v[112:115],v[76:79],a[32:35]
T v[108:109],v9,8
M a[36:39],v[116:119],v[68:71],a[36:39]
T v[110:111],v9,2056
M a[40:43],v[116:119],v[72:75],a[40:43]
T v[112:113],v10,0
M a[44:47],v[116:119],v[76:79],a[44:47]
T v[114:115],v10,2048
M a[48:51],v[120:123],v[68:71],a[48:51]
T v[116:117],v10,8
M a[52:55],v[120:123],v[72:75],a[52:55]
T v[118:119],v10,2056
M a[56:59],v[120:123],v[76:79],a[56:59]
T v[120:121],v9,4096
M a[60:63],v[124:127],v[68:71],a[60:63]
T v[122:123],v9,6144
M a[64:67],v[124:127],v[72:75],a[64:67]
M a[68:71],v[124:127],v[76:79],a[68:71]
T v[124:125],v9,4104
M a[72:75],v[128:131],v[68:71],a[72:75]
T v[126:127],v9,6152
M a[76:79],v[128:131],v[72:75],a[76:79]
M a[80:83],v[128:131],v[76:79],a[80:83]
T v[128:129],v10,4096
M a[84:87],v[132:135],v[68:71],a[84:87]
T v[130:131],v10,6144
M a[88:91],v[132:135],v[72:75],a[88:91]
M a[92:95],v[132:135],v[76:79],a[92:95]
T v[132:133],v10,4104
M a[96:99],v[136:139],v[68:71],a[96:99]
T v[134:135],v10,6152
M a[100:103],v[136:139],v[72:75],a[100:103]
M a[104:107],v[136:139],v[76:79],a[104:107]
T v[136:137],v9,8192
M a[108:111],v[140:143],v[68:71],a[108:111]
T v[138:139],v9,10240
M a[112:115],v[140:143],v[72:75],a[112:115]
M a[116:119],v[140:143],v[76:79],a[116:119]
T v[140:141],v9,8200
M a[120:123],v[144:147],v[68:71],a[120:123]
T v[142:143],v9,10248
M a[124:127],v[144:147],v[72:75],a[124:127]
M a[128:131],v[144:147],v[76:79],a[128:131]
T v[144:145],v10,8192
M a[132:135],v[148:151],v[68:71],a[132:135]
T v[146:147],v10,10240
M a[136:139],v[148:151],v[72:75],a[136:139]
M a[140:143],v[148:151],v[76:79],a[140:143]
T v[148:149],v10,8200
M a[144:147],v[152:155],v[68:71],a[144:147]
T v[150:151],v10,10248
M a[148:151],v[152:155],v[72:75],a[148:151]
M a[152:155],v[152:155],v[76:79],a[152:155]
T v[152:153],v9,12288
M a[156:159],v[156:159],v[68:71],a[156:159]
T v[154:155],v9,14336
M a[160:163],v[156:159],v[72:75],a[160:163]
M a[164:167],v[156:159],v[76:79],a[164:167]
T v[156:157],v9,12296
M a[168:171],v[160:163],v[68:71],a[168:171]
T v[158:159],v9,14344
M a[172:175],v[160:163],v[72:75],a[172:175]
M a[176:179],v[160:163],v[76:79],a[176:179]
T v[160:161],v10,12288
M a[180:183],v[164:167],v[68:71],a[180:183]
T v[162:163],v10,14336
M a[184:187],v[164:167],v[72:75],a[184:187]
M a[188:191],v[164:167],v[76:79],a[188:191]
T v[164:165],v10,12296
M a[192:195],v[168:171],v[68:71],a[192:195]
T v[166:167],v10,14344
M a[196:199],v[168:171],v[72:75],a[196:199]
M a[200:203],v[168:171],v[76:79],a[200:203]
T v[168:169],v9,16384
M a[204:207],v[172:175],v[68:71],a[204:207]
WL 5
T v[170:171],v9,18432
M a[208:211],v[172:175],v[72:75],a[208:211]
M a[212:215],v[172:175],v[76:79],a[212:215]
T v[172:173],v9,16392
M a[0:3],v[104:107],v[80:83],a[0:3]
T v[174:175],v9,18440
v_add_u32 v9,36864,v9
M a[4:7],v[104:107],v[84:87],a[4:7]
v_add_u32 v10,36864,v10
M a[8:11],v[104:107],v[88:91],a[8:11]
M a[12:15],v[108:111],v[80:83],a[12:15]
M a[16:19],v[108:111],v[84:87],a[16:19]
M a[20:23],v[108:111],v[88:91],a[20:23]
s_waitcnt vmcnt(0) lgkmcnt(0)
s_barrier
M a[24:27],v[112:115],v[80:83],a[24:27]
T v[104:105],v9,0
M a[28:31],v[112:115],v[84:87],a[28:31]
T v[106:107],v9,2048
M a[32:35],v[112:115],v[88:91],a[32:35]
T v[108:109],v9,8
M a[36:39],v[116:119],v[80:83],a[36:39]
T v[110:111],v9,2056
M a[40:43],v[116:119],v[84:87],a[40:43]
T v[112:113],v10,0
M a[44:47],v[116:119],v[88:91],a[44:47]
T v[114:115],v10,2048
M a[48:51],v[120:123],v[80:83],a[48:51]
T v[116:117],v10,8
M a[52:55],v[120:123],v[84:87],a[52:55]
T v[118:119],v10,2056
M a[56:59],v[120:123],v[88:91],a[56:59]
T v[120:121],v9,4096
M a[60:63],v[124:127],v[80:83],a[60:63]
T v[122:123],v9,6144
M a[64:67],v[124:127],v[84:87],a[64:67]
M a[68:71],v[124:127],v[88:91],a[68:71]
T v[124:125],v9,4104
M a[72:75],v[128:131],v[80:83],a[72:75]
T v[126:127],v9,6152
M a[76:79],v[128:131],v[84:87],a[76:79]
M a[80:83],v[128:131],v[88:91],a[80:83]
T v[128:129],v10,4096
M a[84:87],v[132:135],v[80:83],a[84:87]
T v[130:131],v10,6144
M a[88:91],v[132:135],v[84:87],a[88:91]
M a[92:95],v[132:135],v[88:91],a[92:95]
T v[132:133],v10,4104
M a[96:99],v[136:139],v[80:83],a[96:99]
T v[134:135],v10,6152
M a[100:103],v[136:139],v[84:87],a[100:103]
M a[104:107],v[136:139],v[88:91],a[104:107]
T v[136:137],v9,8192
M a[108:111],v[140:143],v[80:83],a[108:111]
T v[138:139],v9,10240
M a[112:115],v[140:143],v[84:87],a[112:115]
M a[116:119],v[140:143],v[88:91],a[116:119]
T v[140:141],v9,8200
M a[120:123],v[144:147],v[80:83],a[120:123]
T v[142:143],v9,10248
M a[124:127],v[144:147],v[84:87],a[124:127]
M a[128:131],v[144:147],v[88:91],a[128:131]
T v[144:145],v10,8192
M a[132:135],v[148:151],v[80:83],a[132:135]
T v[146:147],v10,10240
M a[136:139],v[148:151],v[84:87],a[136:139]
M a[140:143],v[148:151],v[88:91],a[140:143]
T v[148:149],v10,8200
M a[144:147],v[152:155],v[80:83],a[144:147]
T v[150:151],v10,10248
M a[148:151],v[152:155],v[84:87],a[148:151]
M a[152:155],v[152:155],v[88:91],a[152:155]
T v[152:153],v9,12288
M a[156:159],v[156:159],v[80:83],a[156:159]
T v[154:155],v9,14336
M a[160:163],v[156:159],v[84:87],a[160:163]
M a[164:167],v[156:159],v[88:91],a[164:167]
T v[156:157],v9,12296
M a[168:171],v[160:163],v[80:83],a[168:171]
T v[158:159],v9,14344
M a[172:175],v[160:163],v[84:87],a[172:175]
M a[176:179],v[160:163],v[88:91],a[176:179]
T v[160:161],v10,12288
M a[180:183],v[164:167],v[80:83],a[180:183]
T v[162:163],v10,14336
)"
R"(M a[184:187],v[164:167],v[84:87],a[184:187]
M a[188:191],v[164:167],v[88:91],a[188:191]
T v[164:165],v10,12296
M a[192:195],v[168:171],v[80:83],a[192:195]
T v[166:167],v10,14344
M a[196:199],v[168:171],v[84:87],a[196:199]
M a[200:203],v[168:171],v[88:91],a[200:203]
T v[168:169],v9,16384
M a[204:207],v[172:175],v[80:83],a[204:207]
WL 5
T v[170:171],v9,18432
M a[208:211],v[172:175],v[84:87],a[208:211]
M a[212:215],v[172:175],v[88:91],a[212:215]
T v[172:173],v9,16392
M a[0:3],v[104:107],v[92:95],a[0:3]
T v[174:175],v9,18440
M a[4:7],v[104:107],v[96:99],a[4:7]
M a[8:11],v[104:107],v[100:103],a[8:11]
M a[12:15],v[108:111],v[92:95],a[12:15]
M a[16:19],v[108:111],v[96:99],a[16:19]
M a[20:23],v[108:111],v[100:103],a[20:23]
M a[24:27],v[112:115],v[92:95],a[24:27]
M a[28:31],v[112:115],v[96:99],a[28:31]
M a[32:35],v[112:115],v[100:103],a[32:35]
M a[36:39],v[116:119],v[92:95],a[36:39]
M a[40:43],v[116:119],v[96:99],a[40:43]
M a[44:47],v[116:119],v[100:103],a[44:47]
M a[48:51],v[120:123],v[92:95],a[48:51]
M a[52:55],v[120:123],v[96:99],a[52:55]
M a[56:59],v[120:123],v[100:103],a[56:59]
M a[60:63],v[124:127],v[92:95],a[60:63]
M a[64:67],v[124:127],v[96:99],a[64:67]
M a[68:71],v[124:127],v[100:103],a[68:71]
M a[72:75],v[128:131],v[92:95],a[72:75]
M a[76:79],v[128:131],v[96:99],a[76:79]
M a[80:83],v[128:131],v[100:103],a[80:83]
M a[84:87],v[132:135],v[92:95],a[84:87]
M a[88:91],v[132:135],v[96:99],a[88:91]
M a[92:95],v[132:135],v[100:103],a[92:95]
M a[96:99],v[136:139],v[92:95],a[96:99]
M a[100:103],v[136:139],v[96:99],a[100:103]
M a[104:107],v[136:139],v[100:103],a[104:107]
M a[108:111],v[140:143],v[92:95],a[108:111]
M a[112:115],v[140:143],v[96:99],a[112:115]
M a[116:119],v[140:143],v[100:103],a[116:119]
M a[120:123],v[144:147],v[92:95],a[120:123]
M a[124:127],v[144:147],v[96:99],a[124:127]
M a[128:131],v[144:147],v[100:103],a[128:131]
M a[132:135],v[148:151],v[92:95],a[132:135]
M a[136:139],v[148:151],v[96:99],a[136:139]
M a[140:143],v[148:151],v[100:103],a[140:143]
M a[144:147],v[152:155],v[92:95],a[144:147]
M a[148:151],v[152:155],v[96:99],a[148:151]
M a[152:155],v[152:155],v[100:103],a[152:155]
M a[156:159],v[156:159],v[92:95],a[156:159]
WL 0
M a[160:163],v[156:159],v[96:99],a[160:163]
M a[164:167],v[156:159],v[100:103],a[164:167]
M a[168:171],v[160:163],v[92:95],a[168:171]
M a[172:175],v[160:163],v[96:99],a[172:175]
M a[176:179],v[160:163],v[100:103],a[176:179]
M a[180:183],v[164:167],v[92:95],a[180:183]
M a[184:187],v[164:167],v[96:99],a[184:187]
M a[188:191],v[164:167],v[100:103],a[188:191]
M a[192:195],v[168:171],v[92:95],a[192:195]
M a[196:199],v[168:171],v[96:99],a[196:199]
M a[200:203],v[168:171],v[100:103],a[200:203]
M a[204:207],v[172:175],v[92:95],a[204:207]
M a[208:211],v[172:175],v[96:99],a[208:211]
M a[212:215],v[172:175],v[100:103],a[212:215]
s_barrier
AR v32,a0
AR v33,a1
AR v34,a2
AR v35,a3
AR v36,a12
AR v37,a13
AR v38,a14
AR v39,a15
C v32,v32,v33
C v33,v34,v35
C v34,v36,v37
C v35,v38,v39
ds_write_b128 v194,v[32:35] offset:0
AR v40,a24
AR v41,a25
AR v42,a26
AR v43,a27
AR v44,a36
AR v45,a37
AR v46,a38
AR v47,a39
C v40,v40,v41
C v41,v42,v43
C v42,v44,v45
C v43,v46,v47
ds_write_b128 v194,v[40:43] offset:64
AR v48,a48
AR v49,a49
AR v50,a50
AR v51,a51
AR v52,a60
AR v53,a61
AR v54,a62
AR v55,a63
C v48,v48,v49
C v49,v50,v51
C v50,v52,v53
C v51,v54,v55
ds_write_b128 v194,v[48:51] offset:128
AR v56,a72
AR v57,a73
AR v58,a74
AR v59,a75
AR v60,a84
AR v61,a85
AR v62,a86
AR v63,a87
C v56,v56,v57
C v57,v58,v59
C v58,v60,v61
C v59,v62,v63
ds_write_b128 v194,v[56:59] offset:192
AR v32,a96
AR v33,a97
AR v34,a98
AR v35,a99
AR v36,a108
AR v37,a109
AR v38,a110
AR v39,a111
C v32,v32,v33
C v33,v34,v35
C v34,v36,v37
C v35,v38,v39
ds_write_b128 v194,v[32:35] offset:256
AR v40,a120
AR v41,a121
AR v42,a122
AR v43,a123
AR v44,a132
AR v45,a133
AR v46,a134
AR v47,a135
C v40,v40,v41
C v41,v42,v43
C v42,v44,v45
C v43,v46,v47
ds_write_b128 v194,v[40:43] offset:320
AR v48,a144
AR v49,a145
AR v50,a146
AR v51,a147
AR v52,a156
AR v53,a157
AR v54,a158
AR v55,a159
C v48,v48,v49
C v49,v50,v51
C v50,v52,v53
C v51,v54,v55
ds_write_b128 v194,v[48:51] offset:384
AR v56,a168
AR v57,a169
AR v58,a170
AR v59,a171
AR v60,a180
AR v61,a181
AR v62,a182
AR v63,a183
C v56,v56,v57
C v57,v58,v59
C v58,v60,v61
C v59,v62,v63
ds_write_b128 v194,v[56:59] offset:448
AR v32,a192
AR v33,a193
AR v34,a194
AR v35,a195
AR v36,a204
AR v37,a205
AR v38,a206
AR v39,a207
C v32,v32,v33
C v33,v34,v35
C v34,v36,v37
C v35,v38,v39
ds_write_b128 v194,v[32:35] offset:512
WL 0
D v[64:67],v176,0
D v[68:71],v177,0
D v[72:75],v178,0
D v[76:79],v179,0
D v[80:83],v180,0
D v[84:87],v181,0
D v[88:91],v182,0
D v[92:95],v183,0
D v[96:99],v184,0
WL 8
buffer_store_dwordx4 v[64:67],v185,s[52:55],s72 offen offset:0
WL 7
buffer_store_dwordx4 v[68:71],v186,s[52:55],s72 offen offset:0
WL 6
buffer_store_dwordx4 v[72:75],v187,s[52:55],s72 offen offset:0
WL 5
buffer_store_dwordx4 v[76:79],v188,s[52:55],s72 offen offset:0
WL 4
buffer_store_dwordx4 v[80:83],v189,s[52:55],s72 offen offset:0
WL 3
buffer_store_dwordx4 v[84:87],v190,s[52:55],s72 offen offset:0
WL 2
buffer_store_dwordx4 v[88:91],v191,s[52:55],s72 offen offset:0
WL 1
buffer_store_dwordx4 v[92:95],v192,s[52:55],s72 offen offset:0
WL 0
buffer_store_dwordx4 v[96:99],v193,s[52:55],s72 offen offset:0
s_add_u32 s72,s72,36864
AR v40,a4
AR v41,a5
AR v42,a6
AR v43,a7
AR v44,a16
AR v45,a17
AR v46,a18
AR v47,a19
C v40,v40,v41
C v41,v42,v43
C v42,v44,v45
C v43,v46,v47
ds_write_b128 v194,v[40:43] offset:9472
AR v48,a28
AR v49,a29
AR v50,a30
AR v51,a31
AR v52,a40
AR v53,a41
AR v54,a42
AR v55,a43
C v48,v48,v49
C v49,v50,v51
C v50,v52,v53
C v51,v54,v55
ds_write_b128 v194,v[48:51] offset:9536
AR v56,a52
AR v57,a53
AR v58,a54
AR v59,a55
AR v60,a64
AR v61,a65
AR v62,a66
AR v63,a67
C v56,v56,v57
C v57,v58,v59
C v58,v60,v61
C v59,v62,v63
ds_write_b128 v194,v[56:59] offset:9600
AR v32,a76
AR v33,a77
AR v34,a78
AR v35,a79
AR v36,a88
AR v37,a89
AR v38,a90
AR v39,a91
C v32,v32,v33
C v33,v34,v35
C v34,v36,v37
C v35,v38,v39
ds_write_b128 v194,v[32:35] offset:9664
AR v40,a100
AR v41,a101
AR v42,a102
AR v43,a103
AR v44,a112
AR v45,a113
AR v46,a114
AR v47,a115
C v40,v40,v41
C v41,v42,v43
C v42,v44,v45
C v43,v46,v47
ds_write_b128 v194,v[40:43] offset:9728
AR v48,a124
AR v49,a125
AR v50,a126
AR v51,a127
AR v52,a136
AR v53,a137
AR v54,a138
AR v55,a139
C v48,v48,v49
C v49,v50,v51
C v50,v52,v53
C v51,v54,v55
ds_write_b128 v194,v[48:51] offset:9792
AR v56,a148
AR v57,a149
AR v58,a150
AR v59,a151
AR v60,a160
AR v61,a161
AR v62,a162
AR v63,a163
C v56,v56,v57
C v57,v58,v59
C v58,v60,v61
C v59,v62,v63
ds_write_b128 v194,v[56:59] offset:9856
AR v32,a172
AR v33,a173
AR v34,a174
AR v35,a175
AR v36,a184
AR v37,a185
AR v38,a186
AR v39,a187
C v32,v32,v33
C v33,v34,v35
C v34,v36,v37
C v35,v38,v39
ds_write_b128 v194,v[32:35] offset:9920
AR v40,a196
AR v41,a197
AR v42,a198
AR v43,a199
AR v44,a208
AR v45,a209
AR v46,a210
AR v47,a211
C v40,v40,v41
C v41,v42,v43
C v42,v44,v45
C v43,v46,v47
ds_write_b128 v194,v[40:43] offset:9984
WL 0
D v[100:103],v176,9472
D v[104:107],v177,9472
D v[108:111],v178,9472
D v[112:115],v179,9472
D v[116:119],v180,9472
D v[120:123],v181,9472
D v[124:127],v182,9472
D v[128:131],v183,9472
D v[132:135],v184,9472
WL 8
buffer_store_dwordx4 v[100:103],v185,s[52:55],s72 offen offset:0
WL 7
buffer_store_dwordx4 v[104:107],v186,s[52:55],s72 offen offset:0
WL 6
buffer_store_dwordx4 v[108:111],v187,s[52:55],s72 offen offset:0
WL 5
buffer_store_dwordx4 v[112:115],v188,s[52:55],s72 offen offset:0
WL 4
buffer_store_dwordx4 v[116:119],v189,s[52:55],s72 offen offset:0
WL 3
buffer_store_dwordx4 v[120:123],v190,s[52:55],s72 offen offset:0
WL 2
buffer_store_dwordx4 v[124:127],v191,s[52:55],s72 offen offset:0
WL 1
buffer_store_dwordx4 v[128:131],v192,s[52:55],s72 offen offset:0
WL 0
buffer_store_dwordx4 v[132:135],v193,s[52:55],s72 offen offset:0
s_add_u32 s72,s72,36864
AR v48,a8
AR v49,a9
AR v50,a10
AR v51,a11
AR v52,a20
AR v53,a21
AR v54,a22
AR v55,a23
C v48,v48,v49
C v49,v50,v51
C v50,v52,v53
C v51,v54,v55
ds_write_b128 v194,v[48:51] offset:18944
AR v56,a32
AR v57,a33
AR v58,a34
AR v59,a35
AR v60,a44
AR v61,a45
AR v62,a46
AR v63,a47
C v56,v56,v57
C v57,v58,v59
C v58,v60,v61
C v59,v62,v63
ds_write_b128 v194,v[56:59] offset:19008
AR v32,a56
AR v33,a57
AR v34,a58
AR v35,a59
AR v36,a68
AR v37,a69
AR v38,a70
AR v39,a71
C v32,v32,v33
C v33,v34,v35
C v34,v36,v37
)"
R"(C v35,v38,v39
ds_write_b128 v194,v[32:35] offset:19072
AR v40,a80
AR v41,a81
AR v42,a82
AR v43,a83
AR v44,a92
AR v45,a93
AR v46,a94
AR v47,a95
C v40,v40,v41
C v41,v42,v43
C v42,v44,v45
C v43,v46,v47
ds_write_b128 v194,v[40:43] offset:19136
AR v48,a104
AR v49,a105
AR v50,a106
AR v51,a107
AR v52,a116
AR v53,a117
AR v54,a118
AR v55,a119
C v48,v48,v49
C v49,v50,v51
C v50,v52,v53
C v51,v54,v55
ds_write_b128 v194,v[48:51] offset:19200
AR v56,a128
AR v57,a129
AR v58,a130
AR v59,a131
AR v60,a140
AR v61,a141
AR v62,a142
AR v63,a143
C v56,v56,v57
C v57,v58,v59
C v58,v60,v61
C v59,v62,v63
ds_write_b128 v194,v[56:59] offset:19264
AR v32,a152
AR v33,a153
AR v34,a154
AR v35,a155
AR v36,a164
AR v37,a165
AR v38,a166
AR v39,a167
C v32,v32,v33
C v33,v34,v35
C v34,v36,v37
C v35,v38,v39
ds_write_b128 v194,v[32:35] offset:19328
AR v40,a176
AR v41,a177
AR v42,a178
AR v43,a179
AR v44,a188
AR v45,a189
AR v46,a190
AR v47,a191
C v40,v40,v41
C v41,v42,v43
C v42,v44,v45
C v43,v46,v47
ds_write_b128 v194,v[40:43] offset:19392
AR v48,a200
AR v49,a201
AR v50,a202
AR v51,a203
AR v52,a212
AR v53,a213
AR v54,a214
AR v55,a215
C v48,v48,v49
C v49,v50,v51
C v50,v52,v53
C v51,v54,v55
ds_write_b128 v194,v[48:51] offset:19456
WL 0
D v[64:67],v176,18944
D v[68:71],v177,18944
D v[72:75],v178,18944
D v[76:79],v179,18944
D v[80:83],v180,18944
D v[84:87],v181,18944
D v[88:91],v182,18944
D v[92:95],v183,18944
D v[96:99],v184,18944
WL 8
buffer_store_dwordx4 v[64:67],v185,s[52:55],s72 offen offset:0
WL 7
buffer_store_dwordx4 v[68:71],v186,s[52:55],s72 offen offset:0
WL 6
buffer_store_dwordx4 v[72:75],v187,s[52:55],s72 offen offset:0
WL 5
buffer_store_dwordx4 v[76:79],v188,s[52:55],s72 offen offset:0
WL 4
buffer_store_dwordx4 v[80:83],v189,s[52:55],s72 offen offset:0
WL 3
buffer_store_dwordx4 v[84:87],v190,s[52:55],s72 offen offset:0
WL 2
buffer_store_dwordx4 v[88:91],v191,s[52:55],s72 offen offset:0
WL 1
buffer_store_dwordx4 v[92:95],v192,s[52:55],s72 offen offset:0
WL 0
buffer_store_dwordx4 v[96:99],v193,s[52:55],s72 offen offset:0
s_cmp_lg_u32 %2,255
s_cbranch_scc1 f_fin
v_lshlrev_b32 v28,2,v0
s_lshl_b32 s21,s20,12
v_add_u32 v28,s21,v28
global_load_dwordx2 v[26:27],v28,s[94:95] sc1
WV 0
v_cmp_ne_u32 vcc,s96,v26
v_cmp_ne_u32 s[92:93],s97,v27
s_nop 1
s_or_b64 vcc,vcc,s[92:93]
s_cbranch_vccz f_c4_ok
s_mov_b32 s98,200000
f_c4_spin:
s_sleep 2
global_load_dwordx2 v[26:27],v28,s[94:95] sc1
WV 0
v_cmp_ne_u32 vcc,s96,v26
v_cmp_ne_u32 s[92:93],s97,v27
s_nop 1
s_or_b64 vcc,vcc,s[92:93]
s_cbranch_vccz f_c4_ok
s_sub_u32 s98,s98,1
s_cbranch_scc0 f_c4_spin
f_c4_ok:
s_barrier
s_cmp_lg_u32 s20,0
s_cbranch_scc1 f_fin
v_mov_b32 v28,65536
v_mov_b32 v26,s96
s_mov_b64 exec,1
global_store_dword v28,v26,s[94:95] sc1
s_mov_b64 exec,-1
f_fin:
f_last:
.purgem M
.purgem C
.purgem AR
.purgem AW
.purgem E
.purgem D
.purgem T
.purgem G
.purgem GS
.purgem WL
.purgem WV
)"
    :: "s"(__builtin_amdgcn_kernarg_segment_ptr()), "s"(wid), "s"(bid) : "v0", "v1", "v2", "v3", "v4", "v5", "v6", "v7", "v8", "v9", "v10", "v11", "v12", "v13", "v14", "v15", "v16", "v17", "v18", "v19", "v20", "v21", "v22", "v23", "v24", "v25", "v26", "v27", "v28", "v29", "v30", "v31", "v32", "v33", "v34", "v35", "v36", "v37", "v38", "v39", "v40", "v41", "v42", "v43", "v44", "v45", "v46", "v47", "v48", "v49", "v50", "v51", "v52", "v53", "v54", "v55", "v56", "v57", "v58", "v59", "v60", "v61", "v62", "v63", "v64", "v65", "v66", "v67", "v68", "v69", "v70", "v71", "v72", "v73", "v74", "v75", "v76", "v77", "v78", "v79", "v80", "v81", "v82", "v83", "v84", "v85", "v86", "v87", "v88", "v89", "v90", "v91", "v92", "v93", "v94", "v95", "v96", "v97", "v98", "v99", "v100", "v101", "v102", "v103", "v104", "v105", "v106", "v107", "v108", "v109", "v110", "v111", "v112", "v113", "v114", "v115", "v116", "v117", "v118", "v119", "v120", "v121", "v122", "v123", "v124", "v125", "v126", "v127", "v128", "v129", "v130", "v131", "v132", "v133", "v134", "v135", "v136", "v137", "v138", "v139", "v140", "v141", "v142", "v143", "v144", "v145", "v146", "v147", "v148", "v149", "v150", "v151", "v152", "v153", "v154", "v155", "v156", "v157", "v158", "v159", "v160", "v161", "v162", "v163", "v164", "v165", "v166", "v167", "v168", "v169", "v170", "v171", "v172", "v173", "v174", "v175", "v176", "v177", "v178", "v179", "v180", "v181", "v182", "v183", "v184", "v185", "v186", "v187", "v188", "v189", "v190", "v191", "v192", "v193", "v194", "v195", "v196", "v197", "v198", "v199", "v200", "v201", "v202", "v203", "v204", "v205", "v206", "v207", "v208", "v209", "v210", "v211", "v212", "v213", "v214", "v215", "v216", "v217", "v218", "v219", "v220", "v221", "v222", "v223", "v224", "v225", "v226", "v227", "v228", "v229", "v230", "v231", "v232", "v233", "v234", "v235", "v236", "v237", "v238", "v239", "v240", "v241", "v242", "v243", "v244", "v245", "v246", "v247", "v248", "v249", "v250", "v251", "v252", "v253", "v254", "v255", "a0", "a1", "a2", "a3", "a4", "a5", "a6", "a7", "a8", "a9", "a10", "a11", "a12", "a13", "a14", "a15", "a16", "a17", "a18", "a19", "a20", "a21", "a22", "a23", "a24", "a25", "a26", "a27", "a28", "a29", "a30", "a31", "a32", "a33", "a34", "a35", "a36", "a37", "a38", "a39", "a40", "a41", "a42", "a43", "a44", "a45", "a46", "a47", "a48", "a49", "a50", "a51", "a52", "a53", "a54", "a55", "a56", "a57", "a58", "a59", "a60", "a61", "a62", "a63", "a64", "a65", "a66", "a67", "a68", "a69", "a70", "a71", "a72", "a73", "a74", "a75", "a76", "a77", "a78", "a79", "a80", "a81", "a82", "a83", "a84", "a85", "a86", "a87", "a88", "a89", "a90", "a91", "a92", "a93", "a94", "a95", "a96", "a97", "a98", "a99", "a100", "a101", "a102", "a103", "a104", "a105", "a106", "a107", "a108", "a109", "a110", "a111", "a112", "a113", "a114", "a115", "a116", "a117", "a118", "a119", "a120", "a121", "a122", "a123", "a124", "a125", "a126", "a127", "a128", "a129", "a130", "a131", "a132", "a133", "a134", "a135", "a136", "a137", "a138", "a139", "a140", "a141", "a142", "a143", "a144", "a145", "a146", "a147", "a148", "a149", "a150", "a151", "a152", "a153", "a154", "a155", "a156", "a157", "a158", "a159", "a160", "a161", "a162", "a163", "a164", "a165", "a166", "a167", "a168", "a169", "a170", "a171", "a172", "a173", "a174", "a175", "a176", "a177", "a178", "a179", "a180", "a181", "a182", "a183", "a184", "a185", "a186", "a187", "a188", "a189", "a190", "a191", "a192", "a193", "a194", "a195", "a196", "a197", "a198", "a199", "a200", "a201", "a202", "a203", "a204", "a205", "a206", "a207", "a208", "a209", "a210", "a211", "a212", "a213", "a214", "a215", "a216", "a217", "a218", "a219", "a220", "a221", "a222", "a223", "a224", "a225", "a226", "a227", "a228", "a229", "a230", "a231", "a232", "a233", "a234", "a235", "a236", "a237", "a238", "a239", "a240", "a241", "a242", "a243", "a244", "a245", "a246", "a247", "a248", "a249", "a250", "a251", "a252", "a253", "a254", "a255", "s8", "s9", "s10", "s11", "s12", "s13", "s14", "s15", "s16", "s17", "s18", "s19", "s20", "s21", "s22", "s23", "s24", "s25", "s26", "s27", "s28", "s29", "s30", "s31", "s34", "s35", "s36", "s37", "s38", "s39", "s40", "s41", "s42", "s43", "s44", "s45", "s46", "s47", "s48", "s49", "s50", "s51", "s52", "s53", "s54", "s55", "s56", "s57", "s58", "s59", "s60", "s61", "s62", "s63", "s64", "s65", "s66", "s67", "s68", "s69", "s70", "s71", "s72", "s73", "s74", "s75", "s76", "s77", "s78", "s79", "s80", "s81", "s82", "s83", "s84", "s85", "s86", "s87", "s88", "s89", "s90", "s91", "s92", "s93", "s94", "s95", "s96", "s97", "s98", "s99", "vcc", "memory", "scc");
}

extern "C" void kernel_launch(void* const* d_in, const int* in_sizes, int n_in, void* d_out, int out_size, void* d_ws, size_t ws_size, hipStream_t stream) {
  const u16* const* in = (const u16* const*)d_in;
  (void)hipFuncSetAttribute((const void*)fused_k, hipFuncAttributeMaxDynamicSharedMemorySize, 163840);
  fused_k<<<256, 256, 163840, stream>>>(in[0], in[1], in[2], in[3], in[4], in[5], in[6], in[7], in[8], in[9], (u16*)d_out, (char*)d_ws);
}
```
